```python
import torch

def _softmax(x, axis=-1):
    return torch.softmax(x, dim=axis)
def _gelu(x):
    return torch.nn.functional.gelu(x, approximate="tanh")
def _silu(x):
    return torch.nn.functional.silu(x)
def _sigmoid(x):
    return torch.sigmoid(x)
def _layernorm(x, axis=-1, eps=1e-5):
    m = x.mean(dim=axis, keepdim=True)
    v = x.var(dim=axis, keepdim=True, unbiased=False)
    return (x - m) / torch.sqrt(v + eps)
def _scatter_add(tgt, idx, src, axis=0):
    return tgt.clone().index_add_(axis, idx, src)
def _segment_sum(src, seg, n):
    out = torch.zeros((n,) + src.shape[1:], dtype=src.dtype)
    return out.index_add_(0, seg, src)
def _pad(x, pw):
    flat = tuple(v for pair in reversed(pw) for v in pair)
    return torch.nn.functional.pad(x, flat)

kernel_name = 'gated_mix__grouped_kv_mqa__mixer_token_mlp__prompt'

def setup_inputs():

    _w = (lambda a: (lambda t: t.to(torch.bfloat16) if t.is_floating_point() else t)(torch.as_tensor(a)))
    def _A_setup():
        B, S, D, H = 1, 2048, 1024, 32
        Dh = D // H
        return {
            "x": _w((lambda *s: torch.randn(*s, dtype=torch.bfloat16) * 0.05)(B, S, D)),
            "wq": _w((lambda *s: torch.randn(*s, dtype=torch.bfloat16) * 0.8)(D, H * Dh)),
            "wk": _w((lambda *s: torch.randn(*s, dtype=torch.bfloat16) * 0.8)(D, Dh)),
            "wv": _w((lambda *s: torch.randn(*s, dtype=torch.bfloat16) * 0.05)(D, Dh)),
            "wo": _w((lambda *s: torch.randn(*s, dtype=torch.bfloat16) * 0.05)(H * Dh, D)),
        }
    _A_in = _A_setup()

    def _B_setup():
        B, S, D, F = 1, 2048, 1024, 3584
        return {
            "x": _w((lambda *s: torch.randn(*s, dtype=torch.bfloat16) * 0.05)(B, S, D)),
            "w1": _w((lambda *s: torch.randn(*s, dtype=torch.bfloat16) * 0.4)(S, F) * (S ** -0.5)),
            "b1": _w((lambda *s: torch.randn(*s, dtype=torch.bfloat16) * 3.2)(F) * 0.02),
            "w2": _w((lambda *s: torch.randn(*s, dtype=torch.bfloat16) * 0.1)(F, S) * (F ** -0.5)),
            "b2": _w((lambda *s: torch.randn(*s, dtype=torch.bfloat16) * 0.4)(S) * 0.02),
        }
    _B_in = _B_setup()

    return {
        "A_x": _A_in["x"],
        "A_wq": _A_in["wq"],
        "A_wk": _A_in["wk"],
        "A_wv": _A_in["wv"],
        "A_wo": _A_in["wo"],
        "B_x": _B_in["x"],
        "B_w1": _B_in["w1"],
        "B_b1": _B_in["b1"],
        "B_w2": _B_in["w2"],
        "B_b2": _B_in["b2"],
        "gate_w": _w((lambda *s: torch.randn(*s, dtype=torch.bfloat16) * 0.05)(1024) * 16.0),
    }

def reference(A_x, A_wq, A_wk, A_wv, A_wo, B_x, B_w1, B_b1, B_w2, B_b2, gate_w):
    def _A_ref(x, wq, wk, wv, wo):
        B, S, D = x.shape
        H = 32
        Dh = D // H
        q = (lambda x, *d: x.permute(*d))((x @ wq).reshape(B, S, H, Dh), 0, 2, 1, 3)
        k = x @ wk
        v = x @ wv
        scores = torch.einsum("bhqd,bkd->bhqk", q, k) * (Dh ** -0.5)
        p = _softmax(scores, axis=-1)
        o = torch.einsum("bhqk,bkd->bhqd", p, v)
        return (lambda x, *d: x.permute(*d))(o, 0, 2, 1, 3).reshape(B, S, H * Dh) @ wo

    def _B_ref(x, w1, b1, w2, b2):
        B, S, D = x.shape
        h = _layernorm(x, axis=-1)
        u = torch.einsum("bsd,sf->bdf", h, w1) + b1
        u = _gelu(u)
        y = torch.einsum("bdf,fs->bds", u, w2) + b2
        return (lambda x, *d: x.permute(*d))(y, 0, 2, 1)

    x = A_x
    g = _sigmoid((x * gate_w).sum(-1, keepdims=True))
    a = _A_ref(x, A_wq, A_wk, A_wv, A_wo)
    b = _B_ref(x, B_w1, B_b1, B_w2, B_b2)
    return g * a + (1 - g) * b
```

```cpp
#include <hip/hip_runtime.h>
#include <hip/hip_bf16.h>
#include <stdint.h>

typedef short bf16x8 __attribute__((ext_vector_type(8)));
typedef float f32x16 __attribute__((ext_vector_type(16)));
typedef float f32x8 __attribute__((ext_vector_type(8)));
typedef float f32x4 __attribute__((ext_vector_type(4)));
typedef unsigned u32x4 __attribute__((ext_vector_type(4)));
typedef unsigned u32x2 __attribute__((ext_vector_type(2)));
typedef __attribute__((address_space(3))) const char* lds_cptr;
typedef __attribute__((address_space(1))) unsigned gu32;
typedef __amdgpu_buffer_rsrc_t rsrc_t;

constexpr int SEQ = 2048, DM = 1024, DF = 3584;
constexpr unsigned CT_G = 0, CT_O = 64, CT_Y = 128;
constexpr unsigned WS_KV = 1u << 20;
constexpr unsigned WS_GATE = WS_KV + (256u << 10);
constexpr unsigned WS_KN = WS_GATE + (8u << 10);
constexpr unsigned WS_HT = 2u << 20;
constexpr unsigned WS_OP = 6u << 20;
constexpr unsigned WS_G = 10u << 20;
constexpr unsigned WS_Y = 18u << 20;
constexpr unsigned WS_END = 26u << 20;
constexpr int SC1 = 16;
constexpr int LDS_X = 73728, LDS_BYTES = LDS_X + 2048 + 256;
constexpr float QSCALE = 0.17677669529663687f * 1.4426950408889634f;
constexpr float SCORE_BOUND = 60.f;

#define LSTAMP(i)
#define STAMP(i)
#define POLL_SLEEP 8
#define PRIO_A 0
#define PRIO_B 0
#define PRIO_EPI 2
#define MFMA32(a, b, c) __builtin_amdgcn_mfma_f32_32x32x16_bf16(a, b, c, 0, 0, 0)
#define BC8(x) __builtin_bit_cast(bf16x8, x)
#define BARRIER() asm volatile("s_barrier" ::: "memory")
#define WAITV_BAR(N) asm volatile("s_waitcnt vmcnt(%0)\n\ts_barrier" ::"i"(N) : "memory")
#define LGKM_BAR() asm volatile("s_waitcnt lgkmcnt(0)\n\ts_barrier" ::: "memory")
#define CLOB8(a, b, c, d, e, f, g, h) "v" #a, "v" #b, "v" #c, "v" #d, "v" #e, "v" #f, "v" #g, "v" #h
#define CLOB16(a, b, c, d, e, f, g, h, i, j, k, l, m, n, o, p) CLOB8(a, b, c, d, e, f, g, h), CLOB8(i, j, k, l, m, n, o, p)

__device__ __forceinline__ unsigned cvtpk(float lo, float hi) {
    typedef __bf16 bf2 __attribute__((ext_vector_type(2)));
    typedef float f2 __attribute__((ext_vector_type(2)));
    return __builtin_bit_cast(unsigned, __builtin_convertvector((f2){lo, hi}, bf2));
}
__device__ __forceinline__ float bflo(unsigned u) { return __uint_as_float(u << 16); }
__device__ __forceinline__ float bfhi(unsigned u) { return __uint_as_float(u & 0xffff0000u); }
__device__ __forceinline__ u32x4 pack8(const f32x16& a, int c, float m) {
    return (u32x4){cvtpk(a[8 * c] * m, a[8 * c + 1] * m), cvtpk(a[8 * c + 2] * m, a[8 * c + 3] * m),
                   cvtpk(a[8 * c + 4] * m, a[8 * c + 5] * m), cvtpk(a[8 * c + 6] * m, a[8 * c + 7] * m)};
}
__device__ __forceinline__ float sumsq8(u32x4 v) {
    float n = 0.f;
#pragma unroll
    for (int i = 0; i < 4; ++i) n = fmaf(bflo(v[i]), bflo(v[i]), fmaf(bfhi(v[i]), bfhi(v[i]), n));
    return n;
}
__device__ __forceinline__ void dma_s(unsigned voff, const void* sbase, unsigned lds) {
    asm volatile("s_mov_b32 m0, %2\n\ts_nop 0\n\tglobal_load_lds_dwordx4 %0, %1" ::"v"(voff), "s"(sbase), "s"(lds) : "memory");
}
__device__ __forceinline__ void dma_c(unsigned voff, const void* sbase, unsigned lds) {
    asm volatile("s_mov_b32 m0, %2\n\ts_nop 0\n\tglobal_load_lds_dwordx4 %0, %1 sc1" ::"v"(voff), "s"(sbase), "s"(lds) : "memory");
}
__device__ __forceinline__ void dma_s2(unsigned voff, const void* sbase, unsigned lds) {
    asm volatile("s_mov_b32 m0, %2\n\ts_nop 0\n\tglobal_load_lds_dwordx4 %0, %1\n\tglobal_load_lds_dwordx4 %0, %1 offset:1024"
                 ::"v"(voff), "s"(sbase), "s"(lds) : "memory");
}
__device__ __forceinline__ void dma_c2(unsigned voff, const void* sbase, unsigned lds) {
    asm volatile("s_mov_b32 m0, %2\n\ts_nop 0\n\tglobal_load_lds_dwordx4 %0, %1 sc1\n\tglobal_load_lds_dwordx4 %0, %1 offset:1024 sc1"
                 ::"v"(voff), "s"(sbase), "s"(lds) : "memory");
}
__device__ __forceinline__ void st16(rsrc_t rw, unsigned off, u32x4 v) { __builtin_amdgcn_raw_buffer_store_b128(v, rw, off, 0, SC1); }
typedef short s16x4 __attribute__((ext_vector_type(4)));
__device__ __forceinline__ s16x4 ldstr(lds_cptr p) {
    return __builtin_bit_cast(s16x4, __builtin_amdgcn_ds_read_tr16_b64_v4i16((__attribute__((address_space(3))) s16x4*)p));
}
__device__ __forceinline__ bf16x8 lds128(lds_cptr p) { return *(const __attribute__((address_space(3))) bf16x8*)p; }
__device__ __forceinline__ int fresh_lane() {
    int l;
    asm volatile("v_mbcnt_lo_u32_b32 %0, -1, 0\n\tv_mbcnt_hi_u32_b32 %0, -1, %0" : "=v"(l));
    return l;
}
__device__ __forceinline__ float wave_sum(float v) {
#pragma unroll
    for (int o = 32; o > 0; o >>= 1) v += __shfl_xor(v, o);
    return v;
}
__device__ __forceinline__ float wave_max(float v) {
#pragma unroll
    for (int o = 32; o > 0; o >>= 1) v = fmaxf(v, __shfl_xor(v, o));
    return v;
}
__device__ __forceinline__ void arrive(char* ws, unsigned line, int reps, int lane) {
    if (lane < reps) __hip_atomic_fetch_add((gu32*)ws + (line + lane) * 32, 1u, __ATOMIC_RELAXED, __HIP_MEMORY_SCOPE_AGENT);
}
__device__ __forceinline__ void wait_count(char* ws, unsigned line, unsigned n) {
    for (unsigned spins = 0; spins < (1u << 20); ++spins) {
        if (__builtin_amdgcn_readfirstlane(__hip_atomic_load((gu32*)ws + line * 32, __ATOMIC_RELAXED, __HIP_MEMORY_SCOPE_AGENT)) >= n) break;
        __builtin_amdgcn_s_sleep(POLL_SLEEP);
    }
    asm volatile("" ::: "memory");
}
#define ASM_ATTN \
    "s_mov_b32 %[cnt], 4\n" \
    "1:\n" \
    "s_waitcnt lgkmcnt(2)\n" \
    "v_mfma_f32_32x32x16_bf16 v[32:47], v[108:111], v[80:83], 0\n" \
    "v_exp_f32 v48, v48\n" \
    "v_exp_f32 v49, v49\n" \
    "v_exp_f32 v50, v50\n" \
    "v_exp_f32 v51, v51\n" \
    "v_mfma_f32_32x32x16_bf16 v[32:47], v[112:115], v[84:87], v[32:47]\n" \
    "v_cvt_pk_bf16_f32 v72, v48, v49\n" \
    "v_cvt_pk_bf16_f32 v73, v50, v51\n" \
    "v_exp_f32 v52, v52\n" \
    "v_exp_f32 v53, v53\n" \
    "v_exp_f32 v54, v54\n" \
    "s_waitcnt lgkmcnt(1)\n" \
    "v_mfma_f32_32x32x16_bf16 v[0:15], v[116:119], v[64:67], v[0:15]\n" \
    "v_exp_f32 v55, v55\n" \
    "v_cvt_pk_bf16_f32 v74, v52, v53\n" \
    "v_exp_f32 v56, v56\n" \
    "v_exp_f32 v57, v57\n" \
    "v_cvt_pk_bf16_f32 v75, v54, v55\n" \
    "v_mfma_f32_16x16x32_bf16 v[100:103], v[96:99], v[64:67], v[100:103]\n" \
    "v_exp_f32 v58, v58\n" \
    "v_exp_f32 v59, v59\n" \
    "s_waitcnt lgkmcnt(0)\n" \
    "v_mfma_f32_32x32x16_bf16 v[0:15], v[120:123], v[68:71], v[0:15]\n" \
    "v_cvt_pk_bf16_f32 v76, v56, v57\n" \
    "v_exp_f32 v60, v60\n" \
    "v_exp_f32 v61, v61\n" \
    "v_cvt_pk_bf16_f32 v77, v58, v59\n" \
    "v_exp_f32 v62, v62\n" \
    "v_mfma_f32_16x16x32_bf16 v[100:103], v[96:99], v[68:71], v[100:103]\n" \
    "v_exp_f32 v63, v63\n" \
    "v_cvt_pk_bf16_f32 v78, v60, v61\n" \
    "v_cvt_pk_bf16_f32 v79, v62, v63\n" \
    "v_mfma_f32_32x32x16_bf16 v[48:63], v[108:111], v[88:91], 0\n" \
    "v_exp_f32 v32, v32\n" \
    "ds_read_b128 v[108:111], v124 offset:36864\n" \
    "v_exp_f32 v33, v33\n" \
    "v_exp_f32 v34, v34\n" \
    "v_exp_f32 v35, v35\n" \
    "v_mfma_f32_32x32x16_bf16 v[48:63], v[112:115], v[92:95], v[48:63]\n" \
    "v_cvt_pk_bf16_f32 v64, v32, v33\n" \
    "ds_read_b128 v[112:115], v124 offset:37888\n" \
    "v_cvt_pk_bf16_f32 v65, v34, v35\n" \
    "v_exp_f32 v36, v36\n" \
    "v_exp_f32 v37, v37\n" \
    "v_exp_f32 v38, v38\n" \
    "v_mfma_f32_32x32x16_bf16 v[16:31], v[116:119], v[72:75], v[16:31]\n" \
    "v_exp_f32 v39, v39\n" \
    "ds_read_b128 v[116:119], v124 offset:38912\n" \
    "v_cvt_pk_bf16_f32 v66, v36, v37\n" \
    "v_exp_f32 v40, v40\n" \
    "v_exp_f32 v41, v41\n" \
    "v_cvt_pk_bf16_f32 v67, v38, v39\n" \
    "v_mfma_f32_16x16x32_bf16 v[104:107], v[96:99], v[72:75], v[104:107]\n" \
    "v_exp_f32 v42, v42\n" \
    "v_exp_f32 v43, v43\n" \
    "v_mfma_f32_32x32x16_bf16 v[16:31], v[120:123], v[76:79], v[16:31]\n" \
    "v_cvt_pk_bf16_f32 v68, v40, v41\n" \
    "ds_read_b128 v[120:123], v124 offset:39936\n" \
    "v_exp_f32 v44, v44\n" \
    "v_exp_f32 v45, v45\n" \
    "v_cvt_pk_bf16_f32 v69, v42, v43\n" \
    "v_exp_f32 v46, v46\n" \
    "v_mfma_f32_16x16x32_bf16 v[104:107], v[96:99], v[76:79], v[104:107]\n" \
    "v_exp_f32 v47, v47\n" \
    "v_cvt_pk_bf16_f32 v70, v44, v45\n" \
    "v_cvt_pk_bf16_f32 v71, v46, v47\n" \
    "s_waitcnt lgkmcnt(2)\n" \
    "v_mfma_f32_32x32x16_bf16 v[32:47], v[108:111], v[80:83], 0\n" \
    "v_exp_f32 v48, v48\n" \
    "v_exp_f32 v49, v49\n" \
    "v_exp_f32 v50, v50\n" \
    "v_exp_f32 v51, v51\n" \
    "v_mfma_f32_32x32x16_bf16 v[32:47], v[112:115], v[84:87], v[32:47]\n" \
    "v_cvt_pk_bf16_f32 v72, v48, v49\n" \
    "v_cvt_pk_bf16_f32 v73, v50, v51\n" \
    "v_exp_f32 v52, v52\n" \
    "v_exp_f32 v53, v53\n" \
    "v_exp_f32 v54, v54\n" \
    "s_waitcnt lgkmcnt(1)\n" \
    "v_mfma_f32_32x32x16_bf16 v[0:15], v[116:119], v[64:67], v[0:15]\n" \
    "v_exp_f32 v55, v55\n" \
    "v_cvt_pk_bf16_f32 v74, v52, v53\n" \
    "v_exp_f32 v56, v56\n" \
    "v_exp_f32 v57, v57\n" \
    "v_cvt_pk_bf16_f32 v75, v54, v55\n" \
    "v_mfma_f32_16x16x32_bf16 v[100:103], v[96:99], v[64:67], v[100:103]\n" \
    "v_exp_f32 v58, v58\n" \
    "v_exp_f32 v59, v59\n" \
    "s_waitcnt lgkmcnt(0)\n" \
    "v_mfma_f32_32x32x16_bf16 v[0:15], v[120:123], v[68:71], v[0:15]\n" \
    "v_cvt_pk_bf16_f32 v76, v56, v57\n" \
    "v_exp_f32 v60, v60\n" \
    "v_exp_f32 v61, v61\n" \
    "v_cvt_pk_bf16_f32 v77, v58, v59\n" \
    "v_exp_f32 v62, v62\n" \
    "v_mfma_f32_16x16x32_bf16 v[100:103], v[96:99], v[68:71], v[100:103]\n" \
    "v_exp_f32 v63, v63\n" \
    "v_cvt_pk_bf16_f32 v78, v60, v61\n" \
    "v_cvt_pk_bf16_f32 v79, v62, v63\n" \
    "v_mfma_f32_32x32x16_bf16 v[48:63], v[108:111], v[88:91], 0\n" \
    "v_exp_f32 v32, v32\n" \
    "ds_read_b128 v[108:111], v124 offset:40960\n" \
    "v_exp_f32 v33, v33\n" \
    "v_exp_f32 v34, v34\n" \
    "v_exp_f32 v35, v35\n" \
    "v_mfma_f32_32x32x16_bf16 v[48:63], v[112:115], v[92:95], v[48:63]\n" \
    "v_cvt_pk_bf16_f32 v64, v32, v33\n" \
    "ds_read_b128 v[112:115], v124 offset:41984\n" \
    "v_cvt_pk_bf16_f32 v65, v34, v35\n" \
    "v_exp_f32 v36, v36\n" \
    "v_exp_f32 v37, v37\n" \
    "v_exp_f32 v38, v38\n" \
    "v_mfma_f32_32x32x16_bf16 v[16:31], v[116:119], v[72:75], v[16:31]\n" \
    "v_exp_f32 v39, v39\n" \
    "ds_read_b128 v[116:119], v124 offset:43008\n" \
    "v_cvt_pk_bf16_f32 v66, v36, v37\n" \
    "v_exp_f32 v40, v40\n" \
    "v_exp_f32 v41, v41\n" \
    "v_cvt_pk_bf16_f32 v67, v38, v39\n" \
    "v_mfma_f32_16x16x32_bf16 v[104:107], v[96:99], v[72:75], v[104:107]\n" \
    "v_exp_f32 v42, v42\n" \
    "v_exp_f32 v43, v43\n" \
    "v_mfma_f32_32x32x16_bf16 v[16:31], v[120:123], v[76:79], v[16:31]\n" \
    "v_cvt_pk_bf16_f32 v68, v40, v41\n" \
    "ds_read_b128 v[120:123], v124 offset:44032\n" \
    "v_exp_f32 v44, v44\n" \
    "v_exp_f32 v45, v45\n" \
    "v_cvt_pk_bf16_f32 v69, v42, v43\n" \
    "v_exp_f32 v46, v46\n" \
    "v_mfma_f32_16x16x32_bf16 v[104:107], v[96:99], v[76:79], v[104:107]\n" \
    "v_exp_f32 v47, v47\n" \
    "v_cvt_pk_bf16_f32 v70, v44, v45\n" \
    "v_cvt_pk_bf16_f32 v71, v46, v47\n" \
    "s_waitcnt lgkmcnt(2)\n" \
    "v_mfma_f32_32x32x16_bf16 v[32:47], v[108:111], v[80:83], 0\n" \
    "v_exp_f32 v48, v48\n" \
    "v_exp_f32 v49, v49\n" \
    "v_exp_f32 v50, v50\n" \
    "v_exp_f32 v51, v51\n" \
    "v_mfma_f32_32x32x16_bf16 v[32:47], v[112:115], v[84:87], v[32:47]\n" \
    "v_cvt_pk_bf16_f32 v72, v48, v49\n" \
    "v_cvt_pk_bf16_f32 v73, v50, v51\n" \
    "v_exp_f32 v52, v52\n" \
    "v_exp_f32 v53, v53\n" \
    "v_exp_f32 v54, v54\n" \
    "s_waitcnt lgkmcnt(1)\n" \
    "v_mfma_f32_32x32x16_bf16 v[0:15], v[116:119], v[64:67], v[0:15]\n" \
    "v_exp_f32 v55, v55\n" \
    "v_cvt_pk_bf16_f32 v74, v52, v53\n" \
    "v_exp_f32 v56, v56\n" \
    "v_exp_f32 v57, v57\n" \
    "v_cvt_pk_bf16_f32 v75, v54, v55\n" \
    "v_mfma_f32_16x16x32_bf16 v[100:103], v[96:99], v[64:67], v[100:103]\n" \
    "v_exp_f32 v58, v58\n" \
    "v_exp_f32 v59, v59\n" \
    "s_waitcnt lgkmcnt(0)\n" \
    "v_mfma_f32_32x32x16_bf16 v[0:15], v[120:123], v[68:71], v[0:15]\n" \
    "v_cvt_pk_bf16_f32 v76, v56, v57\n" \
    "v_exp_f32 v60, v60\n" \
    "v_exp_f32 v61, v61\n" \
    "v_cvt_pk_bf16_f32 v77, v58, v59\n" \
    "v_exp_f32 v62, v62\n" \
    "v_mfma_f32_16x16x32_bf16 v[100:103], v[96:99], v[68:71], v[100:103]\n" \
    "v_exp_f32 v63, v63\n" \
    "v_cvt_pk_bf16_f32 v78, v60, v61\n" \
    "v_cvt_pk_bf16_f32 v79, v62, v63\n" \
    "v_mfma_f32_32x32x16_bf16 v[48:63], v[108:111], v[88:91], 0\n" \
    "v_exp_f32 v32, v32\n" \
    "ds_read_b128 v[108:111], v124 offset:45056\n" \
    "v_exp_f32 v33, v33\n" \
    "v_exp_f32 v34, v34\n" \
    "v_exp_f32 v35, v35\n" \
    "v_mfma_f32_32x32x16_bf16 v[48:63], v[112:115], v[92:95], v[48:63]\n" \
    "v_cvt_pk_bf16_f32 v64, v32, v33\n" \
    "ds_read_b128 v[112:115], v124 offset:46080\n" \
    "v_cvt_pk_bf16_f32 v65, v34, v35\n" \
    "v_exp_f32 v36, v36\n" \
    "v_exp_f32 v37, v37\n" \
    "v_exp_f32 v38, v38\n" \
    "v_mfma_f32_32x32x16_bf16 v[16:31], v[116:119], v[72:75], v[16:31]\n" \
    "v_exp_f32 v39, v39\n" \
    "ds_read_b128 v[116:119], v124 offset:47104\n" \
    "v_cvt_pk_bf16_f32 v66, v36, v37\n" \
    "v_exp_f32 v40, v40\n" \
    "v_exp_f32 v41, v41\n" \
    "v_cvt_pk_bf16_f32 v67, v38, v39\n" \
    "v_mfma_f32_16x16x32_bf16 v[104:107], v[96:99], v[72:75], v[104:107]\n" \
    "v_exp_f32 v42, v42\n" \
    "v_exp_f32 v43, v43\n" \
    "v_mfma_f32_32x32x16_bf16 v[16:31], v[120:123], v[76:79], v[16:31]\n" \
    "v_cvt_pk_bf16_f32 v68, v40, v41\n" \
    "ds_read_b128 v[120:123], v124 offset:48128\n" \
    "v_exp_f32 v44, v44\n" \
    "v_exp_f32 v45, v45\n" \
    "v_cvt_pk_bf16_f32 v69, v42, v43\n" \
    "v_exp_f32 v46, v46\n" \
    "v_mfma_f32_16x16x32_bf16 v[104:107], v[96:99], v[76:79], v[104:107]\n" \
    "v_exp_f32 v47, v47\n" \
    "v_cvt_pk_bf16_f32 v70, v44, v45\n" \
    "v_cvt_pk_bf16_f32 v71, v46, v47\n" \
    "s_waitcnt lgkmcnt(2)\n" \
    "v_mfma_f32_32x32x16_bf16 v[32:47], v[108:111], v[80:83], 0\n" \
    "v_exp_f32 v48, v48\n" \
    "v_exp_f32 v49, v49\n" \
    "v_exp_f32 v50, v50\n" \
    "v_exp_f32 v51, v51\n" \
    "v_mfma_f32_32x32x16_bf16 v[32:47], v[112:115], v[84:87], v[32:47]\n" \
    "v_cvt_pk_bf16_f32 v72, v48, v49\n" \
    "v_cvt_pk_bf16_f32 v73, v50, v51\n" \
    "v_exp_f32 v52, v52\n" \
    "v_exp_f32 v53, v53\n" \
    "v_exp_f32 v54, v54\n" \
    "s_waitcnt lgkmcnt(1)\n" \
    "v_mfma_f32_32x32x16_bf16 v[0:15], v[116:119], v[64:67], v[0:15]\n" \
    "v_exp_f32 v55, v55\n" \
    "v_cvt_pk_bf16_f32 v74, v52, v53\n" \
    "v_exp_f32 v56, v56\n" \
    "v_exp_f32 v57, v57\n" \
    "v_cvt_pk_bf16_f32 v75, v54, v55\n" \
    "v_mfma_f32_16x16x32_bf16 v[100:103], v[96:99], v[64:67], v[100:103]\n" \
    "v_exp_f32 v58, v58\n" \
    "v_exp_f32 v59, v59\n" \
    "s_waitcnt lgkmcnt(0)\n" \
    "v_mfma_f32_32x32x16_bf16 v[0:15], v[120:123], v[68:71], v[0:15]\n" \
    "v_cvt_pk_bf16_f32 v76, v56, v57\n" \
    "v_exp_f32 v60, v60\n" \
    "v_exp_f32 v61, v61\n" \
    "v_cvt_pk_bf16_f32 v77, v58, v59\n" \
    "v_exp_f32 v62, v62\n" \
    "v_mfma_f32_16x16x32_bf16 v[100:103], v[96:99], v[68:71], v[100:103]\n" \
    "v_exp_f32 v63, v63\n" \
    "v_cvt_pk_bf16_f32 v78, v60, v61\n" \
    "v_cvt_pk_bf16_f32 v79, v62, v63\n" \
    "v_mfma_f32_32x32x16_bf16 v[48:63], v[108:111], v[88:91], 0\n" \
    "v_exp_f32 v32, v32\n" \
    "ds_read_b128 v[108:111], v124 offset:49152\n" \
    "v_exp_f32 v33, v33\n" \
    "v_exp_f32 v34, v34\n" \
    "v_exp_f32 v35, v35\n" \
    "v_mfma_f32_32x32x16_bf16 v[48:63], v[112:115], v[92:95], v[48:63]\n" \
    "v_cvt_pk_bf16_f32 v64, v32, v33\n" \
    "ds_read_b128 v[112:115], v124 offset:50176\n" \
    "v_cvt_pk_bf16_f32 v65, v34, v35\n" \
    "v_exp_f32 v36, v36\n" \
    "v_exp_f32 v37, v37\n" \
    "v_exp_f32 v38, v38\n" \
    "v_mfma_f32_32x32x16_bf16 v[16:31], v[116:119], v[72:75], v[16:31]\n" \
    "v_exp_f32 v39, v39\n" \
    "ds_read_b128 v[116:119], v124 offset:51200\n" \
    "v_cvt_pk_bf16_f32 v66, v36, v37\n" \
    "v_exp_f32 v40, v40\n" \
    "v_exp_f32 v41, v41\n" \
    "v_cvt_pk_bf16_f32 v67, v38, v39\n" \
    "v_mfma_f32_16x16x32_bf16 v[104:107], v[96:99], v[72:75], v[104:107]\n" \
    "v_exp_f32 v42, v42\n" \
    "v_exp_f32 v43, v43\n" \
    "v_mfma_f32_32x32x16_bf16 v[16:31], v[120:123], v[76:79], v[16:31]\n" \
    "v_cvt_pk_bf16_f32 v68, v40, v41\n" \
    "ds_read_b128 v[120:123], v124 offset:52224\n" \
    "v_exp_f32 v44, v44\n" \
    "v_exp_f32 v45, v45\n" \
    "v_cvt_pk_bf16_f32 v69, v42, v43\n" \
    "v_exp_f32 v46, v46\n" \
    "v_mfma_f32_16x16x32_bf16 v[104:107], v[96:99], v[76:79], v[104:107]\n" \
    "v_exp_f32 v47, v47\n" \
    "v_cvt_pk_bf16_f32 v70, v44, v45\n" \
    "v_cvt_pk_bf16_f32 v71, v46, v47\n" \
    "s_waitcnt lgkmcnt(2)\n" \
    "v_mfma_f32_32x32x16_bf16 v[32:47], v[108:111], v[80:83], 0\n" \
    "v_exp_f32 v48, v48\n" \
    "v_exp_f32 v49, v49\n" \
    "v_exp_f32 v50, v50\n" \
    "v_exp_f32 v51, v51\n" \
    "v_mfma_f32_32x32x16_bf16 v[32:47], v[112:115], v[84:87], v[32:47]\n" \
    "v_cvt_pk_bf16_f32 v72, v48, v49\n" \
    "v_cvt_pk_bf16_f32 v73, v50, v51\n" \
    "v_exp_f32 v52, v52\n" \
    "v_exp_f32 v53, v53\n" \
    "v_exp_f32 v54, v54\n" \
    "s_waitcnt lgkmcnt(1)\n" \
    "v_mfma_f32_32x32x16_bf16 v[0:15], v[116:119], v[64:67], v[0:15]\n" \
    "v_exp_f32 v55, v55\n" \
    "v_cvt_pk_bf16_f32 v74, v52, v53\n" \
    "v_exp_f32 v56, v56\n" \
    "v_exp_f32 v57, v57\n" \
    "v_cvt_pk_bf16_f32 v75, v54, v55\n" \
    "v_mfma_f32_16x16x32_bf16 v[100:103], v[96:99], v[64:67], v[100:103]\n" \
    "v_exp_f32 v58, v58\n" \
    "v_exp_f32 v59, v59\n" \
    "s_waitcnt lgkmcnt(0)\n" \
    "v_mfma_f32_32x32x16_bf16 v[0:15], v[120:123], v[68:71], v[0:15]\n" \
    "v_cvt_pk_bf16_f32 v76, v56, v57\n" \
    "v_exp_f32 v60, v60\n" \
    "v_exp_f32 v61, v61\n" \
    "v_cvt_pk_bf16_f32 v77, v58, v59\n" \
    "v_exp_f32 v62, v62\n" \
    "v_mfma_f32_16x16x32_bf16 v[100:103], v[96:99], v[68:71], v[100:103]\n" \
    "v_exp_f32 v63, v63\n" \
    "v_cvt_pk_bf16_f32 v78, v60, v61\n" \
    "v_cvt_pk_bf16_f32 v79, v62, v63\n" \
    "v_mfma_f32_32x32x16_bf16 v[48:63], v[108:111], v[88:91], 0\n" \
    "v_exp_f32 v32, v32\n" \
    "ds_read_b128 v[108:111], v124 offset:53248\n" \
    "v_exp_f32 v33, v33\n" \
    "v_exp_f32 v34, v34\n" \
    "v_exp_f32 v35, v35\n" \
    "v_mfma_f32_32x32x16_bf16 v[48:63], v[112:115], v[92:95], v[48:63]\n" \
    "v_cvt_pk_bf16_f32 v64, v32, v33\n" \
    "ds_read_b128 v[112:115], v124 offset:54272\n" \
    "v_cvt_pk_bf16_f32 v65, v34, v35\n" \
    "v_exp_f32 v36, v36\n" \
    "v_exp_f32 v37, v37\n" \
    "v_exp_f32 v38, v38\n" \
    "v_mfma_f32_32x32x16_bf16 v[16:31], v[116:119], v[72:75], v[16:31]\n" \
    "v_exp_f32 v39, v39\n" \
    "ds_read_b128 v[116:119], v124 offset:55296\n" \
    "v_cvt_pk_bf16_f32 v66, v36, v37\n" \
    "v_exp_f32 v40, v40\n" \
    "v_exp_f32 v41, v41\n" \
    "v_cvt_pk_bf16_f32 v67, v38, v39\n" \
    "v_mfma_f32_16x16x32_bf16 v[104:107], v[96:99], v[72:75], v[104:107]\n" \
    "v_exp_f32 v42, v42\n" \
    "v_exp_f32 v43, v43\n" \
    "v_mfma_f32_32x32x16_bf16 v[16:31], v[120:123], v[76:79], v[16:31]\n" \
    "v_cvt_pk_bf16_f32 v68, v40, v41\n" \
    "ds_read_b128 v[120:123], v124 offset:56320\n" \
    "v_exp_f32 v44, v44\n" \
    "v_exp_f32 v45, v45\n" \
    "v_cvt_pk_bf16_f32 v69, v42, v43\n" \
    "v_exp_f32 v46, v46\n" \
    "v_mfma_f32_16x16x32_bf16 v[104:107], v[96:99], v[76:79], v[104:107]\n" \
    "v_exp_f32 v47, v47\n" \
    "v_cvt_pk_bf16_f32 v70, v44, v45\n" \
    "v_cvt_pk_bf16_f32 v71, v46, v47\n" \
    "s_waitcnt lgkmcnt(2)\n" \
    "v_mfma_f32_32x32x16_bf16 v[32:47], v[108:111], v[80:83], 0\n" \
    "v_exp_f32 v48, v48\n" \
    "v_exp_f32 v49, v49\n" \
    "v_exp_f32 v50, v50\n" \
    "v_exp_f32 v51, v51\n" \
    "v_mfma_f32_32x32x16_bf16 v[32:47], v[112:115], v[84:87], v[32:47]\n" \
    "v_cvt_pk_bf16_f32 v72, v48, v49\n" \
    "v_cvt_pk_bf16_f32 v73, v50, v51\n" \
    "v_exp_f32 v52, v52\n" \
    "v_exp_f32 v53, v53\n" \
    "v_exp_f32 v54, v54\n" \
    "s_waitcnt lgkmcnt(1)\n" \
    "v_mfma_f32_32x32x16_bf16 v[0:15], v[116:119], v[64:67], v[0:15]\n" \
    "v_exp_f32 v55, v55\n" \
    "v_cvt_pk_bf16_f32 v74, v52, v53\n" \
    "v_exp_f32 v56, v56\n" \
    "v_exp_f32 v57, v57\n" \
    "v_cvt_pk_bf16_f32 v75, v54, v55\n" \
    "v_mfma_f32_16x16x32_bf16 v[100:103], v[96:99], v[64:67], v[100:103]\n" \
    "v_exp_f32 v58, v58\n" \
    "v_exp_f32 v59, v59\n" \
    "s_waitcnt lgkmcnt(0)\n" \
    "v_mfma_f32_32x32x16_bf16 v[0:15], v[120:123], v[68:71], v[0:15]\n" \
    "v_cvt_pk_bf16_f32 v76, v56, v57\n" \
    "v_exp_f32 v60, v60\n" \
    "v_exp_f32 v61, v61\n" \
    "v_cvt_pk_bf16_f32 v77, v58, v59\n" \
    "v_exp_f32 v62, v62\n" \
    "v_mfma_f32_16x16x32_bf16 v[100:103], v[96:99], v[68:71], v[100:103]\n" \
    "v_exp_f32 v63, v63\n" \
    "v_cvt_pk_bf16_f32 v78, v60, v61\n" \
    "v_cvt_pk_bf16_f32 v79, v62, v63\n" \
    "v_mfma_f32_32x32x16_bf16 v[48:63], v[108:111], v[88:91], 0\n" \
    "v_exp_f32 v32, v32\n" \
    "ds_read_b128 v[108:111], v124 offset:57344\n" \
    "v_exp_f32 v33, v33\n" \
    "v_exp_f32 v34, v34\n" \
    "v_exp_f32 v35, v35\n" \
    "v_mfma_f32_32x32x16_bf16 v[48:63], v[112:115], v[92:95], v[48:63]\n" \
    "v_cvt_pk_bf16_f32 v64, v32, v33\n" \
    "ds_read_b128 v[112:115], v124 offset:58368\n" \
    "v_cvt_pk_bf16_f32 v65, v34, v35\n" \
    "v_exp_f32 v36, v36\n" \
    "v_exp_f32 v37, v37\n" \
    "v_exp_f32 v38, v38\n" \
    "v_mfma_f32_32x32x16_bf16 v[16:31], v[116:119], v[72:75], v[16:31]\n" \
    "v_exp_f32 v39, v39\n" \
    "ds_read_b128 v[116:119], v124 offset:59392\n" \
    "v_cvt_pk_bf16_f32 v66, v36, v37\n" \
    "v_exp_f32 v40, v40\n" \
    "v_exp_f32 v41, v41\n" \
    "v_cvt_pk_bf16_f32 v67, v38, v39\n" \
    "v_mfma_f32_16x16x32_bf16 v[104:107], v[96:99], v[72:75], v[104:107]\n" \
    "v_exp_f32 v42, v42\n" \
    "v_exp_f32 v43, v43\n" \
    "v_mfma_f32_32x32x16_bf16 v[16:31], v[120:123], v[76:79], v[16:31]\n" \
    "v_cvt_pk_bf16_f32 v68, v40, v41\n" \
    "ds_read_b128 v[120:123], v124 offset:60416\n" \
    "v_exp_f32 v44, v44\n" \
    "v_exp_f32 v45, v45\n" \
    "v_cvt_pk_bf16_f32 v69, v42, v43\n" \
    "v_exp_f32 v46, v46\n" \
    "v_mfma_f32_16x16x32_bf16 v[104:107], v[96:99], v[76:79], v[104:107]\n" \
    "v_exp_f32 v47, v47\n" \
    "v_cvt_pk_bf16_f32 v70, v44, v45\n" \
    "v_cvt_pk_bf16_f32 v71, v46, v47\n" \
    "s_waitcnt lgkmcnt(2)\n" \
    "v_mfma_f32_32x32x16_bf16 v[32:47], v[108:111], v[80:83], 0\n" \
    "v_exp_f32 v48, v48\n" \
    "v_exp_f32 v49, v49\n" \
    "v_exp_f32 v50, v50\n" \
    "v_exp_f32 v51, v51\n" \
    "v_mfma_f32_32x32x16_bf16 v[32:47], v[112:115], v[84:87], v[32:47]\n" \
    "v_cvt_pk_bf16_f32 v72, v48, v49\n" \
    "v_cvt_pk_bf16_f32 v73, v50, v51\n" \
    "v_exp_f32 v52, v52\n" \
    "v_exp_f32 v53, v53\n" \
    "v_exp_f32 v54, v54\n" \
    "s_waitcnt lgkmcnt(1)\n" \
    "v_mfma_f32_32x32x16_bf16 v[0:15], v[116:119], v[64:67], v[0:15]\n" \
    "v_exp_f32 v55, v55\n" \
    "v_cvt_pk_bf16_f32 v74, v52, v53\n" \
    "v_exp_f32 v56, v56\n" \
    "v_exp_f32 v57, v57\n" \
    "v_cvt_pk_bf16_f32 v75, v54, v55\n" \
    "v_mfma_f32_16x16x32_bf16 v[100:103], v[96:99], v[64:67], v[100:103]\n" \
    "v_exp_f32 v58, v58\n" \
    "v_exp_f32 v59, v59\n" \
    "s_waitcnt lgkmcnt(0)\n" \
    "v_mfma_f32_32x32x16_bf16 v[0:15], v[120:123], v[68:71], v[0:15]\n" \
    "v_cvt_pk_bf16_f32 v76, v56, v57\n" \
    "v_exp_f32 v60, v60\n" \
    "v_exp_f32 v61, v61\n" \
    "v_cvt_pk_bf16_f32 v77, v58, v59\n" \
    "v_exp_f32 v62, v62\n" \
    "v_mfma_f32_16x16x32_bf16 v[100:103], v[96:99], v[68:71], v[100:103]\n" \
    "v_exp_f32 v63, v63\n" \
    "v_cvt_pk_bf16_f32 v78, v60, v61\n" \
    "v_cvt_pk_bf16_f32 v79, v62, v63\n" \
    "v_mfma_f32_32x32x16_bf16 v[48:63], v[108:111], v[88:91], 0\n" \
    "v_exp_f32 v32, v32\n" \
    "ds_read_b128 v[108:111], v124 offset:61440\n" \
    "v_exp_f32 v33, v33\n" \
    "v_exp_f32 v34, v34\n" \
    "v_exp_f32 v35, v35\n" \
    "v_mfma_f32_32x32x16_bf16 v[48:63], v[112:115], v[92:95], v[48:63]\n" \
    "v_cvt_pk_bf16_f32 v64, v32, v33\n" \
    "ds_read_b128 v[112:115], v124 offset:62464\n" \
    "v_cvt_pk_bf16_f32 v65, v34, v35\n" \
    "v_exp_f32 v36, v36\n" \
    "v_exp_f32 v37, v37\n" \
    "v_exp_f32 v38, v38\n" \
    "v_mfma_f32_32x32x16_bf16 v[16:31], v[116:119], v[72:75], v[16:31]\n" \
    "v_exp_f32 v39, v39\n" \
    "ds_read_b128 v[116:119], v124 offset:63488\n" \
    "v_cvt_pk_bf16_f32 v66, v36, v37\n" \
    "v_exp_f32 v40, v40\n" \
    "v_exp_f32 v41, v41\n" \
    "v_cvt_pk_bf16_f32 v67, v38, v39\n" \
    "v_mfma_f32_16x16x32_bf16 v[104:107], v[96:99], v[72:75], v[104:107]\n" \
    "v_exp_f32 v42, v42\n" \
    "v_exp_f32 v43, v43\n" \
    "v_mfma_f32_32x32x16_bf16 v[16:31], v[120:123], v[76:79], v[16:31]\n" \
    "v_cvt_pk_bf16_f32 v68, v40, v41\n" \
    "ds_read_b128 v[120:123], v124 offset:64512\n" \
    "v_exp_f32 v44, v44\n" \
    "v_exp_f32 v45, v45\n" \
    "v_cvt_pk_bf16_f32 v69, v42, v43\n" \
    "v_exp_f32 v46, v46\n" \
    "v_mfma_f32_16x16x32_bf16 v[104:107], v[96:99], v[76:79], v[104:107]\n" \
    "v_exp_f32 v47, v47\n" \
    "v_cvt_pk_bf16_f32 v70, v44, v45\n" \
    "v_cvt_pk_bf16_f32 v71, v46, v47\n" \
    "s_waitcnt lgkmcnt(2)\n" \
    "v_mfma_f32_32x32x16_bf16 v[32:47], v[108:111], v[80:83], 0\n" \
    "v_exp_f32 v48, v48\n" \
    "v_exp_f32 v49, v49\n" \
    "v_exp_f32 v50, v50\n" \
    "v_exp_f32 v51, v51\n" \
    "v_mfma_f32_32x32x16_bf16 v[32:47], v[112:115], v[84:87], v[32:47]\n" \
    "v_cvt_pk_bf16_f32 v72, v48, v49\n" \
    "v_cvt_pk_bf16_f32 v73, v50, v51\n" \
    "v_exp_f32 v52, v52\n" \
    "v_exp_f32 v53, v53\n" \
    "v_exp_f32 v54, v54\n" \
    "s_waitcnt lgkmcnt(1)\n" \
    "v_mfma_f32_32x32x16_bf16 v[0:15], v[116:119], v[64:67], v[0:15]\n" \
    "v_exp_f32 v55, v55\n" \
    "v_cvt_pk_bf16_f32 v74, v52, v53\n" \
    "v_exp_f32 v56, v56\n" \
    "v_exp_f32 v57, v57\n" \
    "v_cvt_pk_bf16_f32 v75, v54, v55\n" \
    "v_mfma_f32_16x16x32_bf16 v[100:103], v[96:99], v[64:67], v[100:103]\n" \
    "v_exp_f32 v58, v58\n" \
    "v_exp_f32 v59, v59\n" \
    "s_waitcnt lgkmcnt(0)\n" \
    "v_mfma_f32_32x32x16_bf16 v[0:15], v[120:123], v[68:71], v[0:15]\n" \
    "v_cvt_pk_bf16_f32 v76, v56, v57\n" \
    "v_exp_f32 v60, v60\n" \
    "v_exp_f32 v61, v61\n" \
    "v_cvt_pk_bf16_f32 v77, v58, v59\n" \
    "v_exp_f32 v62, v62\n" \
    "v_mfma_f32_16x16x32_bf16 v[100:103], v[96:99], v[68:71], v[100:103]\n" \
    "v_exp_f32 v63, v63\n" \
    "v_cvt_pk_bf16_f32 v78, v60, v61\n" \
    "v_cvt_pk_bf16_f32 v79, v62, v63\n" \
    "s_barrier\n" \
    "v_mfma_f32_32x32x16_bf16 v[48:63], v[108:111], v[88:91], 0\n" \
    "v_exp_f32 v32, v32\n" \
    "ds_read_b128 v[108:111], v124 offset:0\n" \
    "v_exp_f32 v33, v33\n" \
    "v_exp_f32 v34, v34\n" \
    "v_exp_f32 v35, v35\n" \
    "v_mfma_f32_32x32x16_bf16 v[48:63], v[112:115], v[92:95], v[48:63]\n" \
    "v_cvt_pk_bf16_f32 v64, v32, v33\n" \
    "ds_read_b128 v[112:115], v124 offset:1024\n" \
    "v_cvt_pk_bf16_f32 v65, v34, v35\n" \
    "v_exp_f32 v36, v36\n" \
    "v_exp_f32 v37, v37\n" \
    "v_exp_f32 v38, v38\n" \
    "v_mfma_f32_32x32x16_bf16 v[16:31], v[116:119], v[72:75], v[16:31]\n" \
    "v_exp_f32 v39, v39\n" \
    "ds_read_b128 v[116:119], v124 offset:2048\n" \
    "v_cvt_pk_bf16_f32 v66, v36, v37\n" \
    "v_exp_f32 v40, v40\n" \
    "v_exp_f32 v41, v41\n" \
    "v_cvt_pk_bf16_f32 v67, v38, v39\n" \
    "v_mfma_f32_16x16x32_bf16 v[104:107], v[96:99], v[72:75], v[104:107]\n" \
    "v_exp_f32 v42, v42\n" \
    "v_exp_f32 v43, v43\n" \
    "v_mfma_f32_32x32x16_bf16 v[16:31], v[120:123], v[76:79], v[16:31]\n" \
    "v_cvt_pk_bf16_f32 v68, v40, v41\n" \
    "ds_read_b128 v[120:123], v124 offset:3072\n" \
    "v_exp_f32 v44, v44\n" \
    "v_exp_f32 v45, v45\n" \
    "v_cvt_pk_bf16_f32 v69, v42, v43\n" \
    "v_exp_f32 v46, v46\n" \
    "v_mfma_f32_16x16x32_bf16 v[104:107], v[96:99], v[76:79], v[104:107]\n" \
    "v_exp_f32 v47, v47\n" \
    "v_cvt_pk_bf16_f32 v70, v44, v45\n" \
    "v_cvt_pk_bf16_f32 v71, v46, v47\n" \
    "s_waitcnt lgkmcnt(2)\n" \
    "v_mfma_f32_32x32x16_bf16 v[32:47], v[108:111], v[80:83], 0\n" \
    "v_exp_f32 v48, v48\n" \
    "v_exp_f32 v49, v49\n" \
    "v_exp_f32 v50, v50\n" \
    "v_exp_f32 v51, v51\n" \
    "v_mfma_f32_32x32x16_bf16 v[32:47], v[112:115], v[84:87], v[32:47]\n" \
    "v_cvt_pk_bf16_f32 v72, v48, v49\n" \
    "v_cvt_pk_bf16_f32 v73, v50, v51\n" \
    "v_exp_f32 v52, v52\n" \
    "v_exp_f32 v53, v53\n" \
    "v_exp_f32 v54, v54\n" \
    "s_waitcnt lgkmcnt(1)\n" \
    "v_mfma_f32_32x32x16_bf16 v[0:15], v[116:119], v[64:67], v[0:15]\n" \
    "v_exp_f32 v55, v55\n" \
    "v_cvt_pk_bf16_f32 v74, v52, v53\n" \
    "v_exp_f32 v56, v56\n" \
    "v_exp_f32 v57, v57\n" \
    "v_cvt_pk_bf16_f32 v75, v54, v55\n" \
    "v_mfma_f32_16x16x32_bf16 v[100:103], v[96:99], v[64:67], v[100:103]\n" \
    "v_exp_f32 v58, v58\n" \
    "v_exp_f32 v59, v59\n" \
    "s_waitcnt lgkmcnt(0)\n" \
    "v_mfma_f32_32x32x16_bf16 v[0:15], v[120:123], v[68:71], v[0:15]\n" \
    "v_cvt_pk_bf16_f32 v76, v56, v57\n" \
    "v_exp_f32 v60, v60\n" \
    "v_exp_f32 v61, v61\n" \
    "v_cvt_pk_bf16_f32 v77, v58, v59\n" \
    "v_exp_f32 v62, v62\n" \
    "v_mfma_f32_16x16x32_bf16 v[100:103], v[96:99], v[68:71], v[100:103]\n" \
    "v_exp_f32 v63, v63\n" \
    "v_cvt_pk_bf16_f32 v78, v60, v61\n" \
    "v_cvt_pk_bf16_f32 v79, v62, v63\n" \
    "v_mfma_f32_32x32x16_bf16 v[48:63], v[108:111], v[88:91], 0\n" \
    "v_exp_f32 v32, v32\n" \
    "ds_read_b128 v[108:111], v124 offset:4096\n" \
    "v_exp_f32 v33, v33\n" \
    "v_exp_f32 v34, v34\n" \
    "v_exp_f32 v35, v35\n" \
    "v_mfma_f32_32x32x16_bf16 v[48:63], v[112:115], v[92:95], v[48:63]\n" \
    "v_cvt_pk_bf16_f32 v64, v32, v33\n" \
    "ds_read_b128 v[112:115], v124 offset:5120\n" \
    "v_cvt_pk_bf16_f32 v65, v34, v35\n" \
    "v_exp_f32 v36, v36\n" \
    "v_exp_f32 v37, v37\n" \
    "v_exp_f32 v38, v38\n" \
    "v_mfma_f32_32x32x16_bf16 v[16:31], v[116:119], v[72:75], v[16:31]\n" \
    "v_exp_f32 v39, v39\n" \
    "ds_read_b128 v[116:119], v124 offset:6144\n" \
    "v_cvt_pk_bf16_f32 v66, v36, v37\n" \
    "v_exp_f32 v40, v40\n" \
    "v_exp_f32 v41, v41\n" \
    "v_cvt_pk_bf16_f32 v67, v38, v39\n" \
    "v_mfma_f32_16x16x32_bf16 v[104:107], v[96:99], v[72:75], v[104:107]\n" \
    "v_exp_f32 v42, v42\n" \
    "v_exp_f32 v43, v43\n" \
    "v_mfma_f32_32x32x16_bf16 v[16:31], v[120:123], v[76:79], v[16:31]\n" \
    "v_cvt_pk_bf16_f32 v68, v40, v41\n" \
    "ds_read_b128 v[120:123], v124 offset:7168\n" \
    "v_exp_f32 v44, v44\n" \
    "v_exp_f32 v45, v45\n" \
    "v_cvt_pk_bf16_f32 v69, v42, v43\n" \
    "v_exp_f32 v46, v46\n" \
    "v_mfma_f32_16x16x32_bf16 v[104:107], v[96:99], v[76:79], v[104:107]\n" \
    "v_exp_f32 v47, v47\n" \
    "v_cvt_pk_bf16_f32 v70, v44, v45\n" \
    "v_cvt_pk_bf16_f32 v71, v46, v47\n" \
    "s_waitcnt lgkmcnt(2)\n" \
    "v_mfma_f32_32x32x16_bf16 v[32:47], v[108:111], v[80:83], 0\n" \
    "v_exp_f32 v48, v48\n" \
    "v_exp_f32 v49, v49\n" \
    "v_exp_f32 v50, v50\n" \
    "v_exp_f32 v51, v51\n" \
    "v_mfma_f32_32x32x16_bf16 v[32:47], v[112:115], v[84:87], v[32:47]\n" \
    "v_cvt_pk_bf16_f32 v72, v48, v49\n" \
    "v_cvt_pk_bf16_f32 v73, v50, v51\n" \
    "v_exp_f32 v52, v52\n" \
    "v_exp_f32 v53, v53\n" \
    "v_exp_f32 v54, v54\n" \
    "s_waitcnt lgkmcnt(1)\n" \
    "v_mfma_f32_32x32x16_bf16 v[0:15], v[116:119], v[64:67], v[0:15]\n" \
    "v_exp_f32 v55, v55\n" \
    "v_cvt_pk_bf16_f32 v74, v52, v53\n" \
    "v_exp_f32 v56, v56\n" \
    "v_exp_f32 v57, v57\n" \
    "v_cvt_pk_bf16_f32 v75, v54, v55\n" \
    "v_mfma_f32_16x16x32_bf16 v[100:103], v[96:99], v[64:67], v[100:103]\n" \
    "v_exp_f32 v58, v58\n" \
    "v_exp_f32 v59, v59\n" \
    "s_waitcnt lgkmcnt(0)\n" \
    "v_mfma_f32_32x32x16_bf16 v[0:15], v[120:123], v[68:71], v[0:15]\n" \
    "v_cvt_pk_bf16_f32 v76, v56, v57\n" \
    "v_exp_f32 v60, v60\n" \
    "v_exp_f32 v61, v61\n" \
    "v_cvt_pk_bf16_f32 v77, v58, v59\n" \
    "v_exp_f32 v62, v62\n" \
    "v_mfma_f32_16x16x32_bf16 v[100:103], v[96:99], v[68:71], v[100:103]\n" \
    "v_exp_f32 v63, v63\n" \
    "v_cvt_pk_bf16_f32 v78, v60, v61\n" \
    "v_cvt_pk_bf16_f32 v79, v62, v63\n" \
    "v_mfma_f32_32x32x16_bf16 v[48:63], v[108:111], v[88:91], 0\n" \
    "v_exp_f32 v32, v32\n" \
    "ds_read_b128 v[108:111], v124 offset:8192\n" \
    "v_exp_f32 v33, v33\n" \
    "v_exp_f32 v34, v34\n" \
    "v_exp_f32 v35, v35\n" \
    "v_mfma_f32_32x32x16_bf16 v[48:63], v[112:115], v[92:95], v[48:63]\n" \
    "v_cvt_pk_bf16_f32 v64, v32, v33\n" \
    "ds_read_b128 v[112:115], v124 offset:9216\n" \
    "v_cvt_pk_bf16_f32 v65, v34, v35\n" \
    "v_exp_f32 v36, v36\n" \
    "v_exp_f32 v37, v37\n" \
    "v_exp_f32 v38, v38\n" \
    "v_mfma_f32_32x32x16_bf16 v[16:31], v[116:119], v[72:75], v[16:31]\n" \
    "v_exp_f32 v39, v39\n" \
    "ds_read_b128 v[116:119], v124 offset:10240\n" \
    "v_cvt_pk_bf16_f32 v66, v36, v37\n" \
    "v_exp_f32 v40, v40\n" \
    "v_exp_f32 v41, v41\n" \
    "v_cvt_pk_bf16_f32 v67, v38, v39\n" \
    "v_mfma_f32_16x16x32_bf16 v[104:107], v[96:99], v[72:75], v[104:107]\n" \
    "v_exp_f32 v42, v42\n" \
    "v_exp_f32 v43, v43\n" \
    "v_mfma_f32_32x32x16_bf16 v[16:31], v[120:123], v[76:79], v[16:31]\n" \
    "v_cvt_pk_bf16_f32 v68, v40, v41\n" \
    "ds_read_b128 v[120:123], v124 offset:11264\n" \
    "v_exp_f32 v44, v44\n" \
    "v_exp_f32 v45, v45\n" \
    "v_cvt_pk_bf16_f32 v69, v42, v43\n" \
    "v_exp_f32 v46, v46\n" \
    "v_mfma_f32_16x16x32_bf16 v[104:107], v[96:99], v[76:79], v[104:107]\n" \
    "v_exp_f32 v47, v47\n" \
    "v_cvt_pk_bf16_f32 v70, v44, v45\n" \
    "v_cvt_pk_bf16_f32 v71, v46, v47\n" \
    "s_waitcnt lgkmcnt(2)\n" \
    "v_mfma_f32_32x32x16_bf16 v[32:47], v[108:111], v[80:83], 0\n" \
    "v_exp_f32 v48, v48\n" \
    "v_exp_f32 v49, v49\n" \
    "v_exp_f32 v50, v50\n" \
    "v_exp_f32 v51, v51\n" \
    "v_mfma_f32_32x32x16_bf16 v[32:47], v[112:115], v[84:87], v[32:47]\n" \
    "v_cvt_pk_bf16_f32 v72, v48, v49\n" \
    "v_cvt_pk_bf16_f32 v73, v50, v51\n" \
    "v_exp_f32 v52, v52\n" \
    "v_exp_f32 v53, v53\n" \
    "v_exp_f32 v54, v54\n" \
    "s_waitcnt lgkmcnt(1)\n" \
    "v_mfma_f32_32x32x16_bf16 v[0:15], v[116:119], v[64:67], v[0:15]\n" \
    "v_exp_f32 v55, v55\n" \
    "v_cvt_pk_bf16_f32 v74, v52, v53\n" \
    "v_exp_f32 v56, v56\n" \
    "v_exp_f32 v57, v57\n" \
    "v_cvt_pk_bf16_f32 v75, v54, v55\n" \
    "v_mfma_f32_16x16x32_bf16 v[100:103], v[96:99], v[64:67], v[100:103]\n" \
    "v_exp_f32 v58, v58\n" \
    "v_exp_f32 v59, v59\n" \
    "s_waitcnt lgkmcnt(0)\n" \
    "v_mfma_f32_32x32x16_bf16 v[0:15], v[120:123], v[68:71], v[0:15]\n" \
    "v_cvt_pk_bf16_f32 v76, v56, v57\n" \
    "v_exp_f32 v60, v60\n" \
    "v_exp_f32 v61, v61\n" \
    "v_cvt_pk_bf16_f32 v77, v58, v59\n" \
    "v_exp_f32 v62, v62\n" \
    "v_mfma_f32_16x16x32_bf16 v[100:103], v[96:99], v[68:71], v[100:103]\n" \
    "v_exp_f32 v63, v63\n" \
    "v_cvt_pk_bf16_f32 v78, v60, v61\n" \
    "v_cvt_pk_bf16_f32 v79, v62, v63\n" \
    "v_mfma_f32_32x32x16_bf16 v[48:63], v[108:111], v[88:91], 0\n" \
    "v_exp_f32 v32, v32\n" \
    "ds_read_b128 v[108:111], v124 offset:12288\n" \
    "v_exp_f32 v33, v33\n" \
    "v_exp_f32 v34, v34\n" \
    "v_exp_f32 v35, v35\n" \
    "v_mfma_f32_32x32x16_bf16 v[48:63], v[112:115], v[92:95], v[48:63]\n" \
    "v_cvt_pk_bf16_f32 v64, v32, v33\n" \
    "ds_read_b128 v[112:115], v124 offset:13312\n" \
    "v_cvt_pk_bf16_f32 v65, v34, v35\n" \
    "v_exp_f32 v36, v36\n" \
    "v_exp_f32 v37, v37\n" \
    "v_exp_f32 v38, v38\n" \
    "v_mfma_f32_32x32x16_bf16 v[16:31], v[116:119], v[72:75], v[16:31]\n" \
    "v_exp_f32 v39, v39\n" \
    "ds_read_b128 v[116:119], v124 offset:14336\n" \
    "v_cvt_pk_bf16_f32 v66, v36, v37\n" \
    "v_exp_f32 v40, v40\n" \
    "v_exp_f32 v41, v41\n" \
    "v_cvt_pk_bf16_f32 v67, v38, v39\n" \
    "v_mfma_f32_16x16x32_bf16 v[104:107], v[96:99], v[72:75], v[104:107]\n" \
    "v_exp_f32 v42, v42\n" \
    "v_exp_f32 v43, v43\n" \
    "v_mfma_f32_32x32x16_bf16 v[16:31], v[120:123], v[76:79], v[16:31]\n" \
    "v_cvt_pk_bf16_f32 v68, v40, v41\n" \
    "ds_read_b128 v[120:123], v124 offset:15360\n" \
    "v_exp_f32 v44, v44\n" \
    "v_exp_f32 v45, v45\n" \
    "v_cvt_pk_bf16_f32 v69, v42, v43\n" \
    "v_exp_f32 v46, v46\n" \
    "v_mfma_f32_16x16x32_bf16 v[104:107], v[96:99], v[76:79], v[104:107]\n" \
    "v_exp_f32 v47, v47\n" \
    "v_cvt_pk_bf16_f32 v70, v44, v45\n" \
    "v_cvt_pk_bf16_f32 v71, v46, v47\n" \
    "s_waitcnt lgkmcnt(2)\n" \
    "v_mfma_f32_32x32x16_bf16 v[32:47], v[108:111], v[80:83], 0\n" \
    "v_exp_f32 v48, v48\n" \
    "v_exp_f32 v49, v49\n" \
    "v_exp_f32 v50, v50\n" \
    "v_exp_f32 v51, v51\n" \
    "v_mfma_f32_32x32x16_bf16 v[32:47], v[112:115], v[84:87], v[32:47]\n" \
    "v_cvt_pk_bf16_f32 v72, v48, v49\n" \
    "v_cvt_pk_bf16_f32 v73, v50, v51\n" \
    "v_exp_f32 v52, v52\n" \
    "v_exp_f32 v53, v53\n" \
    "v_exp_f32 v54, v54\n" \
    "s_waitcnt lgkmcnt(1)\n" \
    "v_mfma_f32_32x32x16_bf16 v[0:15], v[116:119], v[64:67], v[0:15]\n" \
    "v_exp_f32 v55, v55\n" \
    "v_cvt_pk_bf16_f32 v74, v52, v53\n" \
    "v_exp_f32 v56, v56\n" \
    "v_exp_f32 v57, v57\n" \
    "v_cvt_pk_bf16_f32 v75, v54, v55\n" \
    "v_mfma_f32_16x16x32_bf16 v[100:103], v[96:99], v[64:67], v[100:103]\n" \
    "v_exp_f32 v58, v58\n" \
    "v_exp_f32 v59, v59\n" \
    "s_waitcnt lgkmcnt(0)\n" \
    "v_mfma_f32_32x32x16_bf16 v[0:15], v[120:123], v[68:71], v[0:15]\n" \
    "v_cvt_pk_bf16_f32 v76, v56, v57\n" \
    "v_exp_f32 v60, v60\n" \
    "v_exp_f32 v61, v61\n" \
    "v_cvt_pk_bf16_f32 v77, v58, v59\n" \
    "v_exp_f32 v62, v62\n" \
    "v_mfma_f32_16x16x32_bf16 v[100:103], v[96:99], v[68:71], v[100:103]\n" \
    "v_exp_f32 v63, v63\n" \
    "v_cvt_pk_bf16_f32 v78, v60, v61\n" \
    "v_cvt_pk_bf16_f32 v79, v62, v63\n" \
    "v_mfma_f32_32x32x16_bf16 v[48:63], v[108:111], v[88:91], 0\n" \
    "v_exp_f32 v32, v32\n" \
    "ds_read_b128 v[108:111], v124 offset:16384\n" \
    "v_exp_f32 v33, v33\n" \
    "v_exp_f32 v34, v34\n" \
    "v_exp_f32 v35, v35\n" \
    "v_mfma_f32_32x32x16_bf16 v[48:63], v[112:115], v[92:95], v[48:63]\n" \
    "v_cvt_pk_bf16_f32 v64, v32, v33\n" \
    "ds_read_b128 v[112:115], v124 offset:17408\n" \
    "v_cvt_pk_bf16_f32 v65, v34, v35\n" \
    "v_exp_f32 v36, v36\n" \
    "v_exp_f32 v37, v37\n" \
    "v_exp_f32 v38, v38\n" \
    "v_mfma_f32_32x32x16_bf16 v[16:31], v[116:119], v[72:75], v[16:31]\n" \
    "v_exp_f32 v39, v39\n" \
    "ds_read_b128 v[116:119], v124 offset:18432\n" \
    "v_cvt_pk_bf16_f32 v66, v36, v37\n" \
    "v_exp_f32 v40, v40\n" \
    "v_exp_f32 v41, v41\n" \
    "v_cvt_pk_bf16_f32 v67, v38, v39\n" \
    "v_mfma_f32_16x16x32_bf16 v[104:107], v[96:99], v[72:75], v[104:107]\n" \
    "v_exp_f32 v42, v42\n" \
    "v_exp_f32 v43, v43\n" \
    "v_mfma_f32_32x32x16_bf16 v[16:31], v[120:123], v[76:79], v[16:31]\n" \
    "v_cvt_pk_bf16_f32 v68, v40, v41\n" \
    "ds_read_b128 v[120:123], v124 offset:19456\n" \
    "v_exp_f32 v44, v44\n" \
    "v_exp_f32 v45, v45\n" \
    "v_cvt_pk_bf16_f32 v69, v42, v43\n" \
    "v_exp_f32 v46, v46\n" \
    "v_mfma_f32_16x16x32_bf16 v[104:107], v[96:99], v[76:79], v[104:107]\n" \
    "v_exp_f32 v47, v47\n" \
    "v_cvt_pk_bf16_f32 v70, v44, v45\n" \
    "v_cvt_pk_bf16_f32 v71, v46, v47\n" \
    "s_waitcnt lgkmcnt(2)\n" \
    "v_mfma_f32_32x32x16_bf16 v[32:47], v[108:111], v[80:83], 0\n" \
    "v_exp_f32 v48, v48\n" \
    "v_exp_f32 v49, v49\n" \
    "v_exp_f32 v50, v50\n" \
    "v_exp_f32 v51, v51\n" \
    "v_mfma_f32_32x32x16_bf16 v[32:47], v[112:115], v[84:87], v[32:47]\n" \
    "v_cvt_pk_bf16_f32 v72, v48, v49\n" \
    "v_cvt_pk_bf16_f32 v73, v50, v51\n" \
    "v_exp_f32 v52, v52\n" \
    "v_exp_f32 v53, v53\n" \
    "v_exp_f32 v54, v54\n" \
    "s_waitcnt lgkmcnt(1)\n" \
    "v_mfma_f32_32x32x16_bf16 v[0:15], v[116:119], v[64:67], v[0:15]\n" \
    "v_exp_f32 v55, v55\n" \
    "v_cvt_pk_bf16_f32 v74, v52, v53\n" \
    "v_exp_f32 v56, v56\n" \
    "v_exp_f32 v57, v57\n" \
    "v_cvt_pk_bf16_f32 v75, v54, v55\n" \
    "v_mfma_f32_16x16x32_bf16 v[100:103], v[96:99], v[64:67], v[100:103]\n" \
    "v_exp_f32 v58, v58\n" \
    "v_exp_f32 v59, v59\n" \
    "s_waitcnt lgkmcnt(0)\n" \
    "v_mfma_f32_32x32x16_bf16 v[0:15], v[120:123], v[68:71], v[0:15]\n" \
    "v_cvt_pk_bf16_f32 v76, v56, v57\n" \
    "v_exp_f32 v60, v60\n" \
    "v_exp_f32 v61, v61\n" \
    "v_cvt_pk_bf16_f32 v77, v58, v59\n" \
    "v_exp_f32 v62, v62\n" \
    "v_mfma_f32_16x16x32_bf16 v[100:103], v[96:99], v[68:71], v[100:103]\n" \
    "v_exp_f32 v63, v63\n" \
    "v_cvt_pk_bf16_f32 v78, v60, v61\n" \
    "v_cvt_pk_bf16_f32 v79, v62, v63\n" \
    "v_mfma_f32_32x32x16_bf16 v[48:63], v[108:111], v[88:91], 0\n" \
    "v_exp_f32 v32, v32\n" \
    "ds_read_b128 v[108:111], v124 offset:20480\n" \
    "v_exp_f32 v33, v33\n" \
    "v_exp_f32 v34, v34\n" \
    "v_exp_f32 v35, v35\n" \
    "v_mfma_f32_32x32x16_bf16 v[48:63], v[112:115], v[92:95], v[48:63]\n" \
    "v_cvt_pk_bf16_f32 v64, v32, v33\n" \
    "ds_read_b128 v[112:115], v124 offset:21504\n" \
    "v_cvt_pk_bf16_f32 v65, v34, v35\n" \
    "v_exp_f32 v36, v36\n" \
    "v_exp_f32 v37, v37\n" \
    "v_exp_f32 v38, v38\n" \
    "v_mfma_f32_32x32x16_bf16 v[16:31], v[116:119], v[72:75], v[16:31]\n" \
    "v_exp_f32 v39, v39\n" \
    "ds_read_b128 v[116:119], v124 offset:22528\n" \
    "v_cvt_pk_bf16_f32 v66, v36, v37\n" \
    "v_exp_f32 v40, v40\n" \
    "v_exp_f32 v41, v41\n" \
    "v_cvt_pk_bf16_f32 v67, v38, v39\n" \
    "v_mfma_f32_16x16x32_bf16 v[104:107], v[96:99], v[72:75], v[104:107]\n" \
    "v_exp_f32 v42, v42\n" \
    "v_exp_f32 v43, v43\n" \
    "v_mfma_f32_32x32x16_bf16 v[16:31], v[120:123], v[76:79], v[16:31]\n" \
    "v_cvt_pk_bf16_f32 v68, v40, v41\n" \
    "ds_read_b128 v[120:123], v124 offset:23552\n" \
    "v_exp_f32 v44, v44\n" \
    "v_exp_f32 v45, v45\n" \
    "v_cvt_pk_bf16_f32 v69, v42, v43\n" \
    "v_exp_f32 v46, v46\n" \
    "v_mfma_f32_16x16x32_bf16 v[104:107], v[96:99], v[76:79], v[104:107]\n" \
    "v_exp_f32 v47, v47\n" \
    "v_cvt_pk_bf16_f32 v70, v44, v45\n" \
    "v_cvt_pk_bf16_f32 v71, v46, v47\n" \
    "s_waitcnt lgkmcnt(2)\n" \
    "v_mfma_f32_32x32x16_bf16 v[32:47], v[108:111], v[80:83], 0\n" \
    "v_exp_f32 v48, v48\n" \
    "v_exp_f32 v49, v49\n" \
    "v_exp_f32 v50, v50\n" \
    "v_exp_f32 v51, v51\n" \
    "v_mfma_f32_32x32x16_bf16 v[32:47], v[112:115], v[84:87], v[32:47]\n" \
    "v_cvt_pk_bf16_f32 v72, v48, v49\n" \
    "v_cvt_pk_bf16_f32 v73, v50, v51\n" \
    "v_exp_f32 v52, v52\n" \
    "v_exp_f32 v53, v53\n" \
    "v_exp_f32 v54, v54\n" \
    "s_waitcnt lgkmcnt(1)\n" \
    "v_mfma_f32_32x32x16_bf16 v[0:15], v[116:119], v[64:67], v[0:15]\n" \
    "v_exp_f32 v55, v55\n" \
    "v_cvt_pk_bf16_f32 v74, v52, v53\n" \
    "v_exp_f32 v56, v56\n" \
    "v_exp_f32 v57, v57\n" \
    "v_cvt_pk_bf16_f32 v75, v54, v55\n" \
    "v_mfma_f32_16x16x32_bf16 v[100:103], v[96:99], v[64:67], v[100:103]\n" \
    "v_exp_f32 v58, v58\n" \
    "v_exp_f32 v59, v59\n" \
    "s_waitcnt lgkmcnt(0)\n" \
    "v_mfma_f32_32x32x16_bf16 v[0:15], v[120:123], v[68:71], v[0:15]\n" \
    "v_cvt_pk_bf16_f32 v76, v56, v57\n" \
    "v_exp_f32 v60, v60\n" \
    "v_exp_f32 v61, v61\n" \
    "v_cvt_pk_bf16_f32 v77, v58, v59\n" \
    "v_exp_f32 v62, v62\n" \
    "v_mfma_f32_16x16x32_bf16 v[100:103], v[96:99], v[68:71], v[100:103]\n" \
    "v_exp_f32 v63, v63\n" \
    "v_cvt_pk_bf16_f32 v78, v60, v61\n" \
    "v_cvt_pk_bf16_f32 v79, v62, v63\n" \
    "v_mfma_f32_32x32x16_bf16 v[48:63], v[108:111], v[88:91], 0\n" \
    "v_exp_f32 v32, v32\n" \
    "ds_read_b128 v[108:111], v124 offset:24576\n" \
    "v_exp_f32 v33, v33\n" \
    "v_exp_f32 v34, v34\n" \
    "v_exp_f32 v35, v35\n" \
    "v_mfma_f32_32x32x16_bf16 v[48:63], v[112:115], v[92:95], v[48:63]\n" \
    "v_cvt_pk_bf16_f32 v64, v32, v33\n" \
    "ds_read_b128 v[112:115], v124 offset:25600\n" \
    "v_cvt_pk_bf16_f32 v65, v34, v35\n" \
    "v_exp_f32 v36, v36\n" \
    "v_exp_f32 v37, v37\n" \
    "v_exp_f32 v38, v38\n" \
    "v_mfma_f32_32x32x16_bf16 v[16:31], v[116:119], v[72:75], v[16:31]\n" \
    "v_exp_f32 v39, v39\n" \
    "ds_read_b128 v[116:119], v124 offset:26624\n" \
    "v_cvt_pk_bf16_f32 v66, v36, v37\n" \
    "v_exp_f32 v40, v40\n" \
    "v_exp_f32 v41, v41\n" \
    "v_cvt_pk_bf16_f32 v67, v38, v39\n" \
    "v_mfma_f32_16x16x32_bf16 v[104:107], v[96:99], v[72:75], v[104:107]\n" \
    "v_exp_f32 v42, v42\n" \
    "v_exp_f32 v43, v43\n" \
    "v_mfma_f32_32x32x16_bf16 v[16:31], v[120:123], v[76:79], v[16:31]\n" \
    "v_cvt_pk_bf16_f32 v68, v40, v41\n" \
    "ds_read_b128 v[120:123], v124 offset:27648\n" \
    "v_exp_f32 v44, v44\n" \
    "v_exp_f32 v45, v45\n" \
    "v_cvt_pk_bf16_f32 v69, v42, v43\n" \
    "v_exp_f32 v46, v46\n" \
    "v_mfma_f32_16x16x32_bf16 v[104:107], v[96:99], v[76:79], v[104:107]\n" \
    "v_exp_f32 v47, v47\n" \
    "v_cvt_pk_bf16_f32 v70, v44, v45\n" \
    "v_cvt_pk_bf16_f32 v71, v46, v47\n" \
    "s_waitcnt lgkmcnt(2)\n" \
    "v_mfma_f32_32x32x16_bf16 v[32:47], v[108:111], v[80:83], 0\n" \
    "v_exp_f32 v48, v48\n" \
    "v_exp_f32 v49, v49\n" \
    "v_exp_f32 v50, v50\n" \
    "v_exp_f32 v51, v51\n" \
    "v_mfma_f32_32x32x16_bf16 v[32:47], v[112:115], v[84:87], v[32:47]\n" \
    "v_cvt_pk_bf16_f32 v72, v48, v49\n" \
    "v_cvt_pk_bf16_f32 v73, v50, v51\n" \
    "v_exp_f32 v52, v52\n" \
    "v_exp_f32 v53, v53\n" \
    "v_exp_f32 v54, v54\n" \
    "s_waitcnt lgkmcnt(1)\n" \
    "v_mfma_f32_32x32x16_bf16 v[0:15], v[116:119], v[64:67], v[0:15]\n" \
    "v_exp_f32 v55, v55\n" \
    "v_cvt_pk_bf16_f32 v74, v52, v53\n" \
    "v_exp_f32 v56, v56\n" \
    "v_exp_f32 v57, v57\n" \
    "v_cvt_pk_bf16_f32 v75, v54, v55\n" \
    "v_mfma_f32_16x16x32_bf16 v[100:103], v[96:99], v[64:67], v[100:103]\n" \
    "v_exp_f32 v58, v58\n" \
    "v_exp_f32 v59, v59\n" \
    "s_waitcnt lgkmcnt(0)\n" \
    "v_mfma_f32_32x32x16_bf16 v[0:15], v[120:123], v[68:71], v[0:15]\n" \
    "v_cvt_pk_bf16_f32 v76, v56, v57\n" \
    "v_exp_f32 v60, v60\n" \
    "v_exp_f32 v61, v61\n" \
    "v_cvt_pk_bf16_f32 v77, v58, v59\n" \
    "v_exp_f32 v62, v62\n" \
    "v_mfma_f32_16x16x32_bf16 v[100:103], v[96:99], v[68:71], v[100:103]\n" \
    "v_exp_f32 v63, v63\n" \
    "v_cvt_pk_bf16_f32 v78, v60, v61\n" \
    "v_cvt_pk_bf16_f32 v79, v62, v63\n" \
    "v_mfma_f32_32x32x16_bf16 v[48:63], v[108:111], v[88:91], 0\n" \
    "v_exp_f32 v32, v32\n" \
    "ds_read_b128 v[108:111], v124 offset:28672\n" \
    "v_exp_f32 v33, v33\n" \
    "v_exp_f32 v34, v34\n" \
    "v_exp_f32 v35, v35\n" \
    "v_mfma_f32_32x32x16_bf16 v[48:63], v[112:115], v[92:95], v[48:63]\n" \
    "v_cvt_pk_bf16_f32 v64, v32, v33\n" \
    "ds_read_b128 v[112:115], v124 offset:29696\n" \
    "v_cvt_pk_bf16_f32 v65, v34, v35\n" \
    "v_exp_f32 v36, v36\n" \
    "v_exp_f32 v37, v37\n" \
    "v_exp_f32 v38, v38\n" \
    "v_mfma_f32_32x32x16_bf16 v[16:31], v[116:119], v[72:75], v[16:31]\n" \
    "v_exp_f32 v39, v39\n" \
    "ds_read_b128 v[116:119], v124 offset:30720\n" \
    "v_cvt_pk_bf16_f32 v66, v36, v37\n" \
    "v_exp_f32 v40, v40\n" \
    "v_exp_f32 v41, v41\n" \
    "v_cvt_pk_bf16_f32 v67, v38, v39\n" \
    "v_mfma_f32_16x16x32_bf16 v[104:107], v[96:99], v[72:75], v[104:107]\n" \
    "v_exp_f32 v42, v42\n" \
    "v_exp_f32 v43, v43\n" \
    "v_mfma_f32_32x32x16_bf16 v[16:31], v[120:123], v[76:79], v[16:31]\n" \
    "v_cvt_pk_bf16_f32 v68, v40, v41\n" \
    "ds_read_b128 v[120:123], v124 offset:31744\n" \
    "v_exp_f32 v44, v44\n" \
    "v_exp_f32 v45, v45\n" \
    "v_cvt_pk_bf16_f32 v69, v42, v43\n" \
    "v_exp_f32 v46, v46\n" \
    "v_mfma_f32_16x16x32_bf16 v[104:107], v[96:99], v[76:79], v[104:107]\n" \
    "v_exp_f32 v47, v47\n" \
    "v_cvt_pk_bf16_f32 v70, v44, v45\n" \
    "v_cvt_pk_bf16_f32 v71, v46, v47\n" \
    "s_waitcnt lgkmcnt(2)\n" \
    "v_mfma_f32_32x32x16_bf16 v[32:47], v[108:111], v[80:83], 0\n" \
    "v_exp_f32 v48, v48\n" \
    "v_exp_f32 v49, v49\n" \
    "v_exp_f32 v50, v50\n" \
    "v_exp_f32 v51, v51\n" \
    "v_mfma_f32_32x32x16_bf16 v[32:47], v[112:115], v[84:87], v[32:47]\n" \
    "v_cvt_pk_bf16_f32 v72, v48, v49\n" \
    "v_cvt_pk_bf16_f32 v73, v50, v51\n" \
    "v_exp_f32 v52, v52\n" \
    "v_exp_f32 v53, v53\n" \
    "v_exp_f32 v54, v54\n" \
    "s_waitcnt lgkmcnt(1)\n" \
    "v_mfma_f32_32x32x16_bf16 v[0:15], v[116:119], v[64:67], v[0:15]\n" \
    "v_exp_f32 v55, v55\n" \
    "v_cvt_pk_bf16_f32 v74, v52, v53\n" \
    "v_exp_f32 v56, v56\n" \
    "v_exp_f32 v57, v57\n" \
    "v_cvt_pk_bf16_f32 v75, v54, v55\n" \
    "v_mfma_f32_16x16x32_bf16 v[100:103], v[96:99], v[64:67], v[100:103]\n" \
    "v_exp_f32 v58, v58\n" \
    "v_exp_f32 v59, v59\n" \
    "s_waitcnt lgkmcnt(0)\n" \
    "v_mfma_f32_32x32x16_bf16 v[0:15], v[120:123], v[68:71], v[0:15]\n" \
    "v_cvt_pk_bf16_f32 v76, v56, v57\n" \
    "v_exp_f32 v60, v60\n" \
    "v_exp_f32 v61, v61\n" \
    "v_cvt_pk_bf16_f32 v77, v58, v59\n" \
    "v_exp_f32 v62, v62\n" \
    "v_mfma_f32_16x16x32_bf16 v[100:103], v[96:99], v[68:71], v[100:103]\n" \
    "v_exp_f32 v63, v63\n" \
    "v_cvt_pk_bf16_f32 v78, v60, v61\n" \
    "v_cvt_pk_bf16_f32 v79, v62, v63\n" \
    "s_barrier\n" \
    "v_mfma_f32_32x32x16_bf16 v[48:63], v[108:111], v[88:91], 0\n" \
    "v_exp_f32 v32, v32\n" \
    "ds_read_b128 v[108:111], v124 offset:32768\n" \
    "v_exp_f32 v33, v33\n" \
    "v_exp_f32 v34, v34\n" \
    "v_exp_f32 v35, v35\n" \
    "v_mfma_f32_32x32x16_bf16 v[48:63], v[112:115], v[92:95], v[48:63]\n" \
    "v_cvt_pk_bf16_f32 v64, v32, v33\n" \
    "ds_read_b128 v[112:115], v124 offset:33792\n" \
    "v_cvt_pk_bf16_f32 v65, v34, v35\n" \
    "v_exp_f32 v36, v36\n" \
    "v_exp_f32 v37, v37\n" \
    "v_exp_f32 v38, v38\n" \
    "v_mfma_f32_32x32x16_bf16 v[16:31], v[116:119], v[72:75], v[16:31]\n" \
    "v_exp_f32 v39, v39\n" \
    "ds_read_b128 v[116:119], v124 offset:34816\n" \
    "v_cvt_pk_bf16_f32 v66, v36, v37\n" \
    "v_exp_f32 v40, v40\n" \
    "v_exp_f32 v41, v41\n" \
    "v_cvt_pk_bf16_f32 v67, v38, v39\n" \
    "v_mfma_f32_16x16x32_bf16 v[104:107], v[96:99], v[72:75], v[104:107]\n" \
    "v_exp_f32 v42, v42\n" \
    "v_exp_f32 v43, v43\n" \
    "v_mfma_f32_32x32x16_bf16 v[16:31], v[120:123], v[76:79], v[16:31]\n" \
    "v_cvt_pk_bf16_f32 v68, v40, v41\n" \
    "ds_read_b128 v[120:123], v124 offset:35840\n" \
    "v_exp_f32 v44, v44\n" \
    "v_exp_f32 v45, v45\n" \
    "v_cvt_pk_bf16_f32 v69, v42, v43\n" \
    "v_exp_f32 v46, v46\n" \
    "v_mfma_f32_16x16x32_bf16 v[104:107], v[96:99], v[76:79], v[104:107]\n" \
    "v_exp_f32 v47, v47\n" \
    "v_cvt_pk_bf16_f32 v70, v44, v45\n" \
    "v_cvt_pk_bf16_f32 v71, v46, v47\n" \
    "s_sub_u32 %[cnt], %[cnt], 1\n" \
    "s_cmp_lg_u32 %[cnt], 0\n" \
    "s_cbranch_scc1 1b\n" \
    "s_waitcnt lgkmcnt(0)\n" \
    "s_nop 15\n"
#define ASM_MIX1 \
    "s_mov_b32 %[cnt], 16\n" \
    "1:\n" \
    "s_waitcnt lgkmcnt(0)\n" \
    "s_barrier\n" \
    "v_mfma_f32_32x32x16_bf16 v[0:15], v[80:83], v[88:91], v[0:15]\n" \
    "ds_read_b64_tr_b16 v[64:65], v96 offset:0\n" \
    "ds_read_b64_tr_b16 v[66:67], v96 offset:1024\n" \
    "v_mfma_f32_32x32x16_bf16 v[16:31], v[80:83], v[92:95], v[16:31]\n" \
    "ds_read_b128 v[72:75], v98 offset:0\n" \
    "ds_read_b128 v[76:79], v98 offset:512\n" \
    "v_mfma_f32_32x32x16_bf16 v[32:47], v[84:87], v[88:91], v[32:47]\n" \
    "ds_read_b64_tr_b16 v[68:69], v97 offset:0\n" \
    "ds_read_b64_tr_b16 v[70:71], v97 offset:1024\n" \
    "v_mfma_f32_32x32x16_bf16 v[48:63], v[84:87], v[92:95], v[48:63]\n" \
    "s_waitcnt lgkmcnt(0)\n" \
    "v_mfma_f32_32x32x16_bf16 v[0:15], v[64:67], v[72:75], v[0:15]\n" \
    "ds_read_b64_tr_b16 v[80:81], v96 offset:4096\n" \
    "ds_read_b64_tr_b16 v[82:83], v96 offset:5120\n" \
    "v_mfma_f32_32x32x16_bf16 v[16:31], v[64:67], v[76:79], v[16:31]\n" \
    "ds_read_b128 v[88:91], v98 offset:4096\n" \
    "ds_read_b128 v[92:95], v98 offset:4608\n" \
    "v_mfma_f32_32x32x16_bf16 v[32:47], v[68:71], v[72:75], v[32:47]\n" \
    "ds_read_b64_tr_b16 v[84:85], v97 offset:4096\n" \
    "ds_read_b64_tr_b16 v[86:87], v97 offset:5120\n" \
    "v_mfma_f32_32x32x16_bf16 v[48:63], v[68:71], v[76:79], v[48:63]\n" \
    "s_waitcnt lgkmcnt(0)\n" \
    "s_barrier\n" \
    "v_mfma_f32_32x32x16_bf16 v[0:15], v[80:83], v[88:91], v[0:15]\n" \
    "ds_read_b64_tr_b16 v[64:65], v96 offset:16384\n" \
    "ds_read_b64_tr_b16 v[66:67], v96 offset:17408\n" \
    "v_mfma_f32_32x32x16_bf16 v[16:31], v[80:83], v[92:95], v[16:31]\n" \
    "ds_read_b128 v[72:75], v98 offset:16384\n" \
    "ds_read_b128 v[76:79], v98 offset:16896\n" \
    "v_mfma_f32_32x32x16_bf16 v[32:47], v[84:87], v[88:91], v[32:47]\n" \
    "ds_read_b64_tr_b16 v[68:69], v97 offset:16384\n" \
    "ds_read_b64_tr_b16 v[70:71], v97 offset:17408\n" \
    "v_mfma_f32_32x32x16_bf16 v[48:63], v[84:87], v[92:95], v[48:63]\n" \
    "s_waitcnt lgkmcnt(0)\n" \
    "v_mfma_f32_32x32x16_bf16 v[0:15], v[64:67], v[72:75], v[0:15]\n" \
    "ds_read_b64_tr_b16 v[80:81], v96 offset:20480\n" \
    "ds_read_b64_tr_b16 v[82:83], v96 offset:21504\n" \
    "v_mfma_f32_32x32x16_bf16 v[16:31], v[64:67], v[76:79], v[16:31]\n" \
    "ds_read_b128 v[88:91], v98 offset:20480\n" \
    "ds_read_b128 v[92:95], v98 offset:20992\n" \
    "v_mfma_f32_32x32x16_bf16 v[32:47], v[68:71], v[72:75], v[32:47]\n" \
    "ds_read_b64_tr_b16 v[84:85], v97 offset:20480\n" \
    "ds_read_b64_tr_b16 v[86:87], v97 offset:21504\n" \
    "v_mfma_f32_32x32x16_bf16 v[48:63], v[68:71], v[76:79], v[48:63]\n" \
    "s_waitcnt lgkmcnt(0)\n" \
    "s_barrier\n" \
    "v_mfma_f32_32x32x16_bf16 v[0:15], v[80:83], v[88:91], v[0:15]\n" \
    "ds_read_b64_tr_b16 v[64:65], v96 offset:32768\n" \
    "ds_read_b64_tr_b16 v[66:67], v96 offset:33792\n" \
    "v_mfma_f32_32x32x16_bf16 v[16:31], v[80:83], v[92:95], v[16:31]\n" \
    "ds_read_b128 v[72:75], v98 offset:32768\n" \
    "ds_read_b128 v[76:79], v98 offset:33280\n" \
    "v_mfma_f32_32x32x16_bf16 v[32:47], v[84:87], v[88:91], v[32:47]\n" \
    "ds_read_b64_tr_b16 v[68:69], v97 offset:32768\n" \
    "ds_read_b64_tr_b16 v[70:71], v97 offset:33792\n" \
    "v_mfma_f32_32x32x16_bf16 v[48:63], v[84:87], v[92:95], v[48:63]\n" \
    "s_waitcnt lgkmcnt(0)\n" \
    "v_mfma_f32_32x32x16_bf16 v[0:15], v[64:67], v[72:75], v[0:15]\n" \
    "ds_read_b64_tr_b16 v[80:81], v96 offset:36864\n" \
    "ds_read_b64_tr_b16 v[82:83], v96 offset:37888\n" \
    "v_mfma_f32_32x32x16_bf16 v[16:31], v[64:67], v[76:79], v[16:31]\n" \
    "ds_read_b128 v[88:91], v98 offset:36864\n" \
    "ds_read_b128 v[92:95], v98 offset:37376\n" \
    "v_mfma_f32_32x32x16_bf16 v[32:47], v[68:71], v[72:75], v[32:47]\n" \
    "ds_read_b64_tr_b16 v[84:85], v97 offset:36864\n" \
    "ds_read_b64_tr_b16 v[86:87], v97 offset:37888\n" \
    "v_mfma_f32_32x32x16_bf16 v[48:63], v[68:71], v[76:79], v[48:63]\n" \
    "s_waitcnt lgkmcnt(0)\n" \
    "s_barrier\n" \
    "v_mfma_f32_32x32x16_bf16 v[0:15], v[80:83], v[88:91], v[0:15]\n" \
    "ds_read_b64_tr_b16 v[64:65], v96 offset:49152\n" \
    "ds_read_b64_tr_b16 v[66:67], v96 offset:50176\n" \
    "v_mfma_f32_32x32x16_bf16 v[16:31], v[80:83], v[92:95], v[16:31]\n" \
    "ds_read_b128 v[72:75], v98 offset:49152\n" \
    "ds_read_b128 v[76:79], v98 offset:49664\n" \
    "v_mfma_f32_32x32x16_bf16 v[32:47], v[84:87], v[88:91], v[32:47]\n" \
    "ds_read_b64_tr_b16 v[68:69], v97 offset:49152\n" \
    "ds_read_b64_tr_b16 v[70:71], v97 offset:50176\n" \
    "v_mfma_f32_32x32x16_bf16 v[48:63], v[84:87], v[92:95], v[48:63]\n" \
    "s_waitcnt lgkmcnt(0)\n" \
    "v_mfma_f32_32x32x16_bf16 v[0:15], v[64:67], v[72:75], v[0:15]\n" \
    "ds_read_b64_tr_b16 v[80:81], v96 offset:53248\n" \
    "ds_read_b64_tr_b16 v[82:83], v96 offset:54272\n" \
    "v_mfma_f32_32x32x16_bf16 v[16:31], v[64:67], v[76:79], v[16:31]\n" \
    "ds_read_b128 v[88:91], v98 offset:53248\n" \
    "ds_read_b128 v[92:95], v98 offset:53760\n" \
    "v_mfma_f32_32x32x16_bf16 v[32:47], v[68:71], v[72:75], v[32:47]\n" \
    "ds_read_b64_tr_b16 v[84:85], v97 offset:53248\n" \
    "ds_read_b64_tr_b16 v[86:87], v97 offset:54272\n" \
    "v_mfma_f32_32x32x16_bf16 v[48:63], v[68:71], v[76:79], v[48:63]\n" \
    "s_sub_u32 %[cnt], %[cnt], 1\n" \
    "s_cmp_lg_u32 %[cnt], 0\n" \
    "s_cbranch_scc1 1b\n" \
    "s_waitcnt lgkmcnt(0)\n" \
    "v_mfma_f32_32x32x16_bf16 v[0:15], v[80:83], v[88:91], v[0:15]\n" \
    "v_mfma_f32_32x32x16_bf16 v[16:31], v[80:83], v[92:95], v[16:31]\n" \
    "v_mfma_f32_32x32x16_bf16 v[32:47], v[84:87], v[88:91], v[32:47]\n" \
    "v_mfma_f32_32x32x16_bf16 v[48:63], v[84:87], v[92:95], v[48:63]\n" \
    "s_nop 15\n"
#define ASM_MIX2 \
    "s_mov_b32 %[cnt], 14\n" \
    "1:\n" \
    "s_waitcnt lgkmcnt(0)\n" \
    "s_barrier\n" \
    "v_mfma_f32_32x32x16_bf16 v[0:15], v[88:91], v[80:83], v[0:15]\n" \
    "ds_read_b64_tr_b16 v[64:65], v96 offset:0\n" \
    "ds_read_b64_tr_b16 v[66:67], v96 offset:2048\n" \
    "v_mfma_f32_32x32x16_bf16 v[32:47], v[92:95], v[80:83], v[32:47]\n" \
    "ds_read_b128 v[72:75], v98 offset:0\n" \
    "ds_read_b128 v[76:79], v98 offset:512\n" \
    "v_mfma_f32_32x32x16_bf16 v[16:31], v[88:91], v[84:87], v[16:31]\n" \
    "ds_read_b64_tr_b16 v[68:69], v97 offset:0\n" \
    "ds_read_b64_tr_b16 v[70:71], v97 offset:2048\n" \
    "v_mfma_f32_32x32x16_bf16 v[48:63], v[92:95], v[84:87], v[48:63]\n" \
    "s_waitcnt lgkmcnt(0)\n" \
    "v_mfma_f32_32x32x16_bf16 v[0:15], v[72:75], v[64:67], v[0:15]\n" \
    "ds_read_b64_tr_b16 v[80:81], v96 offset:4096\n" \
    "ds_read_b64_tr_b16 v[82:83], v96 offset:6144\n" \
    "v_mfma_f32_32x32x16_bf16 v[32:47], v[76:79], v[64:67], v[32:47]\n" \
    "ds_read_b128 v[88:91], v98 offset:4096\n" \
    "ds_read_b128 v[92:95], v98 offset:4608\n" \
    "v_mfma_f32_32x32x16_bf16 v[16:31], v[72:75], v[68:71], v[16:31]\n" \
    "ds_read_b64_tr_b16 v[84:85], v97 offset:4096\n" \
    "ds_read_b64_tr_b16 v[86:87], v97 offset:6144\n" \
    "v_mfma_f32_32x32x16_bf16 v[48:63], v[76:79], v[68:71], v[48:63]\n" \
    "s_waitcnt lgkmcnt(0)\n" \
    "s_barrier\n" \
    "v_mfma_f32_32x32x16_bf16 v[0:15], v[88:91], v[80:83], v[0:15]\n" \
    "ds_read_b64_tr_b16 v[64:65], v96 offset:16384\n" \
    "ds_read_b64_tr_b16 v[66:67], v96 offset:18432\n" \
    "v_mfma_f32_32x32x16_bf16 v[32:47], v[92:95], v[80:83], v[32:47]\n" \
    "ds_read_b128 v[72:75], v98 offset:16384\n" \
    "ds_read_b128 v[76:79], v98 offset:16896\n" \
    "v_mfma_f32_32x32x16_bf16 v[16:31], v[88:91], v[84:87], v[16:31]\n" \
    "ds_read_b64_tr_b16 v[68:69], v97 offset:16384\n" \
    "ds_read_b64_tr_b16 v[70:71], v97 offset:18432\n" \
    "v_mfma_f32_32x32x16_bf16 v[48:63], v[92:95], v[84:87], v[48:63]\n" \
    "s_waitcnt lgkmcnt(0)\n" \
    "v_mfma_f32_32x32x16_bf16 v[0:15], v[72:75], v[64:67], v[0:15]\n" \
    "ds_read_b64_tr_b16 v[80:81], v96 offset:20480\n" \
    "ds_read_b64_tr_b16 v[82:83], v96 offset:22528\n" \
    "v_mfma_f32_32x32x16_bf16 v[32:47], v[76:79], v[64:67], v[32:47]\n" \
    "ds_read_b128 v[88:91], v98 offset:20480\n" \
    "ds_read_b128 v[92:95], v98 offset:20992\n" \
    "v_mfma_f32_32x32x16_bf16 v[16:31], v[72:75], v[68:71], v[16:31]\n" \
    "ds_read_b64_tr_b16 v[84:85], v97 offset:20480\n" \
    "ds_read_b64_tr_b16 v[86:87], v97 offset:22528\n" \
    "v_mfma_f32_32x32x16_bf16 v[48:63], v[76:79], v[68:71], v[48:63]\n" \
    "s_waitcnt lgkmcnt(0)\n" \
    "s_barrier\n" \
    "v_mfma_f32_32x32x16_bf16 v[0:15], v[88:91], v[80:83], v[0:15]\n" \
    "ds_read_b64_tr_b16 v[64:65], v96 offset:32768\n" \
    "ds_read_b64_tr_b16 v[66:67], v96 offset:34816\n" \
    "v_mfma_f32_32x32x16_bf16 v[32:47], v[92:95], v[80:83], v[32:47]\n" \
    "ds_read_b128 v[72:75], v98 offset:32768\n" \
    "ds_read_b128 v[76:79], v98 offset:33280\n" \
    "v_mfma_f32_32x32x16_bf16 v[16:31], v[88:91], v[84:87], v[16:31]\n" \
    "ds_read_b64_tr_b16 v[68:69], v97 offset:32768\n" \
    "ds_read_b64_tr_b16 v[70:71], v97 offset:34816\n" \
    "v_mfma_f32_32x32x16_bf16 v[48:63], v[92:95], v[84:87], v[48:63]\n" \
    "s_waitcnt lgkmcnt(0)\n" \
    "v_mfma_f32_32x32x16_bf16 v[0:15], v[72:75], v[64:67], v[0:15]\n" \
    "ds_read_b64_tr_b16 v[80:81], v96 offset:36864\n" \
    "ds_read_b64_tr_b16 v[82:83], v96 offset:38912\n" \
    "v_mfma_f32_32x32x16_bf16 v[32:47], v[76:79], v[64:67], v[32:47]\n" \
    "ds_read_b128 v[88:91], v98 offset:36864\n" \
    "ds_read_b128 v[92:95], v98 offset:37376\n" \
    "v_mfma_f32_32x32x16_bf16 v[16:31], v[72:75], v[68:71], v[16:31]\n" \
    "ds_read_b64_tr_b16 v[84:85], v97 offset:36864\n" \
    "ds_read_b64_tr_b16 v[86:87], v97 offset:38912\n" \
    "v_mfma_f32_32x32x16_bf16 v[48:63], v[76:79], v[68:71], v[48:63]\n" \
    "s_waitcnt lgkmcnt(0)\n" \
    "s_barrier\n" \
    "v_mfma_f32_32x32x16_bf16 v[0:15], v[88:91], v[80:83], v[0:15]\n" \
    "ds_read_b64_tr_b16 v[64:65], v96 offset:49152\n" \
    "ds_read_b64_tr_b16 v[66:67], v96 offset:51200\n" \
    "v_mfma_f32_32x32x16_bf16 v[32:47], v[92:95], v[80:83], v[32:47]\n" \
    "ds_read_b128 v[72:75], v98 offset:49152\n" \
    "ds_read_b128 v[76:79], v98 offset:49664\n" \
    "v_mfma_f32_32x32x16_bf16 v[16:31], v[88:91], v[84:87], v[16:31]\n" \
    "ds_read_b64_tr_b16 v[68:69], v97 offset:49152\n" \
    "ds_read_b64_tr_b16 v[70:71], v97 offset:51200\n" \
    "v_mfma_f32_32x32x16_bf16 v[48:63], v[92:95], v[84:87], v[48:63]\n" \
    "s_waitcnt lgkmcnt(0)\n" \
    "v_mfma_f32_32x32x16_bf16 v[0:15], v[72:75], v[64:67], v[0:15]\n" \
    "ds_read_b64_tr_b16 v[80:81], v96 offset:53248\n" \
    "ds_read_b64_tr_b16 v[82:83], v96 offset:55296\n" \
    "v_mfma_f32_32x32x16_bf16 v[32:47], v[76:79], v[64:67], v[32:47]\n" \
    "ds_read_b128 v[88:91], v98 offset:53248\n" \
    "ds_read_b128 v[92:95], v98 offset:53760\n" \
    "v_mfma_f32_32x32x16_bf16 v[16:31], v[72:75], v[68:71], v[16:31]\n" \
    "ds_read_b64_tr_b16 v[84:85], v97 offset:53248\n" \
    "ds_read_b64_tr_b16 v[86:87], v97 offset:55296\n" \
    "v_mfma_f32_32x32x16_bf16 v[48:63], v[76:79], v[68:71], v[48:63]\n" \
    "s_sub_u32 %[cnt], %[cnt], 1\n" \
    "s_cmp_lg_u32 %[cnt], 0\n" \
    "s_cbranch_scc1 1b\n" \
    "s_waitcnt lgkmcnt(0)\n" \
    "v_mfma_f32_32x32x16_bf16 v[0:15], v[88:91], v[80:83], v[0:15]\n" \
    "v_mfma_f32_32x32x16_bf16 v[32:47], v[92:95], v[80:83], v[32:47]\n" \
    "v_mfma_f32_32x32x16_bf16 v[16:31], v[88:91], v[84:87], v[16:31]\n" \
    "v_mfma_f32_32x32x16_bf16 v[48:63], v[92:95], v[84:87], v[48:63]\n" \
    "s_nop 15\n"
#define ASM_QPROJ \
    "s_mov_b32 %[cnt], 5\n" \
    "1:\n" \
    "s_waitcnt lgkmcnt(0)\n" \
    "s_barrier\n" \
    "v_mfma_f32_32x32x16_bf16 v[0:15], v[56:59], v[60:63], v[0:15]\n" \
    "ds_read_b64_tr_b16 v[32:33], v80 offset:0\n" \
    "ds_read_b64_tr_b16 v[34:35], v80 offset:512\n" \
    "ds_read_b128 v[36:39], v81 offset:0\n" \
    "v_mfma_f32_32x32x16_bf16 v[16:31], v[56:59], v[64:67], v[16:31]\n" \
    "ds_read_b128 v[40:43], v81 offset:4096\n" \
    "ds_read_b64_tr_b16 v[44:45], v80 offset:2048\n" \
    "ds_read_b64_tr_b16 v[46:47], v80 offset:2560\n" \
    "v_mfma_f32_32x32x16_bf16 v[0:15], v[68:71], v[72:75], v[0:15]\n" \
    "ds_read_b128 v[48:51], v82 offset:0\n" \
    "ds_read_b128 v[52:55], v82 offset:4096\n" \
    "v_mfma_f32_32x32x16_bf16 v[16:31], v[68:71], v[76:79], v[16:31]\n" \
    "s_waitcnt lgkmcnt(0)\n" \
    "v_mfma_f32_32x32x16_bf16 v[0:15], v[32:35], v[36:39], v[0:15]\n" \
    "ds_read_b64_tr_b16 v[56:57], v80 offset:4096\n" \
    "ds_read_b64_tr_b16 v[58:59], v80 offset:4608\n" \
    "ds_read_b128 v[60:63], v83 offset:0\n" \
    "v_mfma_f32_32x32x16_bf16 v[16:31], v[32:35], v[40:43], v[16:31]\n" \
    "ds_read_b128 v[64:67], v83 offset:4096\n" \
    "ds_read_b64_tr_b16 v[68:69], v80 offset:6144\n" \
    "ds_read_b64_tr_b16 v[70:71], v80 offset:6656\n" \
    "v_mfma_f32_32x32x16_bf16 v[0:15], v[44:47], v[48:51], v[0:15]\n" \
    "ds_read_b128 v[72:75], v84 offset:0\n" \
    "ds_read_b128 v[76:79], v84 offset:4096\n" \
    "v_mfma_f32_32x32x16_bf16 v[16:31], v[44:47], v[52:55], v[16:31]\n" \
    "s_waitcnt lgkmcnt(0)\n" \
    "s_barrier\n" \
    "v_mfma_f32_32x32x16_bf16 v[0:15], v[56:59], v[60:63], v[0:15]\n" \
    "ds_read_b64_tr_b16 v[32:33], v80 offset:24576\n" \
    "ds_read_b64_tr_b16 v[34:35], v80 offset:25088\n" \
    "ds_read_b128 v[36:39], v81 offset:24576\n" \
    "v_mfma_f32_32x32x16_bf16 v[16:31], v[56:59], v[64:67], v[16:31]\n" \
    "ds_read_b128 v[40:43], v81 offset:28672\n" \
    "ds_read_b64_tr_b16 v[44:45], v80 offset:26624\n" \
    "ds_read_b64_tr_b16 v[46:47], v80 offset:27136\n" \
    "v_mfma_f32_32x32x16_bf16 v[0:15], v[68:71], v[72:75], v[0:15]\n" \
    "ds_read_b128 v[48:51], v82 offset:24576\n" \
    "ds_read_b128 v[52:55], v82 offset:28672\n" \
    "v_mfma_f32_32x32x16_bf16 v[16:31], v[68:71], v[76:79], v[16:31]\n" \
    "s_waitcnt lgkmcnt(0)\n" \
    "v_mfma_f32_32x32x16_bf16 v[0:15], v[32:35], v[36:39], v[0:15]\n" \
    "ds_read_b64_tr_b16 v[56:57], v80 offset:28672\n" \
    "ds_read_b64_tr_b16 v[58:59], v80 offset:29184\n" \
    "ds_read_b128 v[60:63], v83 offset:24576\n" \
    "v_mfma_f32_32x32x16_bf16 v[16:31], v[32:35], v[40:43], v[16:31]\n" \
    "ds_read_b128 v[64:67], v83 offset:28672\n" \
    "ds_read_b64_tr_b16 v[68:69], v80 offset:30720\n" \
    "ds_read_b64_tr_b16 v[70:71], v80 offset:31232\n" \
    "v_mfma_f32_32x32x16_bf16 v[0:15], v[44:47], v[48:51], v[0:15]\n" \
    "ds_read_b128 v[72:75], v84 offset:24576\n" \
    "ds_read_b128 v[76:79], v84 offset:28672\n" \
    "v_mfma_f32_32x32x16_bf16 v[16:31], v[44:47], v[52:55], v[16:31]\n" \
    "s_waitcnt lgkmcnt(0)\n" \
    "s_barrier\n" \
    "v_mfma_f32_32x32x16_bf16 v[0:15], v[56:59], v[60:63], v[0:15]\n" \
    "ds_read_b64_tr_b16 v[32:33], v80 offset:49152\n" \
    "ds_read_b64_tr_b16 v[34:35], v80 offset:49664\n" \
    "ds_read_b128 v[36:39], v81 offset:49152\n" \
    "v_mfma_f32_32x32x16_bf16 v[16:31], v[56:59], v[64:67], v[16:31]\n" \
    "ds_read_b128 v[40:43], v81 offset:53248\n" \
    "ds_read_b64_tr_b16 v[44:45], v80 offset:51200\n" \
    "ds_read_b64_tr_b16 v[46:47], v80 offset:51712\n" \
    "v_mfma_f32_32x32x16_bf16 v[0:15], v[68:71], v[72:75], v[0:15]\n" \
    "ds_read_b128 v[48:51], v82 offset:49152\n" \
    "ds_read_b128 v[52:55], v82 offset:53248\n" \
    "v_mfma_f32_32x32x16_bf16 v[16:31], v[68:71], v[76:79], v[16:31]\n" \
    "s_waitcnt lgkmcnt(0)\n" \
    "v_mfma_f32_32x32x16_bf16 v[0:15], v[32:35], v[36:39], v[0:15]\n" \
    "ds_read_b64_tr_b16 v[56:57], v80 offset:53248\n" \
    "ds_read_b64_tr_b16 v[58:59], v80 offset:53760\n" \
    "ds_read_b128 v[60:63], v83 offset:49152\n" \
    "v_mfma_f32_32x32x16_bf16 v[16:31], v[32:35], v[40:43], v[16:31]\n" \
    "ds_read_b128 v[64:67], v83 offset:53248\n" \
    "ds_read_b64_tr_b16 v[68:69], v80 offset:55296\n" \
    "ds_read_b64_tr_b16 v[70:71], v80 offset:55808\n" \
    "v_mfma_f32_32x32x16_bf16 v[0:15], v[44:47], v[48:51], v[0:15]\n" \
    "ds_read_b128 v[72:75], v84 offset:49152\n" \
    "ds_read_b128 v[76:79], v84 offset:53248\n" \
    "v_mfma_f32_32x32x16_bf16 v[16:31], v[44:47], v[52:55], v[16:31]\n" \
    "s_sub_u32 %[cnt], %[cnt], 1\n" \
    "s_cmp_lg_u32 %[cnt], 0\n" \
    "s_cbranch_scc1 1b\n" \
    "s_waitcnt lgkmcnt(0)\n" \
    "s_barrier\n" \
    "v_mfma_f32_32x32x16_bf16 v[0:15], v[56:59], v[60:63], v[0:15]\n" \
    "ds_read_b64_tr_b16 v[32:33], v80 offset:0\n" \
    "ds_read_b64_tr_b16 v[34:35], v80 offset:512\n" \
    "ds_read_b128 v[36:39], v81 offset:0\n" \
    "v_mfma_f32_32x32x16_bf16 v[16:31], v[56:59], v[64:67], v[16:31]\n" \
    "ds_read_b128 v[40:43], v81 offset:4096\n" \
    "ds_read_b64_tr_b16 v[44:45], v80 offset:2048\n" \
    "ds_read_b64_tr_b16 v[46:47], v80 offset:2560\n" \
    "v_mfma_f32_32x32x16_bf16 v[0:15], v[68:71], v[72:75], v[0:15]\n" \
    "ds_read_b128 v[48:51], v82 offset:0\n" \
    "ds_read_b128 v[52:55], v82 offset:4096\n" \
    "v_mfma_f32_32x32x16_bf16 v[16:31], v[68:71], v[76:79], v[16:31]\n" \
    "s_waitcnt lgkmcnt(0)\n" \
    "v_mfma_f32_32x32x16_bf16 v[0:15], v[32:35], v[36:39], v[0:15]\n" \
    "ds_read_b64_tr_b16 v[56:57], v80 offset:4096\n" \
    "ds_read_b64_tr_b16 v[58:59], v80 offset:4608\n" \
    "ds_read_b128 v[60:63], v83 offset:0\n" \
    "v_mfma_f32_32x32x16_bf16 v[16:31], v[32:35], v[40:43], v[16:31]\n" \
    "ds_read_b128 v[64:67], v83 offset:4096\n" \
    "ds_read_b64_tr_b16 v[68:69], v80 offset:6144\n" \
    "ds_read_b64_tr_b16 v[70:71], v80 offset:6656\n" \
    "v_mfma_f32_32x32x16_bf16 v[0:15], v[44:47], v[48:51], v[0:15]\n" \
    "ds_read_b128 v[72:75], v84 offset:0\n" \
    "ds_read_b128 v[76:79], v84 offset:4096\n" \
    "v_mfma_f32_32x32x16_bf16 v[16:31], v[44:47], v[52:55], v[16:31]\n" \
    "s_waitcnt lgkmcnt(0)\n" \
    "v_mfma_f32_32x32x16_bf16 v[0:15], v[56:59], v[60:63], v[0:15]\n" \
    "v_mfma_f32_32x32x16_bf16 v[16:31], v[56:59], v[64:67], v[16:31]\n" \
    "v_mfma_f32_32x32x16_bf16 v[0:15], v[68:71], v[72:75], v[0:15]\n" \
    "v_mfma_f32_32x32x16_bf16 v[16:31], v[68:71], v[76:79], v[16:31]\n" \
    "s_nop 15\n"
#define ASM_OPROJ_A \
    "s_mov_b32 %[cnt], 4\n" \
    "1:\n" \
    "s_waitcnt lgkmcnt(0)\n" \
    "s_barrier\n" \
    "v_mfma_f32_32x32x16_bf16 v[0:15], v[56:59], v[60:63], v[0:15]\n" \
    "ds_read_b64_tr_b16 v[32:33], v80 offset:0\n" \
    "ds_read_b64_tr_b16 v[34:35], v80 offset:2048\n" \
    "ds_read_b128 v[36:39], v81 offset:0\n" \
    "v_mfma_f32_32x32x16_bf16 v[16:31], v[56:59], v[64:67], v[16:31]\n" \
    "ds_read_b128 v[40:43], v81 offset:512\n" \
    "ds_read_b64_tr_b16 v[44:45], v80 offset:4096\n" \
    "ds_read_b64_tr_b16 v[46:47], v80 offset:6144\n" \
    "v_mfma_f32_32x32x16_bf16 v[0:15], v[68:71], v[72:75], v[0:15]\n" \
    "ds_read_b128 v[48:51], v81 offset:2048\n" \
    "ds_read_b128 v[52:55], v81 offset:2560\n" \
    "v_mfma_f32_32x32x16_bf16 v[16:31], v[68:71], v[76:79], v[16:31]\n" \
    "s_waitcnt lgkmcnt(0)\n" \
    "v_mfma_f32_32x32x16_bf16 v[0:15], v[32:35], v[36:39], v[0:15]\n" \
    "ds_read_b64_tr_b16 v[56:57], v80 offset:8192\n" \
    "ds_read_b64_tr_b16 v[58:59], v80 offset:10240\n" \
    "ds_read_b128 v[60:63], v81 offset:4096\n" \
    "v_mfma_f32_32x32x16_bf16 v[16:31], v[32:35], v[40:43], v[16:31]\n" \
    "ds_read_b128 v[64:67], v81 offset:4608\n" \
    "ds_read_b64_tr_b16 v[68:69], v80 offset:12288\n" \
    "ds_read_b64_tr_b16 v[70:71], v80 offset:14336\n" \
    "v_mfma_f32_32x32x16_bf16 v[0:15], v[44:47], v[48:51], v[0:15]\n" \
    "ds_read_b128 v[72:75], v81 offset:6144\n" \
    "ds_read_b128 v[76:79], v81 offset:6656\n" \
    "v_mfma_f32_32x32x16_bf16 v[16:31], v[44:47], v[52:55], v[16:31]\n" \
    "s_waitcnt lgkmcnt(0)\n" \
    "s_barrier\n" \
    "v_mfma_f32_32x32x16_bf16 v[0:15], v[56:59], v[60:63], v[0:15]\n" \
    "ds_read_b64_tr_b16 v[32:33], v80 offset:24576\n" \
    "ds_read_b64_tr_b16 v[34:35], v80 offset:26624\n" \
    "ds_read_b128 v[36:39], v81 offset:24576\n" \
    "v_mfma_f32_32x32x16_bf16 v[16:31], v[56:59], v[64:67], v[16:31]\n" \
    "ds_read_b128 v[40:43], v81 offset:25088\n" \
    "ds_read_b64_tr_b16 v[44:45], v80 offset:28672\n" \
    "ds_read_b64_tr_b16 v[46:47], v80 offset:30720\n" \
    "v_mfma_f32_32x32x16_bf16 v[0:15], v[68:71], v[72:75], v[0:15]\n" \
    "ds_read_b128 v[48:51], v81 offset:26624\n" \
    "ds_read_b128 v[52:55], v81 offset:27136\n" \
    "v_mfma_f32_32x32x16_bf16 v[16:31], v[68:71], v[76:79], v[16:31]\n" \
    "s_waitcnt lgkmcnt(0)\n" \
    "v_mfma_f32_32x32x16_bf16 v[0:15], v[32:35], v[36:39], v[0:15]\n" \
    "ds_read_b64_tr_b16 v[56:57], v80 offset:32768\n" \
    "ds_read_b64_tr_b16 v[58:59], v80 offset:34816\n" \
    "ds_read_b128 v[60:63], v81 offset:28672\n" \
    "v_mfma_f32_32x32x16_bf16 v[16:31], v[32:35], v[40:43], v[16:31]\n" \
    "ds_read_b128 v[64:67], v81 offset:29184\n" \
    "ds_read_b64_tr_b16 v[68:69], v80 offset:36864\n" \
    "ds_read_b64_tr_b16 v[70:71], v80 offset:38912\n" \
    "v_mfma_f32_32x32x16_bf16 v[0:15], v[44:47], v[48:51], v[0:15]\n" \
    "ds_read_b128 v[72:75], v81 offset:30720\n" \
    "ds_read_b128 v[76:79], v81 offset:31232\n" \
    "v_mfma_f32_32x32x16_bf16 v[16:31], v[44:47], v[52:55], v[16:31]\n" \
    "s_waitcnt lgkmcnt(0)\n" \
    "s_barrier\n" \
    "v_mfma_f32_32x32x16_bf16 v[0:15], v[56:59], v[60:63], v[0:15]\n" \
    "ds_read_b64_tr_b16 v[32:33], v80 offset:49152\n" \
    "ds_read_b64_tr_b16 v[34:35], v80 offset:51200\n" \
    "ds_read_b128 v[36:39], v81 offset:49152\n" \
    "v_mfma_f32_32x32x16_bf16 v[16:31], v[56:59], v[64:67], v[16:31]\n" \
    "ds_read_b128 v[40:43], v81 offset:49664\n" \
    "ds_read_b64_tr_b16 v[44:45], v80 offset:53248\n" \
    "ds_read_b64_tr_b16 v[46:47], v80 offset:55296\n" \
    "v_mfma_f32_32x32x16_bf16 v[0:15], v[68:71], v[72:75], v[0:15]\n" \
    "ds_read_b128 v[48:51], v81 offset:51200\n" \
    "ds_read_b128 v[52:55], v81 offset:51712\n" \
    "v_mfma_f32_32x32x16_bf16 v[16:31], v[68:71], v[76:79], v[16:31]\n" \
    "s_waitcnt lgkmcnt(0)\n" \
    "v_mfma_f32_32x32x16_bf16 v[0:15], v[32:35], v[36:39], v[0:15]\n" \
    "ds_read_b64_tr_b16 v[56:57], v80 offset:57344\n" \
    "ds_read_b64_tr_b16 v[58:59], v80 offset:59392\n" \
    "ds_read_b128 v[60:63], v81 offset:53248\n" \
    "v_mfma_f32_32x32x16_bf16 v[16:31], v[32:35], v[40:43], v[16:31]\n" \
    "ds_read_b128 v[64:67], v81 offset:53760\n" \
    "ds_read_b64_tr_b16 v[68:69], v80 offset:61440\n" \
    "ds_read_b64_tr_b16 v[70:71], v80 offset:63488\n" \
    "v_mfma_f32_32x32x16_bf16 v[0:15], v[44:47], v[48:51], v[0:15]\n" \
    "ds_read_b128 v[72:75], v81 offset:55296\n" \
    "ds_read_b128 v[76:79], v81 offset:55808\n" \
    "v_mfma_f32_32x32x16_bf16 v[16:31], v[44:47], v[52:55], v[16:31]\n" \
    "s_sub_u32 %[cnt], %[cnt], 1\n" \
    "s_cmp_lg_u32 %[cnt], 0\n" \
    "s_cbranch_scc1 1b\n" \
    "s_waitcnt lgkmcnt(0)\n"
#define ASM_OPROJ_B \
    "v_mfma_f32_32x32x16_bf16 v[0:15], v[56:59], v[60:63], v[0:15]\n" \
    "ds_read_b64_tr_b16 v[32:33], v80 offset:0\n" \
    "ds_read_b64_tr_b16 v[34:35], v80 offset:2048\n" \
    "ds_read_b128 v[36:39], v81 offset:0\n" \
    "v_mfma_f32_32x32x16_bf16 v[16:31], v[56:59], v[64:67], v[16:31]\n" \
    "ds_read_b128 v[40:43], v81 offset:512\n" \
    "ds_read_b64_tr_b16 v[44:45], v80 offset:4096\n" \
    "ds_read_b64_tr_b16 v[46:47], v80 offset:6144\n" \
    "v_mfma_f32_32x32x16_bf16 v[0:15], v[68:71], v[72:75], v[0:15]\n" \
    "ds_read_b128 v[48:51], v81 offset:2048\n" \
    "ds_read_b128 v[52:55], v81 offset:2560\n" \
    "v_mfma_f32_32x32x16_bf16 v[16:31], v[68:71], v[76:79], v[16:31]\n" \
    "s_waitcnt lgkmcnt(0)\n" \
    "v_mfma_f32_32x32x16_bf16 v[0:15], v[32:35], v[36:39], v[0:15]\n" \
    "ds_read_b64_tr_b16 v[56:57], v80 offset:8192\n" \
    "ds_read_b64_tr_b16 v[58:59], v80 offset:10240\n" \
    "ds_read_b128 v[60:63], v81 offset:4096\n" \
    "v_mfma_f32_32x32x16_bf16 v[16:31], v[32:35], v[40:43], v[16:31]\n" \
    "ds_read_b128 v[64:67], v81 offset:4608\n" \
    "ds_read_b64_tr_b16 v[68:69], v80 offset:12288\n" \
    "ds_read_b64_tr_b16 v[70:71], v80 offset:14336\n" \
    "v_mfma_f32_32x32x16_bf16 v[0:15], v[44:47], v[48:51], v[0:15]\n" \
    "ds_read_b128 v[72:75], v81 offset:6144\n" \
    "ds_read_b128 v[76:79], v81 offset:6656\n" \
    "v_mfma_f32_32x32x16_bf16 v[16:31], v[44:47], v[52:55], v[16:31]\n" \
    "s_waitcnt lgkmcnt(0)\n" \
    "s_barrier\n" \
    "v_mfma_f32_32x32x16_bf16 v[0:15], v[56:59], v[60:63], v[0:15]\n" \
    "ds_read_b64_tr_b16 v[32:33], v80 offset:24576\n" \
    "ds_read_b64_tr_b16 v[34:35], v80 offset:26624\n" \
    "ds_read_b128 v[36:39], v81 offset:24576\n" \
    "v_mfma_f32_32x32x16_bf16 v[16:31], v[56:59], v[64:67], v[16:31]\n" \
    "ds_read_b128 v[40:43], v81 offset:25088\n" \
    "ds_read_b64_tr_b16 v[44:45], v80 offset:28672\n" \
    "ds_read_b64_tr_b16 v[46:47], v80 offset:30720\n" \
    "v_mfma_f32_32x32x16_bf16 v[0:15], v[68:71], v[72:75], v[0:15]\n" \
    "ds_read_b128 v[48:51], v81 offset:26624\n" \
    "ds_read_b128 v[52:55], v81 offset:27136\n" \
    "v_mfma_f32_32x32x16_bf16 v[16:31], v[68:71], v[76:79], v[16:31]\n" \
    "s_waitcnt lgkmcnt(0)\n" \
    "v_mfma_f32_32x32x16_bf16 v[0:15], v[32:35], v[36:39], v[0:15]\n" \
    "ds_read_b64_tr_b16 v[56:57], v80 offset:32768\n" \
    "ds_read_b64_tr_b16 v[58:59], v80 offset:34816\n" \
    "ds_read_b128 v[60:63], v81 offset:28672\n" \
    "v_mfma_f32_32x32x16_bf16 v[16:31], v[32:35], v[40:43], v[16:31]\n" \
    "ds_read_b128 v[64:67], v81 offset:29184\n" \
    "ds_read_b64_tr_b16 v[68:69], v80 offset:36864\n" \
    "ds_read_b64_tr_b16 v[70:71], v80 offset:38912\n" \
    "v_mfma_f32_32x32x16_bf16 v[0:15], v[44:47], v[48:51], v[0:15]\n" \
    "ds_read_b128 v[72:75], v81 offset:30720\n" \
    "ds_read_b128 v[76:79], v81 offset:31232\n" \
    "v_mfma_f32_32x32x16_bf16 v[16:31], v[44:47], v[52:55], v[16:31]\n" \
    "s_waitcnt lgkmcnt(0)\n" \
    "s_barrier\n" \
    "v_mfma_f32_32x32x16_bf16 v[0:15], v[56:59], v[60:63], v[0:15]\n" \
    "ds_read_b64_tr_b16 v[32:33], v80 offset:49152\n" \
    "ds_read_b64_tr_b16 v[34:35], v80 offset:51200\n" \
    "ds_read_b128 v[36:39], v81 offset:49152\n" \
    "v_mfma_f32_32x32x16_bf16 v[16:31], v[56:59], v[64:67], v[16:31]\n" \
    "ds_read_b128 v[40:43], v81 offset:49664\n" \
    "ds_read_b64_tr_b16 v[44:45], v80 offset:53248\n" \
    "ds_read_b64_tr_b16 v[46:47], v80 offset:55296\n" \
    "v_mfma_f32_32x32x16_bf16 v[0:15], v[68:71], v[72:75], v[0:15]\n" \
    "ds_read_b128 v[48:51], v81 offset:51200\n" \
    "ds_read_b128 v[52:55], v81 offset:51712\n" \
    "v_mfma_f32_32x32x16_bf16 v[16:31], v[68:71], v[76:79], v[16:31]\n" \
    "s_waitcnt lgkmcnt(0)\n" \
    "v_mfma_f32_32x32x16_bf16 v[0:15], v[32:35], v[36:39], v[0:15]\n" \
    "ds_read_b64_tr_b16 v[56:57], v80 offset:57344\n" \
    "ds_read_b64_tr_b16 v[58:59], v80 offset:59392\n" \
    "ds_read_b128 v[60:63], v81 offset:53248\n" \
    "v_mfma_f32_32x32x16_bf16 v[16:31], v[32:35], v[40:43], v[16:31]\n" \
    "ds_read_b128 v[64:67], v81 offset:53760\n" \
    "ds_read_b64_tr_b16 v[68:69], v80 offset:61440\n" \
    "ds_read_b64_tr_b16 v[70:71], v80 offset:63488\n" \
    "v_mfma_f32_32x32x16_bf16 v[0:15], v[44:47], v[48:51], v[0:15]\n" \
    "ds_read_b128 v[72:75], v81 offset:55296\n" \
    "ds_read_b128 v[76:79], v81 offset:55808\n" \
    "v_mfma_f32_32x32x16_bf16 v[16:31], v[44:47], v[52:55], v[16:31]\n" \
    "s_waitcnt lgkmcnt(0)\n" \
    "s_barrier\n" \
    "v_mfma_f32_32x32x16_bf16 v[0:15], v[56:59], v[60:63], v[0:15]\n" \
    "ds_read_b64_tr_b16 v[32:33], v80 offset:0\n" \
    "ds_read_b64_tr_b16 v[34:35], v80 offset:2048\n" \
    "ds_read_b128 v[36:39], v81 offset:0\n" \
    "v_mfma_f32_32x32x16_bf16 v[16:31], v[56:59], v[64:67], v[16:31]\n" \
    "ds_read_b128 v[40:43], v81 offset:512\n" \
    "ds_read_b64_tr_b16 v[44:45], v80 offset:4096\n" \
    "ds_read_b64_tr_b16 v[46:47], v80 offset:6144\n" \
    "v_mfma_f32_32x32x16_bf16 v[0:15], v[68:71], v[72:75], v[0:15]\n" \
    "ds_read_b128 v[48:51], v81 offset:2048\n" \
    "ds_read_b128 v[52:55], v81 offset:2560\n" \
    "v_mfma_f32_32x32x16_bf16 v[16:31], v[68:71], v[76:79], v[16:31]\n" \
    "s_waitcnt lgkmcnt(0)\n" \
    "v_mfma_f32_32x32x16_bf16 v[0:15], v[32:35], v[36:39], v[0:15]\n" \
    "ds_read_b64_tr_b16 v[56:57], v80 offset:8192\n" \
    "ds_read_b64_tr_b16 v[58:59], v80 offset:10240\n" \
    "ds_read_b128 v[60:63], v81 offset:4096\n" \
    "v_mfma_f32_32x32x16_bf16 v[16:31], v[32:35], v[40:43], v[16:31]\n" \
    "ds_read_b128 v[64:67], v81 offset:4608\n" \
    "ds_read_b64_tr_b16 v[68:69], v80 offset:12288\n" \
    "ds_read_b64_tr_b16 v[70:71], v80 offset:14336\n" \
    "v_mfma_f32_32x32x16_bf16 v[0:15], v[44:47], v[48:51], v[0:15]\n" \
    "ds_read_b128 v[72:75], v81 offset:6144\n" \
    "ds_read_b128 v[76:79], v81 offset:6656\n" \
    "v_mfma_f32_32x32x16_bf16 v[16:31], v[44:47], v[52:55], v[16:31]\n" \
    "s_waitcnt lgkmcnt(0)\n" \
    "v_mfma_f32_32x32x16_bf16 v[0:15], v[56:59], v[60:63], v[0:15]\n" \
    "v_mfma_f32_32x32x16_bf16 v[16:31], v[56:59], v[64:67], v[16:31]\n" \
    "v_mfma_f32_32x32x16_bf16 v[0:15], v[68:71], v[72:75], v[0:15]\n" \
    "v_mfma_f32_32x32x16_bf16 v[16:31], v[68:71], v[76:79], v[16:31]\n" \
    "s_nop 15\n"

struct Args {
    const char *x, *wq, *wo, *w1, *b1, *w2, *b2;
    char *ws, *out;
};

__global__ __launch_bounds__(512) void k_pre(const char* __restrict__ x, const char* __restrict__ wk, const char* __restrict__ wv,
                                              const char* __restrict__ gw, char* __restrict__ ws) {
    __shared__ __attribute__((aligned(1024))) char lds[131072];
    typedef __attribute__((address_space(3))) char* lds_ptr;
    const int tid = threadIdx.x, lane = tid & 63, wid = __builtin_amdgcn_readfirstlane(tid >> 6), b = blockIdx.x;
    if (tid == 0) *(unsigned*)(ws + b * 128) = 0u;
    if (b < 128) {
        const int T = b >> 1, h = lane >> 5, r32 = lane & 31;
        const bool isv = b & 1;
        const unsigned base = (unsigned)(uintptr_t)lds + wid * 16384;
        const unsigned xe = (lane >> 3) * 2048 + (((lane & 7) ^ (lane >> 4)) * 16), xo = xe ^ 64;
        const char* xb = x + (size_t)T * 32 * 2048 + wid * 256;
        const char* wb = (isv ? wv : wk) + wid * 8192;
#pragma unroll
        for (int i = 0; i < 4; ++i) dma_s2(lane * 16, wb + i * 2048, base + 8192 + i * 2048);
#pragma unroll
        for (int i = 0; i < 8; ++i) dma_s((i & 1) ? xo : xe, xb + (i >> 2) * 128 + (i & 3) * 8 * 2048, base + i * 1024);
        asm volatile("s_waitcnt vmcnt(0)" ::: "memory");
        lds_cptr wp = (lds_cptr)lds + wid * 16384 + 8192 + (8 * h + ((lane & 15) >> 2)) * 64 + ((lane >> 4) & 1) * 32 + (lane & 3) * 8;
        const unsigned xa = wid * 16384 + r32 * 128 + ((h ^ ((r32 >> 1) & 7)) * 16);
        f32x16 a = {};
#pragma unroll
        for (int ks = 0; ks < 8; ++ks) {
            const bf16x8 wf = __builtin_shufflevector(ldstr(wp + ks * 1024), ldstr(wp + ks * 1024 + 256), 0, 1, 2, 3, 4, 5, 6, 7);
            const bf16x8 xf = lds128((lds_cptr)lds + ((xa ^ ((ks & 3) * 32)) + (ks >> 2) * 4096));
            a = isv ? MFMA32(xf, wf, a) : MFMA32(wf, xf, a);
        }
#pragma unroll
        for (int k = 0; k < 4; ++k)
            *(__attribute__((address_space(3))) f32x4*)((lds_ptr)lds + wid * 16384 + k * 1024 + lane * 16) = (f32x4){a[4 * k], a[4 * k + 1], a[4 * k + 2], a[4 * k + 3]};
        LGKM_BAR();
        if (wid) return;
#pragma unroll
        for (int w = 1; w < 8; ++w)
#pragma unroll
            for (int k = 0; k < 4; ++k) {
                const f32x4 v = *(const __attribute__((address_space(3))) f32x4*)((lds_cptr)lds + w * 16384 + k * 1024 + lane * 16);
                a[4 * k] += v[0]; a[4 * k + 1] += v[1]; a[4 * k + 2] += v[2]; a[4 * k + 3] += v[3];
            }
        const u32x4 u0 = pack8(a, 0, 1.f), u1 = pack8(a, 1, 1.f);
        char* dst = ws + WS_KV + (isv ? T * 4096 + 2048 : ((T + 63) & 63) * 4096) + lane * 16;
        *(u32x4*)dst = u0;
        *(u32x4*)(dst + 1024) = u1;
        if (!isv) {
            float n = sumsq8(u0) + sumsq8(u1);
            n += __shfl_xor(n, 32);
            n = wave_max(n < 3.0e38f ? n : __builtin_inff());
            if (lane == 0) ((float*)(ws + WS_KN))[T] = n;
        }
        return;
    }
    const int gi = (b - 128) * 2 + (wid >> 2), w4 = wid & 3, t4 = tid & 255;
    const char* xr = x + (size_t)(gi * 8 + w4 * 2) * 2048 + lane * 16;
    const u32x4 a00 = *(const u32x4*)xr, a01 = *(const u32x4*)(xr + 1024), a10 = *(const u32x4*)(xr + 2048), a11 = *(const u32x4*)(xr + 3072);
    const u32x4 g0 = *(const u32x4*)(gw + lane * 16), g1 = *(const u32x4*)(gw + 1024 + lane * 16);
    u32x2 xt[8];
#pragma unroll
    for (int r = 0; r < 8; ++r) xt[r] = *(const u32x2*)(x + (size_t)(gi * 8 + r) * 2048 + t4 * 8);
    float v0[16], v1[16], gv[16];
#pragma unroll
    for (int i = 0; i < 4; ++i) {
        v0[2 * i] = bflo(a00[i]); v0[2 * i + 1] = bfhi(a00[i]); v0[8 + 2 * i] = bflo(a01[i]); v0[9 + 2 * i] = bfhi(a01[i]);
        v1[2 * i] = bflo(a10[i]); v1[2 * i + 1] = bfhi(a10[i]); v1[8 + 2 * i] = bflo(a11[i]); v1[9 + 2 * i] = bfhi(a11[i]);
        gv[2 * i] = bflo(g0[i]); gv[2 * i + 1] = bfhi(g0[i]); gv[8 + 2 * i] = bflo(g1[i]); gv[9 + 2 * i] = bfhi(g1[i]);
    }
    float s0 = 0, s1 = 0, d0 = 0, d1 = 0;
#pragma unroll
    for (int i = 0; i < 16; ++i) { s0 += v0[i]; s1 += v1[i]; d0 = fmaf(v0[i], gv[i], d0); d1 = fmaf(v1[i], gv[i], d1); }
    s0 = wave_sum(s0) * (1.f / DM); s1 = wave_sum(s1) * (1.f / DM);
    float q0 = 0, q1 = 0;
#pragma unroll
    for (int i = 0; i < 16; ++i) { const float e0 = v0[i] - s0, e1 = v1[i] - s1; q0 = fmaf(e0, e0, q0); q1 = fmaf(e1, e1, q1); }
    q0 = wave_sum(q0) * (1.f / DM); q1 = wave_sum(q1) * (1.f / DM); d0 = wave_sum(d0); d1 = wave_sum(d1);
    float* st = (float*)lds + (wid >> 2) * 16;
    if (lane == 0) {
        st[w4 * 4] = s0; st[w4 * 4 + 1] = 1.f / sqrtf(q0 + 1e-5f); st[w4 * 4 + 2] = s1; st[w4 * 4 + 3] = 1.f / sqrtf(q1 + 1e-5f);
        float* gp = (float*)(ws + WS_GATE) + gi * 8 + w4 * 2;
        gp[0] = 1.f / (1.f + __expf(-d0)); gp[1] = 1.f / (1.f + __expf(-d1));
    }
    LGKM_BAR();
    float hv[8][4];
#pragma unroll
    for (int r = 0; r < 8; ++r) {
        const float m = st[2 * r], rs = st[2 * r + 1];
        hv[r][0] = (bflo(xt[r][0]) - m) * rs; hv[r][1] = (bfhi(xt[r][0]) - m) * rs;
        hv[r][2] = (bflo(xt[r][1]) - m) * rs; hv[r][3] = (bfhi(xt[r][1]) - m) * rs;
    }
    char* hp = ws + WS_HT + (size_t)gi * 16384 + t4 * 64;
#pragma unroll
    for (int c = 0; c < 4; ++c)
        *(u32x4*)(hp + c * 16) = (u32x4){cvtpk(hv[0][c], hv[1][c]), cvtpk(hv[2][c], hv[3][c]), cvtpk(hv[4][c], hv[5][c]), cvtpk(hv[6][c], hv[7][c])};
}

__device__ __forceinline__ void attn_store(rsrc_t rw, const f32x16& o, float inv, int head, int s, int h) {
    const unsigned off = WS_OP + ((head * 4 + h) * SEQ + s) * 16;
    st16(rw, off, pack8(o, 0, inv));
    st16(rw, off + 2 * SEQ * 16, pack8(o, 1, inv));
}

__device__ __forceinline__ void attn_online(rsrc_t rw, bf16x8 q0, bf16x8 q1, int lane, f32x16& o, float& inv) {
    float m = -__builtin_inff(), l = 0.f;
    o = f32x16{};
    for (int t = 0; t < 64; ++t) {
        const unsigned kp = WS_KV + ((t + 63) & 63) * 4096, vp = WS_KV + t * 4096 + 2048;
        const bf16x8 k0 = BC8(__builtin_amdgcn_raw_buffer_load_b128(rw, lane * 16, kp, 0));
        const bf16x8 k1 = BC8(__builtin_amdgcn_raw_buffer_load_b128(rw, lane * 16 + 1024, kp, 0));
        const bf16x8 v0 = BC8(__builtin_amdgcn_raw_buffer_load_b128(rw, lane * 16, vp, 0));
        const bf16x8 v1 = BC8(__builtin_amdgcn_raw_buffer_load_b128(rw, lane * 16 + 1024, vp, 0));
        f32x16 s = MFMA32(k0, q0, f32x16{});
        s = MFMA32(k1, q1, s);
        float tm = s[0];
#pragma unroll
        for (int r = 1; r < 16; ++r) tm = fmaxf(tm, s[r]);
        tm = fmaxf(tm, __shfl_xor(tm, 32));
        const float mn = fmaxf(m, tm), al = __builtin_amdgcn_exp2f(m - mn);
        m = mn;
#pragma unroll
        for (int r = 0; r < 16; ++r) { s[r] = __builtin_amdgcn_exp2f(s[r] - mn); o[r] *= al; }
        const u32x4 p0 = pack8(s, 0, 1.f), p1 = pack8(s, 1, 1.f);
        float ts = 0.f;
#pragma unroll
        for (int r = 0; r < 4; ++r) ts += bflo(p0[r]) + bfhi(p0[r]) + bflo(p1[r]) + bfhi(p1[r]);
        l = l * al + ts;
        o = MFMA32(v0, BC8(p0), o);
        o = MFMA32(v1, BC8(p1), o);
    }
    l += __shfl_xor(l, 32);
    inv = 1.f / l;
}

__device__ __forceinline__ void side_a_loader(const Args& A, unsigned lds0, int lane) {
    char* ws = A.ws;
    const int xcd = blockIdx.x & 7, r = blockIdx.x >> 3;
    const unsigned lin = lane * 16;
    {
        const int rb = (xcd >> 1) * 4 + (r & 3), hp = (xcd & 1) * 8 + (r >> 2), rot = ((r & 3) + (r >> 2)) & 3;
        const unsigned xe = (lane >> 3) * 2048 + (((lane & 7) ^ (lane >> 4)) * 16), xo = xe ^ 64;
        const unsigned wo_ = (lane >> 3) * 2048 + (((((lane & 7) >> 2) ^ ((lane >> 4) & 1)) << 2) | (lane & 3)) * 16;
        const char* xb = A.x + (size_t)rb * 128 * 2048;
        const char* wb = A.wq + hp * 128;
        auto stage = [&](int j, int slot) {
            const int st = (j & ~3) | ((j + rot) & 3);
            const unsigned dst = lds0 + slot * 24576;
#pragma unroll
            for (int i = 0; i < 8; ++i) dma_s(wo_, wb + (size_t)(st * 64 + i * 8) * 2048, dst + 16384 + i * 1024);
#pragma unroll
            for (int i = 0; i < 16; ++i) dma_s((i & 1) ? xo : xe, xb + st * 128 + i * 8 * 2048, dst + i * 1024);
        };
        stage(0, 0); stage(1, 1);
        int slot = 2;
        for (int j = 0; j < 14; ++j) { WAITV_BAR(24); if (j == 0) LSTAMP(8); if (j == 4) LSTAMP(9); if (j == 8) LSTAMP(10); stage(j + 2, slot); slot = slot == 2 ? 0 : slot + 1; }
        WAITV_BAR(24); WAITV_BAR(0);
    }
    {
        const char* kv = ws + WS_KV;
        auto chunk = [&](int c, bool pace) {
            for (int i = 0; i < 16; ++i) {
                dma_s2(lin, kv + c * 32768 + i * 2048, lds0 + ((c & 1) ? 8192 : 40960) + i * 2048);
                if (pace) __builtin_amdgcn_s_sleep(2);
            }
        };
        dma_s2(lin, kv + 63 * 4096, lds0 + LDS_X);
        asm volatile("s_mov_b32 m0, %2\n\ts_nop 0\n\tglobal_load_lds_dword %0, %1" ::"v"(lane * 4), "s"(ws + WS_KN), "s"(lds0 + LDS_X + 2048) : "memory");
        chunk(0, false);
        WAITV_BAR(0);
        chunk(1, true);
        for (int c = 0; c < 8; ++c) { WAITV_BAR(0); if (c + 2 < 8) chunk(c + 2, true); }
    }
    {
        const int rb = (xcd >> 1) * 4 + (r & 3);
        const int sb = (xcd >> 1) * 8 + (r & 7), dt = (xcd & 1) * 4 + (r >> 3), rot = ((r & 7) + (r >> 3)) & 3;
        const unsigned woff = (lane >> 4) * 2048 + dt * 256 + (((((lane & 15) >> 2) ^ (lane >> 4)) << 2) | (lane & 3)) * 16;
        const char* ob = ws + WS_OP + sb * 1024;
        auto stw = [&](int j, int slot) {
            const int st = (j & ~3) | ((j + rot) & 3);
#pragma unroll
            for (int i = 0; i < 16; ++i) dma_s(woff, A.wo + (size_t)(st * 64 + i * 4) * 2048, lds0 + slot * 24576 + i * 1024);
        };
        auto sto = [&](int j, int slot) {
            const int st = (j & ~3) | ((j + rot) & 3);
#pragma unroll
            for (int i = 0; i < 8; ++i) dma_c(lin, ob + (size_t)(st * 8 + i) * (SEQ * 16), lds0 + slot * 24576 + 16384 + i * 1024);
        };
        stw(0, 0); stw(1, 1);
        BARRIER();
        arrive(ws, CT_O + rb * 4, 4, lane);
        LSTAMP(12);
        wait_count(ws, CT_O + (sb >> 1) * 4 + (dt & 3), 16);
        LSTAMP(13);
        sto(0, 0); sto(1, 1);
        WAITV_BAR(8);
        LSTAMP(14);
        sto(2, 2); stw(2, 2);
        int slot = 0;
        for (int j = 1; j < 12; ++j) { WAITV_BAR(24); sto(j + 2, slot); stw(j + 2, slot); slot = slot == 2 ? 0 : slot + 1; }
        wait_count(ws, CT_Y + (sb >> 1) * 8 + dt, 2);
        WAITV_BAR(24); sto(14, 2); stw(14, 2);
        WAITV_BAR(24); sto(15, 0); stw(15, 0);
        WAITV_BAR(24); WAITV_BAR(0);
    }
}

__device__ __forceinline__ void side_a(const Args& A, char* lds, int wid) {
    char* ws = A.ws;
    const unsigned lds0 = (unsigned)(uintptr_t)lds;
    const int xcd = blockIdx.x & 7, r = blockIdx.x >> 3;
    const rsrc_t rw = __builtin_amdgcn_make_buffer_rsrc(ws, 0, WS_END, 0x00020000);
    unsigned cnt;
    bf16x8 qA0, qA1, qB0, qB1;
    float qn;
    __builtin_amdgcn_s_setprio(PRIO_A);
    {
        const int lane = fresh_lane(), h = lane >> 5, r32 = lane & 31, q4 = (lane & 15) >> 2;
        const unsigned tb = lds0 + 16384 + (8 * h + q4) * 128 + (((wid & 1) ^ (q4 >> 1)) * 64) + ((lane >> 4) & 1) * 32 + (lane & 3) * 8;
        const unsigned xb = lds0 + ((wid >> 1) * 64 + r32) * 128 + ((h ^ ((r32 >> 1) & 7)) * 16);
        f32x16 a0 = {}, a1 = {}, y0 = {};
        f32x8 y1 = {};
        asm volatile("s_nop 1\n" ASM_QPROJ
                     : [cnt] "=&s"(cnt), "+{v[0:15]}"(a0), "+{v[16:31]}"(a1), "+{v[56:71]}"(y0), "+{v[72:79]}"(y1)
                     : "{v80}"(tb), "{v81}"(xb), "{v82}"(xb ^ 32), "{v83}"(xb ^ 64), "{v84}"(xb ^ 96)
                     : "memory", "scc", CLOB16(32, 33, 34, 35, 36, 37, 38, 39, 40, 41, 42, 43, 44, 45, 46, 47), CLOB8(48, 49, 50, 51, 52, 53, 54, 55));
        STAMP(1);
        const u32x4 u0 = pack8(a0, 0, QSCALE), u1 = pack8(a0, 1, QSCALE), w0 = pack8(a1, 0, QSCALE), w1 = pack8(a1, 1, QSCALE);
        float nA = sumsq8(u0) + sumsq8(u1), nB = sumsq8(w0) + sumsq8(w1);
        nA += __shfl_xor(nA, 32); nB += __shfl_xor(nB, 32);
        qn = wave_max((nA < 3.0e38f && nB < 3.0e38f) ? fmaxf(nA, nB) : __builtin_inff());
        qA0 = BC8(u0); qA1 = BC8(u1); qB0 = BC8(w0); qB1 = BC8(w1);
    }
    const int head = ((xcd & 1) * 8 + (r >> 2)) * 2 + (wid & 1), s0 = ((xcd >> 1) * 4 + (r & 3)) * 128 + (wid >> 1) * 64;
    BARRIER();
    STAMP(2);
    {
        const int lane = fresh_lane();
        lds_cptr p = (lds_cptr)lds + lane * 16;
        const float kn = *(const __attribute__((address_space(3))) float*)((lds_cptr)lds + LDS_X + 2048 + lane * 4);
        if (!__all(qn * kn <= SCORE_BOUND * SCORE_BOUND)) {
            f32x16 o; float inv;
            attn_online(rw, qA0, qA1, lane, o, inv); attn_store(rw, o, inv, head, s0 + (lane & 31), lane >> 5);
            attn_online(rw, qB0, qB1, lane, o, inv); attn_store(rw, o, inv, head, s0 + 32 + (lane & 31), lane >> 5);
            for (int c = 0; c < 8; ++c) BARRIER();
        } else {
            bf16x8 k0 = lds128(p + LDS_X), k1 = lds128(p + LDS_X + 1024);
            const short one = ((lane & 3) == 0 && ((lane >> 2) & 1) == ((lane >> 4) & 1)) ? (short)0x3F80 : (short)0;
            const bf16x8 ones = {one, one, one, one, one, one, one, one};
            f32x16 oA = {}, oB = {};
            f32x4 lA = {}, lB = {};
            f32x16 sA = MFMA32(k0, qA0, f32x16{}), sB = MFMA32(k0, qB0, f32x16{});
            sA = MFMA32(k1, qA1, sA); sB = MFMA32(k1, qB1, sB);
            k0 = lds128(p + 40960); k1 = lds128(p + 40960 + 1024);
            bf16x8 v0 = lds128(p + 40960 + 2048), v1 = lds128(p + 40960 + 3072);
#pragma unroll
            for (int i = 0; i < 16; ++i) sA[i] = __builtin_amdgcn_exp2f(sA[i]);
            u32x4 pA0 = pack8(sA, 0, 1.f), pA1 = pack8(sA, 1, 1.f);
            asm volatile("s_nop 15\n" ASM_ATTN
                         : [cnt] "=&s"(cnt), "+{v[0:15]}"(oA), "+{v[16:31]}"(oB), "+{v[48:63]}"(sB), "+{v[64:67]}"(pA0), "+{v[68:71]}"(pA1),
                           "+{v[100:103]}"(lA), "+{v[104:107]}"(lB), "+{v[108:111]}"(k0), "+{v[112:115]}"(k1), "+{v[116:119]}"(v0), "+{v[120:123]}"(v1)
                         : "{v[80:83]}"(qA0), "{v[84:87]}"(qA1), "{v[88:91]}"(qB0), "{v[92:95]}"(qB1), "{v[96:99]}"(ones),
                           "{v124}"((unsigned)(uintptr_t)p + 8192)
                         : "memory", "scc", CLOB16(32, 33, 34, 35, 36, 37, 38, 39, 40, 41, 42, 43, 44, 45, 46, 47), CLOB8(72, 73, 74, 75, 76, 77, 78, 79));
            const int ln = fresh_lane();
            attn_store(rw, oA, __builtin_amdgcn_rcpf(lA[0]), head, s0 + (ln & 31), ln >> 5);
            attn_store(rw, oB, __builtin_amdgcn_rcpf(lB[0]), head, s0 + 32 + (ln & 31), ln >> 5);
        }
    }
    STAMP(3);
    WAITV_BAR(0);
    {
        const int lane = fresh_lane(), h = lane >> 5, r32 = lane & 31, q4 = (lane & 15) >> 2;
        const int sb = (xcd >> 1) * 8 + (r & 7), dt = (xcd & 1) * 4 + (r >> 3);
        const unsigned tb = lds0 + (4 * h + q4) * 256 + ((wid ^ q4) * 64) + ((lane >> 4) & 1) * 32 + (lane & 3) * 8;
        const unsigned ob = lds0 + 16384 + h * 1024 + r32 * 16;
        f32x16 a0 = {}, a1 = {}, y0 = {};
        f32x8 y1 = {};
#define OPROJ(TXT) asm volatile(TXT : [cnt] "=&s"(cnt), "+{v[0:15]}"(a0), "+{v[16:31]}"(a1), "+{v[56:71]}"(y0), "+{v[72:79]}"(y1) : "{v80}"(tb), "{v81}"(ob) \
                                : "memory", "scc", CLOB16(32, 33, 34, 35, 36, 37, 38, 39, 40, 41, 42, 43, 44, 45, 46, 47), CLOB8(48, 49, 50, 51, 52, 53, 54, 55))
        OPROJ("s_nop 1\n" ASM_OPROJ_A);
        STAMP(4);
        BARRIER();
        STAMP(5);
        const int s = sb * 64 + r32;
        u32x4 yp[2][2][2];
        float gate[2], bias[2];
#pragma unroll
        for (int si = 0; si < 2; ++si) {
#pragma unroll
            for (int kh = 0; kh < 2; ++kh)
#pragma unroll
                for (int c = 0; c < 2; ++c)
                    yp[si][kh][c] = __builtin_amdgcn_raw_buffer_load_b128(
                        rw, lane * 16 + c * 1024, WS_Y + ((((sb >> 1) * 8 + dt) * 2 + kh) * 2 + (sb & 1)) * 16384 + (si * 4 + wid) * 2048, SC1);
            gate[si] = __uint_as_float(__builtin_amdgcn_raw_buffer_load_b32(rw, (s + si * 32) * 4, WS_GATE, 0));
            bias[si] = bflo(*(const unsigned short*)(A.b2 + (s + si * 32) * 2));
        }
        OPROJ(ASM_OPROJ_B);
#undef OPROJ
        STAMP(6);
#pragma unroll
        for (int si = 0; si < 2; ++si) {
            const f32x16& a = si ? a1 : a0;
            const float g = gate[si], og = 1.f - g;
            unsigned pk[8];
#pragma unroll
            for (int k = 0; k < 8; ++k) {
                const unsigned p0 = yp[si][0][k >> 2][k & 3], p1 = yp[si][1][k >> 2][k & 3];
                pk[k] = cvtpk(g * a[2 * k] + og * (bflo(p0) + bflo(p1) + bias[si]), g * a[2 * k + 1] + og * (bfhi(p0) + bfhi(p1) + bias[si]));
            }
            char* op = A.out + (size_t)(s + si * 32) * 2048 + (dt * 128 + wid * 32) * 2 + h * 16;
#pragma unroll
            for (int k = 0; k < 2; ++k) {
                const auto x0 = __builtin_amdgcn_permlane32_swap(pk[4 * k], pk[4 * k + 2], false, false);
                const auto x1 = __builtin_amdgcn_permlane32_swap(pk[4 * k + 1], pk[4 * k + 3], false, false);
                *(u32x4*)(op + k * 32) = (u32x4){x0[0], x1[0], x0[1], x1[1]};
            }
        }
    }
    STAMP(7);
}

__device__ __forceinline__ void side_b_loader(const Args& A, unsigned lds0, int lane) {
    char* ws = A.ws;
    const int gi = blockIdx.x - 256, xcd = gi & 7, r = gi >> 3;
    const unsigned sw256 = (((((lane & 15) >> 2) ^ (lane >> 4)) << 2) | (lane & 3)) * 16;
    if (r >= 28) {
        const int id = (r - 28) * 8 + xcd;
        for (int i = id; i < 14336 + 2048; i += 32) {
            asm volatile("s_waitcnt vmcnt(24)" ::: "memory");
            dma_s(lane * 16, i < 14336 ? A.w2 + (size_t)i * 1024 : A.wo + (size_t)(i - 14336) * 1024, lds0 + 65536 + 1024);
        }
    } else {
        const int dt = (xcd & 1) * 4 + (r & 3), ft = (xcd >> 1) * 7 + (r >> 2), rot = 2 * (((r & 3) + (r >> 2)) & 3);
        const unsigned aoff = (lane >> 4) * (DF * 2) + ft * 256 + sw256, boff = (dt * 128 + lane) * 16;
        auto sta = [&](int j) {
            const int st = (j & ~7) | ((j + rot) & 7);
#pragma unroll
            for (int i = 0; i < 8; ++i) dma_s(aoff, A.w1 + (size_t)(st * 32 + i * 4) * (DF * 2), lds0 + (j & 3) * 16384 + i * 1024);
        };
        auto stb = [&](int j) {
            const int st = (j & ~7) | ((j + rot) & 7);
#pragma unroll
            for (int i = 0; i < 4; ++i) dma_s2(boff, ws + WS_HT + (size_t)(st * 4 + i) * 16384, lds0 + (j & 3) * 16384 + 8192 + i * 2048);
        };
        stb(0); sta(0); stb(1); sta(1); stb(2); sta(2);
        for (int j = 0; j < 61; ++j) { WAITV_BAR(32); if (j == 0) LSTAMP(8); if (j == 16) LSTAMP(11); if (j == 32) LSTAMP(12); stb(j + 3); sta(j + 3); }
        WAITV_BAR(32); WAITV_BAR(16); WAITV_BAR(0);
    }
    {
        const int kh = xcd >> 2, st_ = (xcd & 1) * 8 + (r & 7), dt = ((xcd >> 1) & 1) * 4 + (r >> 3), rot = 2 * (((r & 7) + (r >> 3)) & 3);
        const unsigned aoff = (lane >> 4) * (SEQ * 2) + st_ * 256 + sw256, boff = (dt * 128 + lane) * 16;
        const char* ab = A.w2 + (size_t)kh * 1792 * (SEQ * 2);
        const char* bb = ws + WS_G + (size_t)kh * 224 * 16384;
        auto sta = [&](int j) {
            const int st = (j & ~7) | ((j + rot) & 7);
#pragma unroll
            for (int i = 0; i < 8; ++i) dma_s(aoff, ab + (size_t)(st * 32 + i * 4) * (SEQ * 2), lds0 + (j & 3) * 16384 + i * 1024);
        };
        auto stb = [&](int j) {
            const int st = (j & ~7) | ((j + rot) & 7);
#pragma unroll
            for (int i = 0; i < 4; ++i) dma_c2(boff, bb + (size_t)(st * 4 + i) * 16384, lds0 + (j & 3) * 16384 + 8192 + i * 2048);
        };
        sta(0); sta(1); sta(2);
        if (r < 28) {
            BARRIER();
            arrive(ws, CT_G + (((xcd & 1) * 4 + (r & 3)) * 2 + (xcd >> 2)) * 4, 4, lane);
        }
        LSTAMP(13);
        wait_count(ws, CT_G + (dt * 2 + kh) * 4 + (r & 3), 14);
        LSTAMP(14);
        stb(0); stb(1); stb(2);
        WAITV_BAR(16); stb(3); sta(3);
        WAITV_BAR(24); stb(4); sta(4);
        for (int j = 2; j < 53; ++j) { WAITV_BAR(32); stb(j + 3); sta(j + 3); }
        WAITV_BAR(32); WAITV_BAR(16); WAITV_BAR(0);
        BARRIER();
        arrive(ws, CT_Y + st_ * 8 + dt, 1, lane);
    }
}

__device__ __forceinline__ float gelu_tanh(float u) {
    const float z = u * fmaf(u * u, -0.044715f * 2.302208198f, -2.302208198f);
    return u * __builtin_amdgcn_rcpf(1.f + __builtin_amdgcn_exp2f(z));
}

__device__ __forceinline__ void side_b(const Args& A, char* lds, int wid) {
    char* ws = A.ws;
    const unsigned lds0 = (unsigned)(uintptr_t)lds;
    const int gi = blockIdx.x - 256, xcd = gi & 7, r = gi >> 3;
    const rsrc_t rw = __builtin_amdgcn_make_buffer_rsrc(ws, 0, WS_END, 0x00020000);
    unsigned cnt;
    __builtin_amdgcn_s_setprio(PRIO_B);
#define MIX(TXT) asm volatile("s_nop 1\n" TXT \
        : [cnt] "=&s"(cnt), "+{v[0:15]}"(acc[0][0]), "+{v[16:31]}"(acc[0][1]), "+{v[32:47]}"(acc[1][0]), "+{v[48:63]}"(acc[1][1]), "+{v[80:95]}"(fy) \
        : "{v96}"(tb), "{v97}"(tb ^ 64), "{v98}"(rb) : "memory", "scc", CLOB16(64, 65, 66, 67, 68, 69, 70, 71, 72, 73, 74, 75, 76, 77, 78, 79))
    if (r < 28) {
        const int lane = fresh_lane(), h = lane >> 5, q4 = (lane & 15) >> 2, wf = wid >> 1, wd = wid & 1;
        const int dt = (xcd & 1) * 4 + (r & 3), ft = (xcd >> 1) * 7 + (r >> 2);
        const unsigned tb = lds0 + (8 * h + q4) * 256 + (((2 * wf) ^ q4) * 64) + ((lane >> 4) & 1) * 32 + (lane & 3) * 8;
        const unsigned rb = lds0 + 8192 + h * 2048 + (wd * 64 + (lane & 31)) * 16;
        f32x16 acc[2][2] = {}, fy = {};
        MIX(ASM_MIX1);
        __builtin_amdgcn_s_setprio(PRIO_EPI);
        STAMP(2);
#pragma unroll
        for (int fi = 0; fi < 2; ++fi) {
            const int fb = ft * 4 + wf * 2 + fi;
            float bias[16];
#pragma unroll
            for (int k = 0; k < 4; ++k) {
                const u32x2 bv = *(const u32x2*)(A.b1 + (fb * 32 + 8 * k + 4 * h) * 2);
                bias[4 * k] = bflo(bv[0]); bias[4 * k + 1] = bfhi(bv[0]); bias[4 * k + 2] = bflo(bv[1]); bias[4 * k + 3] = bfhi(bv[1]);
            }
#pragma unroll
            for (int di = 0; di < 2; ++di) {
                f32x16 u;
#pragma unroll
                for (int k = 0; k < 16; ++k) u[k] = gelu_tanh(acc[fi][di][k] + bias[k]);
                const unsigned off = WS_G + ((fb * 4 + h) * DM + dt * 128 + wd * 64 + di * 32 + (lane & 31)) * 16;
                st16(rw, off, pack8(u, 0, 1.f));
                st16(rw, off + 2 * DM * 16, pack8(u, 1, 1.f));
            }
        }
        WAITV_BAR(0);
    }
    __builtin_amdgcn_s_setprio(PRIO_B);
    STAMP(3);
    {
        const int lane = fresh_lane(), h = lane >> 5, q4 = (lane & 15) >> 2, wsq = wid >> 1, wd = wid & 1;
        const int kh = xcd >> 2, st_ = (xcd & 1) * 8 + (r & 7), dt = ((xcd >> 1) & 1) * 4 + (r >> 3);
        const unsigned tb = lds0 + (4 * h + q4) * 256 + (((2 * wsq) ^ q4) * 64) + ((lane >> 4) & 1) * 32 + (lane & 3) * 8;
        const unsigned rb = lds0 + 8192 + h * 2048 + (wd * 64 + (lane & 31)) * 16;
        f32x16 acc[2][2] = {}, fy = {};
        MIX(ASM_MIX2);
        __builtin_amdgcn_s_setprio(PRIO_EPI);
        STAMP(4);
        const unsigned dst = WS_Y + (((st_ * 8 + dt) * 2 + kh) * 2 + wsq) * 16384;
#pragma unroll
        for (int di = 0; di < 2; ++di)
#pragma unroll
            for (int si = 0; si < 2; ++si)
#pragma unroll
                for (int c = 0; c < 2; ++c)
                    st16(rw, dst + (si * 4 + wd * 2 + di) * 2048 + c * 1024 + lane * 16, pack8(acc[di][si], c, 1.f));
        WAITV_BAR(0);
    }
#undef MIX
    STAMP(7);
}

__global__ __launch_bounds__(512, 4) void k_main(const char* x, const char* wq, const char* wo, const char* w1, const char* b1, const char* w2,
                                                  const char* b2, char* ws, char* out) {
    __shared__ __attribute__((aligned(1024))) char lds[LDS_BYTES];
    const int wid = __builtin_amdgcn_readfirstlane(threadIdx.x >> 6);
    if (wid > 4) return;
    const Args A = {x, wq, wo, w1, b1, w2, b2, ws, out};
    STAMP(0);
    if (wid == 4) {
        __builtin_amdgcn_s_setprio(3);
        if (blockIdx.x < 256) side_a_loader(A, (unsigned)(uintptr_t)lds, threadIdx.x & 63);
        else side_b_loader(A, (unsigned)(uintptr_t)lds, threadIdx.x & 63);
    } else {
        if (blockIdx.x < 256) side_a(A, lds, wid);
        else side_b(A, lds, wid);
    }
}

extern "C" void kernel_launch(void* const* d_in, const int* in_sizes, int n_in, void* d_out, int out_size, void* d_ws, size_t ws_size,
                              hipStream_t stream) {
    k_pre<<<256, 512, 0, stream>>>((const char*)d_in[0], (const char*)d_in[2], (const char*)d_in[3], (const char*)d_in[10], (char*)d_ws);
    k_main<<<512, 512, 0, stream>>>((const char*)d_in[0], (const char*)d_in[1], (const char*)d_in[4], (const char*)d_in[6], (const char*)d_in[7],
                                    (const char*)d_in[8], (const char*)d_in[9], (char*)d_ws, (char*)d_out);
}
```

```cpp
#include <hip/hip_runtime.h>
#include <hip/hip_bf16.h>
#include <stdint.h>

typedef short bf16x8 __attribute__((ext_vector_type(8)));
typedef float f32x16 __attribute__((ext_vector_type(16)));
typedef float f32x8 __attribute__((ext_vector_type(8)));
typedef float f32x4 __attribute__((ext_vector_type(4)));
typedef unsigned u32x4 __attribute__((ext_vector_type(4)));
typedef unsigned u32x2 __attribute__((ext_vector_type(2)));
typedef __attribute__((address_space(3))) const char* lds_cptr;
typedef __attribute__((address_space(1))) unsigned gu32;
typedef __amdgpu_buffer_rsrc_t rsrc_t;

constexpr int SEQ = 2048, DM = 1024, DF = 3584;
constexpr unsigned CT_G = 0, CT_O = 64, CT_Y = 128;
constexpr unsigned WS_KV = 1u << 20;
constexpr unsigned WS_GATE = WS_KV + (256u << 10);
constexpr unsigned WS_KN = WS_GATE + (8u << 10);
constexpr unsigned WS_HT = 2u << 20;
constexpr unsigned WS_OP = 6u << 20;
constexpr unsigned WS_G = 10u << 20;
constexpr unsigned WS_Y = 18u << 20;
constexpr unsigned WS_END = 26u << 20;
constexpr int SC1 = 16;
constexpr int LDS_X = 73728, LDS_BYTES = 81920;
constexpr float QSCALE = 0.17677669529663687f * 1.4426950408889634f;
constexpr float SCORE_BOUND = 60.f;

#define LSTAMP(i)
#define STAMP(i)
#define POLL_SLEEP 8
#define HEAD0 0
#define HEAD1 0
#define PRIO_A 0
#define PRIO_B 0
#define PRIO_EPI 2
#define MFMA32(a, b, c) __builtin_amdgcn_mfma_f32_32x32x16_bf16(a, b, c, 0, 0, 0)
#define BC8(x) __builtin_bit_cast(bf16x8, x)
#define BARRIER() asm volatile("s_barrier" ::: "memory")
#define WAITV_BAR(N) asm volatile("s_waitcnt vmcnt(%0)\n\ts_barrier" ::"i"(N) : "memory")
#define LGKM_BAR() asm volatile("s_waitcnt lgkmcnt(0)\n\ts_barrier" ::: "memory")
#define CLOB8(a, b, c, d, e, f, g, h) "v" #a, "v" #b, "v" #c, "v" #d, "v" #e, "v" #f, "v" #g, "v" #h
#define CLOB16(a, b, c, d, e, f, g, h, i, j, k, l, m, n, o, p) CLOB8(a, b, c, d, e, f, g, h), CLOB8(i, j, k, l, m, n, o, p)

__device__ __forceinline__ unsigned cvtpk(float lo, float hi) {
    typedef __bf16 bf2 __attribute__((ext_vector_type(2)));
    typedef float f2 __attribute__((ext_vector_type(2)));
    return __builtin_bit_cast(unsigned, __builtin_convertvector((f2){lo, hi}, bf2));
}
__device__ __forceinline__ float bflo(unsigned u) { return __uint_as_float(u << 16); }
__device__ __forceinline__ float bfhi(unsigned u) { return __uint_as_float(u & 0xffff0000u); }
__device__ __forceinline__ u32x4 pack8(const f32x16& a, int c, float m) {
    return (u32x4){cvtpk(a[8 * c] * m, a[8 * c + 1] * m), cvtpk(a[8 * c + 2] * m, a[8 * c + 3] * m),
                   cvtpk(a[8 * c + 4] * m, a[8 * c + 5] * m), cvtpk(a[8 * c + 6] * m, a[8 * c + 7] * m)};
}
__device__ __forceinline__ float sumsq8(u32x4 v) {
    float n = 0.f;
#pragma unroll
    for (int i = 0; i < 4; ++i) n = fmaf(bflo(v[i]), bflo(v[i]), fmaf(bfhi(v[i]), bfhi(v[i]), n));
    return n;
}
__device__ __forceinline__ void dma_s(unsigned voff, const void* sbase, unsigned lds) {
    asm volatile("s_mov_b32 m0, %2\n\ts_nop 0\n\tglobal_load_lds_dwordx4 %0, %1" ::"v"(voff), "s"(sbase), "s"(lds) : "memory");
}
__device__ __forceinline__ void dma_s2(unsigned voff, const void* sbase, unsigned lds) {
    asm volatile("s_mov_b32 m0, %2\n\ts_nop 0\n\tglobal_load_lds_dwordx4 %0, %1\n\tglobal_load_lds_dwordx4 %0, %1 offset:1024"
                 ::"v"(voff), "s"(sbase), "s"(lds) : "memory");
}
#define DMA4_TXT(SC) "s_mov_b32 m0, %5\n\ts_nop 0\n\tglobal_load_lds_dwordx4 %0, %4" SC "\n\tglobal_load_lds_dwordx4 %1, %4 offset:1024" SC \
                     "\n\tglobal_load_lds_dwordx4 %2, %4 offset:2048" SC "\n\tglobal_load_lds_dwordx4 %3, %4 offset:3072" SC
__device__ __forceinline__ void dma4(const unsigned* v, const char* sbase, unsigned lds) {
    asm volatile(DMA4_TXT("") ::"v"(v[0]), "v"(v[1]), "v"(v[2]), "v"(v[3]), "s"(sbase - 3072), "s"(lds) : "memory");
}
__device__ __forceinline__ void dma4c(const unsigned* v, const char* sbase, unsigned lds) {
    asm volatile(DMA4_TXT(" sc1") ::"v"(v[0]), "v"(v[1]), "v"(v[2]), "v"(v[3]), "s"(sbase - 3072), "s"(lds) : "memory");
}
__device__ __forceinline__ unsigned off4(unsigned want, int piece) { return want + 3072 - (piece & 3) * 1024; }
__device__ __forceinline__ void st16(rsrc_t rw, unsigned off, u32x4 v) { __builtin_amdgcn_raw_buffer_store_b128(v, rw, off, 0, SC1); }
typedef short s16x4 __attribute__((ext_vector_type(4)));
__device__ __forceinline__ s16x4 ldstr(lds_cptr p) {
    return __builtin_bit_cast(s16x4, __builtin_amdgcn_ds_read_tr16_b64_v4i16((__attribute__((address_space(3))) s16x4*)p));
}
__device__ __forceinline__ bf16x8 lds128(lds_cptr p) { return *(const __attribute__((address_space(3))) bf16x8*)p; }
__device__ __forceinline__ int fresh_lane() {
    int l;
    asm volatile("v_mbcnt_lo_u32_b32 %0, -1, 0\n\tv_mbcnt_hi_u32_b32 %0, -1, %0" : "=v"(l));
    return l;
}
__device__ __forceinline__ float wave_sum(float v) {
#pragma unroll
    for (int o = 32; o > 0; o >>= 1) v += __shfl_xor(v, o);
    return v;
}
__device__ __forceinline__ float wave_max(float v) {
#pragma unroll
    for (int o = 32; o > 0; o >>= 1) v = fmaxf(v, __shfl_xor(v, o));
    return v;
}
__device__ __forceinline__ void arrive(char* ws, unsigned line, int reps, int lane) {
    if (lane < reps) __hip_atomic_fetch_add((gu32*)ws + (line + lane) * 32, 1u, __ATOMIC_RELAXED, __HIP_MEMORY_SCOPE_AGENT);
}
__device__ __forceinline__ void wait_count(char* ws, unsigned line, unsigned n) {
    for (unsigned spins = 0; spins < (1u << 20); ++spins) {
        if (__builtin_amdgcn_readfirstlane(__hip_atomic_load((gu32*)ws + line * 32, __ATOMIC_RELAXED, __HIP_MEMORY_SCOPE_AGENT)) >= n) break;
        __builtin_amdgcn_s_sleep(POLL_SLEEP);
    }
    asm volatile("" ::: "memory");
}
#define ASM_ATTN \
    "s_mov_b32 %[cnt], 4\n" \
    "1:\n" \
    "s_waitcnt lgkmcnt(2)\n" \
    "v_mfma_f32_32x32x16_bf16 v[32:47], v[108:111], v[80:83], 0\n" \
    "v_exp_f32 v48, v48\n" \
    "v_exp_f32 v49, v49\n" \
    "v_exp_f32 v50, v50\n" \
    "v_exp_f32 v51, v51\n" \
    "v_mfma_f32_32x32x16_bf16 v[32:47], v[112:115], v[84:87], v[32:47]\n" \
    "v_cvt_pk_bf16_f32 v72, v48, v49\n" \
    "v_cvt_pk_bf16_f32 v73, v50, v51\n" \
    "v_exp_f32 v52, v52\n" \
    "v_exp_f32 v53, v53\n" \
    "v_exp_f32 v54, v54\n" \
    "s_waitcnt lgkmcnt(1)\n" \
    "v_mfma_f32_32x32x16_bf16 v[0:15], v[116:119], v[64:67], v[0:15]\n" \
    "v_exp_f32 v55, v55\n" \
    "v_cvt_pk_bf16_f32 v74, v52, v53\n" \
    "v_exp_f32 v56, v56\n" \
    "v_exp_f32 v57, v57\n" \
    "v_cvt_pk_bf16_f32 v75, v54, v55\n" \
    "v_mfma_f32_16x16x32_bf16 v[100:103], v[96:99], v[64:67], v[100:103]\n" \
    "v_exp_f32 v58, v58\n" \
    "v_exp_f32 v59, v59\n" \
    "s_waitcnt lgkmcnt(0)\n" \
    "v_mfma_f32_32x32x16_bf16 v[0:15], v[120:123], v[68:71], v[0:15]\n" \
    "v_cvt_pk_bf16_f32 v76, v56, v57\n" \
    "v_exp_f32 v60, v60\n" \
    "v_exp_f32 v61, v61\n" \
    "v_cvt_pk_bf16_f32 v77, v58, v59\n" \
    "v_exp_f32 v62, v62\n" \
    "v_mfma_f32_16x16x32_bf16 v[100:103], v[96:99], v[68:71], v[100:103]\n" \
    "v_exp_f32 v63, v63\n" \
    "v_cvt_pk_bf16_f32 v78, v60, v61\n" \
    "v_cvt_pk_bf16_f32 v79, v62, v63\n" \
    "v_mfma_f32_32x32x16_bf16 v[48:63], v[108:111], v[88:91], 0\n" \
    "v_exp_f32 v32, v32\n" \
    "ds_read_b128 v[108:111], v125 offset:45056\n" \
    "v_exp_f32 v33, v33\n" \
    "v_exp_f32 v34, v34\n" \
    "v_exp_f32 v35, v35\n" \
    "v_mfma_f32_32x32x16_bf16 v[48:63], v[112:115], v[92:95], v[48:63]\n" \
    "v_cvt_pk_bf16_f32 v64, v32, v33\n" \
    "ds_read_b128 v[112:115], v125 offset:46080\n" \
    "v_cvt_pk_bf16_f32 v65, v34, v35\n" \
    "v_exp_f32 v36, v36\n" \
    "v_exp_f32 v37, v37\n" \
    "v_exp_f32 v38, v38\n" \
    "v_mfma_f32_32x32x16_bf16 v[16:31], v[116:119], v[72:75], v[16:31]\n" \
    "v_exp_f32 v39, v39\n" \
    "ds_read_b128 v[116:119], v125 offset:47104\n" \
    "v_cvt_pk_bf16_f32 v66, v36, v37\n" \
    "v_exp_f32 v40, v40\n" \
    "v_exp_f32 v41, v41\n" \
    "v_cvt_pk_bf16_f32 v67, v38, v39\n" \
    "v_mfma_f32_16x16x32_bf16 v[104:107], v[96:99], v[72:75], v[104:107]\n" \
    "v_exp_f32 v42, v42\n" \
    "v_exp_f32 v43, v43\n" \
    "v_mfma_f32_32x32x16_bf16 v[16:31], v[120:123], v[76:79], v[16:31]\n" \
    "v_cvt_pk_bf16_f32 v68, v40, v41\n" \
    "ds_read_b128 v[120:123], v125 offset:48128\n" \
    "v_exp_f32 v44, v44\n" \
    "v_exp_f32 v45, v45\n" \
    "v_cvt_pk_bf16_f32 v69, v42, v43\n" \
    "v_exp_f32 v46, v46\n" \
    "v_mfma_f32_16x16x32_bf16 v[104:107], v[96:99], v[76:79], v[104:107]\n" \
    "v_exp_f32 v47, v47\n" \
    "v_cvt_pk_bf16_f32 v70, v44, v45\n" \
    "v_cvt_pk_bf16_f32 v71, v46, v47\n" \
    "s_waitcnt lgkmcnt(2)\n" \
    "v_mfma_f32_32x32x16_bf16 v[32:47], v[108:111], v[80:83], 0\n" \
    "v_exp_f32 v48, v48\n" \
    "v_exp_f32 v49, v49\n" \
    "v_exp_f32 v50, v50\n" \
    "v_exp_f32 v51, v51\n" \
    "v_mfma_f32_32x32x16_bf16 v[32:47], v[112:115], v[84:87], v[32:47]\n" \
    "v_cvt_pk_bf16_f32 v72, v48, v49\n" \
    "v_cvt_pk_bf16_f32 v73, v50, v51\n" \
    "v_exp_f32 v52, v52\n" \
    "v_exp_f32 v53, v53\n" \
    "v_exp_f32 v54, v54\n" \
    "s_waitcnt lgkmcnt(1)\n" \
    "v_mfma_f32_32x32x16_bf16 v[0:15], v[116:119], v[64:67], v[0:15]\n" \
    "v_exp_f32 v55, v55\n" \
    "v_cvt_pk_bf16_f32 v74, v52, v53\n" \
    "v_exp_f32 v56, v56\n" \
    "v_exp_f32 v57, v57\n" \
    "v_cvt_pk_bf16_f32 v75, v54, v55\n" \
    "v_mfma_f32_16x16x32_bf16 v[100:103], v[96:99], v[64:67], v[100:103]\n" \
    "v_exp_f32 v58, v58\n" \
    "v_exp_f32 v59, v59\n" \
    "s_waitcnt lgkmcnt(0)\n" \
    "v_mfma_f32_32x32x16_bf16 v[0:15], v[120:123], v[68:71], v[0:15]\n" \
    "v_cvt_pk_bf16_f32 v76, v56, v57\n" \
    "v_exp_f32 v60, v60\n" \
    "v_exp_f32 v61, v61\n" \
    "v_cvt_pk_bf16_f32 v77, v58, v59\n" \
    "v_exp_f32 v62, v62\n" \
    "v_mfma_f32_16x16x32_bf16 v[100:103], v[96:99], v[68:71], v[100:103]\n" \
    "v_exp_f32 v63, v63\n" \
    "v_cvt_pk_bf16_f32 v78, v60, v61\n" \
    "v_cvt_pk_bf16_f32 v79, v62, v63\n" \
    "v_mfma_f32_32x32x16_bf16 v[48:63], v[108:111], v[88:91], 0\n" \
    "v_exp_f32 v32, v32\n" \
    "ds_read_b128 v[108:111], v124 offset:24576\n" \
    "v_exp_f32 v33, v33\n" \
    "v_exp_f32 v34, v34\n" \
    "v_exp_f32 v35, v35\n" \
    "v_mfma_f32_32x32x16_bf16 v[48:63], v[112:115], v[92:95], v[48:63]\n" \
    "v_cvt_pk_bf16_f32 v64, v32, v33\n" \
    "ds_read_b128 v[112:115], v124 offset:25600\n" \
    "v_cvt_pk_bf16_f32 v65, v34, v35\n" \
    "v_exp_f32 v36, v36\n" \
    "v_exp_f32 v37, v37\n" \
    "v_exp_f32 v38, v38\n" \
    "v_mfma_f32_32x32x16_bf16 v[16:31], v[116:119], v[72:75], v[16:31]\n" \
    "v_exp_f32 v39, v39\n" \
    "ds_read_b128 v[116:119], v124 offset:26624\n" \
    "v_cvt_pk_bf16_f32 v66, v36, v37\n" \
    "v_exp_f32 v40, v40\n" \
    "v_exp_f32 v41, v41\n" \
    "v_cvt_pk_bf16_f32 v67, v38, v39\n" \
    "v_mfma_f32_16x16x32_bf16 v[104:107], v[96:99], v[72:75], v[104:107]\n" \
    "v_exp_f32 v42, v42\n" \
    "v_exp_f32 v43, v43\n" \
    "v_mfma_f32_32x32x16_bf16 v[16:31], v[120:123], v[76:79], v[16:31]\n" \
    "v_cvt_pk_bf16_f32 v68, v40, v41\n" \
    "ds_read_b128 v[120:123], v124 offset:27648\n" \
    "v_exp_f32 v44, v44\n" \
    "v_exp_f32 v45, v45\n" \
    "v_cvt_pk_bf16_f32 v69, v42, v43\n" \
    "v_exp_f32 v46, v46\n" \
    "v_mfma_f32_16x16x32_bf16 v[104:107], v[96:99], v[76:79], v[104:107]\n" \
    "v_exp_f32 v47, v47\n" \
    "v_cvt_pk_bf16_f32 v70, v44, v45\n" \
    "v_cvt_pk_bf16_f32 v71, v46, v47\n" \
    "s_waitcnt lgkmcnt(2)\n" \
    "v_mfma_f32_32x32x16_bf16 v[32:47], v[108:111], v[80:83], 0\n" \
    "v_exp_f32 v48, v48\n" \
    "v_exp_f32 v49, v49\n" \
    "v_exp_f32 v50, v50\n" \
    "v_exp_f32 v51, v51\n" \
    "v_mfma_f32_32x32x16_bf16 v[32:47], v[112:115], v[84:87], v[32:47]\n" \
    "v_cvt_pk_bf16_f32 v72, v48, v49\n" \
    "v_cvt_pk_bf16_f32 v73, v50, v51\n" \
    "v_exp_f32 v52, v52\n" \
    "v_exp_f32 v53, v53\n" \
    "v_exp_f32 v54, v54\n" \
    "s_waitcnt lgkmcnt(1)\n" \
    "v_mfma_f32_32x32x16_bf16 v[0:15], v[116:119], v[64:67], v[0:15]\n" \
    "v_exp_f32 v55, v55\n" \
    "v_cvt_pk_bf16_f32 v74, v52, v53\n" \
    "v_exp_f32 v56, v56\n" \
    "v_exp_f32 v57, v57\n" \
    "v_cvt_pk_bf16_f32 v75, v54, v55\n" \
    "v_mfma_f32_16x16x32_bf16 v[100:103], v[96:99], v[64:67], v[100:103]\n" \
    "v_exp_f32 v58, v58\n" \
    "v_exp_f32 v59, v59\n" \
    "s_waitcnt lgkmcnt(0)\n" \
    "v_mfma_f32_32x32x16_bf16 v[0:15], v[120:123], v[68:71], v[0:15]\n" \
    "v_cvt_pk_bf16_f32 v76, v56, v57\n" \
    "v_exp_f32 v60, v60\n" \
    "v_exp_f32 v61, v61\n" \
    "v_cvt_pk_bf16_f32 v77, v58, v59\n" \
    "v_exp_f32 v62, v62\n" \
    "v_mfma_f32_16x16x32_bf16 v[100:103], v[96:99], v[68:71], v[100:103]\n" \
    "v_exp_f32 v63, v63\n" \
    "v_cvt_pk_bf16_f32 v78, v60, v61\n" \
    "v_cvt_pk_bf16_f32 v79, v62, v63\n" \
    "v_mfma_f32_32x32x16_bf16 v[48:63], v[108:111], v[88:91], 0\n" \
    "v_exp_f32 v32, v32\n" \
    "ds_read_b128 v[108:111], v124 offset:28672\n" \
    "v_exp_f32 v33, v33\n" \
    "v_exp_f32 v34, v34\n" \
    "v_exp_f32 v35, v35\n" \
    "v_mfma_f32_32x32x16_bf16 v[48:63], v[112:115], v[92:95], v[48:63]\n" \
    "v_cvt_pk_bf16_f32 v64, v32, v33\n" \
    "ds_read_b128 v[112:115], v124 offset:29696\n" \
    "v_cvt_pk_bf16_f32 v65, v34, v35\n" \
    "v_exp_f32 v36, v36\n" \
    "v_exp_f32 v37, v37\n" \
    "v_exp_f32 v38, v38\n" \
    "v_mfma_f32_32x32x16_bf16 v[16:31], v[116:119], v[72:75], v[16:31]\n" \
    "v_exp_f32 v39, v39\n" \
    "ds_read_b128 v[116:119], v124 offset:30720\n" \
    "v_cvt_pk_bf16_f32 v66, v36, v37\n" \
    "v_exp_f32 v40, v40\n" \
    "v_exp_f32 v41, v41\n" \
    "v_cvt_pk_bf16_f32 v67, v38, v39\n" \
    "v_mfma_f32_16x16x32_bf16 v[104:107], v[96:99], v[72:75], v[104:107]\n" \
    "v_exp_f32 v42, v42\n" \
    "v_exp_f32 v43, v43\n" \
    "v_mfma_f32_32x32x16_bf16 v[16:31], v[120:123], v[76:79], v[16:31]\n" \
    "v_cvt_pk_bf16_f32 v68, v40, v41\n" \
    "ds_read_b128 v[120:123], v124 offset:31744\n" \
    "v_exp_f32 v44, v44\n" \
    "v_exp_f32 v45, v45\n" \
    "v_cvt_pk_bf16_f32 v69, v42, v43\n" \
    "v_exp_f32 v46, v46\n" \
    "v_mfma_f32_16x16x32_bf16 v[104:107], v[96:99], v[76:79], v[104:107]\n" \
    "v_exp_f32 v47, v47\n" \
    "v_cvt_pk_bf16_f32 v70, v44, v45\n" \
    "v_cvt_pk_bf16_f32 v71, v46, v47\n" \
    "s_waitcnt lgkmcnt(2)\n" \
    "v_mfma_f32_32x32x16_bf16 v[32:47], v[108:111], v[80:83], 0\n" \
    "v_exp_f32 v48, v48\n" \
    "v_exp_f32 v49, v49\n" \
    "v_exp_f32 v50, v50\n" \
    "v_exp_f32 v51, v51\n" \
    "v_mfma_f32_32x32x16_bf16 v[32:47], v[112:115], v[84:87], v[32:47]\n" \
    "v_cvt_pk_bf16_f32 v72, v48, v49\n" \
    "v_cvt_pk_bf16_f32 v73, v50, v51\n" \
    "v_exp_f32 v52, v52\n" \
    "v_exp_f32 v53, v53\n" \
    "v_exp_f32 v54, v54\n" \
    "s_waitcnt lgkmcnt(1)\n" \
    "v_mfma_f32_32x32x16_bf16 v[0:15], v[116:119], v[64:67], v[0:15]\n" \
    "v_exp_f32 v55, v55\n" \
    "v_cvt_pk_bf16_f32 v74, v52, v53\n" \
    "v_exp_f32 v56, v56\n" \
    "v_exp_f32 v57, v57\n" \
    "v_cvt_pk_bf16_f32 v75, v54, v55\n" \
    "v_mfma_f32_16x16x32_bf16 v[100:103], v[96:99], v[64:67], v[100:103]\n" \
    "v_exp_f32 v58, v58\n" \
    "v_exp_f32 v59, v59\n" \
    "s_waitcnt lgkmcnt(0)\n" \
    "v_mfma_f32_32x32x16_bf16 v[0:15], v[120:123], v[68:71], v[0:15]\n" \
    "v_cvt_pk_bf16_f32 v76, v56, v57\n" \
    "v_exp_f32 v60, v60\n" \
    "v_exp_f32 v61, v61\n" \
    "v_cvt_pk_bf16_f32 v77, v58, v59\n" \
    "v_exp_f32 v62, v62\n" \
    "v_mfma_f32_16x16x32_bf16 v[100:103], v[96:99], v[68:71], v[100:103]\n" \
    "v_exp_f32 v63, v63\n" \
    "v_cvt_pk_bf16_f32 v78, v60, v61\n" \
    "v_cvt_pk_bf16_f32 v79, v62, v63\n" \
    "v_mfma_f32_32x32x16_bf16 v[48:63], v[108:111], v[88:91], 0\n" \
    "v_exp_f32 v32, v32\n" \
    "ds_read_b128 v[108:111], v125 offset:0\n" \
    "v_exp_f32 v33, v33\n" \
    "v_exp_f32 v34, v34\n" \
    "v_exp_f32 v35, v35\n" \
    "v_mfma_f32_32x32x16_bf16 v[48:63], v[112:115], v[92:95], v[48:63]\n" \
    "v_cvt_pk_bf16_f32 v64, v32, v33\n" \
    "ds_read_b128 v[112:115], v125 offset:1024\n" \
    "v_cvt_pk_bf16_f32 v65, v34, v35\n" \
    "v_exp_f32 v36, v36\n" \
    "v_exp_f32 v37, v37\n" \
    "v_exp_f32 v38, v38\n" \
    "v_mfma_f32_32x32x16_bf16 v[16:31], v[116:119], v[72:75], v[16:31]\n" \
    "v_exp_f32 v39, v39\n" \
    "ds_read_b128 v[116:119], v125 offset:2048\n" \
    "v_cvt_pk_bf16_f32 v66, v36, v37\n" \
    "v_exp_f32 v40, v40\n" \
    "v_exp_f32 v41, v41\n" \
    "v_cvt_pk_bf16_f32 v67, v38, v39\n" \
    "v_mfma_f32_16x16x32_bf16 v[104:107], v[96:99], v[72:75], v[104:107]\n" \
    "v_exp_f32 v42, v42\n" \
    "v_exp_f32 v43, v43\n" \
    "v_mfma_f32_32x32x16_bf16 v[16:31], v[120:123], v[76:79], v[16:31]\n" \
    "v_cvt_pk_bf16_f32 v68, v40, v41\n" \
    "ds_read_b128 v[120:123], v125 offset:3072\n" \
    "v_exp_f32 v44, v44\n" \
    "v_exp_f32 v45, v45\n" \
    "v_cvt_pk_bf16_f32 v69, v42, v43\n" \
    "v_exp_f32 v46, v46\n" \
    "v_mfma_f32_16x16x32_bf16 v[104:107], v[96:99], v[76:79], v[104:107]\n" \
    "v_exp_f32 v47, v47\n" \
    "v_cvt_pk_bf16_f32 v70, v44, v45\n" \
    "v_cvt_pk_bf16_f32 v71, v46, v47\n" \
    "s_waitcnt lgkmcnt(2)\n" \
    "v_mfma_f32_32x32x16_bf16 v[32:47], v[108:111], v[80:83], 0\n" \
    "v_exp_f32 v48, v48\n" \
    "v_exp_f32 v49, v49\n" \
    "v_exp_f32 v50, v50\n" \
    "v_exp_f32 v51, v51\n" \
    "v_mfma_f32_32x32x16_bf16 v[32:47], v[112:115], v[84:87], v[32:47]\n" \
    "v_cvt_pk_bf16_f32 v72, v48, v49\n" \
    "v_cvt_pk_bf16_f32 v73, v50, v51\n" \
    "v_exp_f32 v52, v52\n" \
    "v_exp_f32 v53, v53\n" \
    "v_exp_f32 v54, v54\n" \
    "s_waitcnt lgkmcnt(1)\n" \
    "v_mfma_f32_32x32x16_bf16 v[0:15], v[116:119], v[64:67], v[0:15]\n" \
    "v_exp_f32 v55, v55\n" \
    "v_cvt_pk_bf16_f32 v74, v52, v53\n" \
    "v_exp_f32 v56, v56\n" \
    "v_exp_f32 v57, v57\n" \
    "v_cvt_pk_bf16_f32 v75, v54, v55\n" \
    "v_mfma_f32_16x16x32_bf16 v[100:103], v[96:99], v[64:67], v[100:103]\n" \
    "v_exp_f32 v58, v58\n" \
    "v_exp_f32 v59, v59\n" \
    "s_waitcnt lgkmcnt(0)\n" \
    "v_mfma_f32_32x32x16_bf16 v[0:15], v[120:123], v[68:71], v[0:15]\n" \
    "v_cvt_pk_bf16_f32 v76, v56, v57\n" \
    "v_exp_f32 v60, v60\n" \
    "v_exp_f32 v61, v61\n" \
    "v_cvt_pk_bf16_f32 v77, v58, v59\n" \
    "v_exp_f32 v62, v62\n" \
    "v_mfma_f32_16x16x32_bf16 v[100:103], v[96:99], v[68:71], v[100:103]\n" \
    "v_exp_f32 v63, v63\n" \
    "v_cvt_pk_bf16_f32 v78, v60, v61\n" \
    "v_cvt_pk_bf16_f32 v79, v62, v63\n" \
    "v_mfma_f32_32x32x16_bf16 v[48:63], v[108:111], v[88:91], 0\n" \
    "v_exp_f32 v32, v32\n" \
    "ds_read_b128 v[108:111], v125 offset:4096\n" \
    "v_exp_f32 v33, v33\n" \
    "v_exp_f32 v34, v34\n" \
    "v_exp_f32 v35, v35\n" \
    "v_mfma_f32_32x32x16_bf16 v[48:63], v[112:115], v[92:95], v[48:63]\n" \
    "v_cvt_pk_bf16_f32 v64, v32, v33\n" \
    "ds_read_b128 v[112:115], v125 offset:5120\n" \
    "v_cvt_pk_bf16_f32 v65, v34, v35\n" \
    "v_exp_f32 v36, v36\n" \
    "v_exp_f32 v37, v37\n" \
    "v_exp_f32 v38, v38\n" \
    "v_mfma_f32_32x32x16_bf16 v[16:31], v[116:119], v[72:75], v[16:31]\n" \
    "v_exp_f32 v39, v39\n" \
    "ds_read_b128 v[116:119], v125 offset:6144\n" \
    "v_cvt_pk_bf16_f32 v66, v36, v37\n" \
    "v_exp_f32 v40, v40\n" \
    "v_exp_f32 v41, v41\n" \
    "v_cvt_pk_bf16_f32 v67, v38, v39\n" \
    "v_mfma_f32_16x16x32_bf16 v[104:107], v[96:99], v[72:75], v[104:107]\n" \
    "v_exp_f32 v42, v42\n" \
    "v_exp_f32 v43, v43\n" \
    "v_mfma_f32_32x32x16_bf16 v[16:31], v[120:123], v[76:79], v[16:31]\n" \
    "v_cvt_pk_bf16_f32 v68, v40, v41\n" \
    "ds_read_b128 v[120:123], v125 offset:7168\n" \
    "v_exp_f32 v44, v44\n" \
    "v_exp_f32 v45, v45\n" \
    "v_cvt_pk_bf16_f32 v69, v42, v43\n" \
    "v_exp_f32 v46, v46\n" \
    "v_mfma_f32_16x16x32_bf16 v[104:107], v[96:99], v[76:79], v[104:107]\n" \
    "v_exp_f32 v47, v47\n" \
    "v_cvt_pk_bf16_f32 v70, v44, v45\n" \
    "v_cvt_pk_bf16_f32 v71, v46, v47\n" \
    "s_waitcnt lgkmcnt(2)\n" \
    "v_mfma_f32_32x32x16_bf16 v[32:47], v[108:111], v[80:83], 0\n" \
    "v_exp_f32 v48, v48\n" \
    "v_exp_f32 v49, v49\n" \
    "v_exp_f32 v50, v50\n" \
    "v_exp_f32 v51, v51\n" \
    "v_mfma_f32_32x32x16_bf16 v[32:47], v[112:115], v[84:87], v[32:47]\n" \
    "v_cvt_pk_bf16_f32 v72, v48, v49\n" \
    "v_cvt_pk_bf16_f32 v73, v50, v51\n" \
    "v_exp_f32 v52, v52\n" \
    "v_exp_f32 v53, v53\n" \
    "v_exp_f32 v54, v54\n" \
    "s_waitcnt lgkmcnt(1)\n" \
    "v_mfma_f32_32x32x16_bf16 v[0:15], v[116:119], v[64:67], v[0:15]\n" \
    "v_exp_f32 v55, v55\n" \
    "v_cvt_pk_bf16_f32 v74, v52, v53\n" \
    "v_exp_f32 v56, v56\n" \
    "v_exp_f32 v57, v57\n" \
    "v_cvt_pk_bf16_f32 v75, v54, v55\n" \
    "v_mfma_f32_16x16x32_bf16 v[100:103], v[96:99], v[64:67], v[100:103]\n" \
    "v_exp_f32 v58, v58\n" \
    "v_exp_f32 v59, v59\n" \
    "s_waitcnt lgkmcnt(0)\n" \
    "v_mfma_f32_32x32x16_bf16 v[0:15], v[120:123], v[68:71], v[0:15]\n" \
    "v_cvt_pk_bf16_f32 v76, v56, v57\n" \
    "v_exp_f32 v60, v60\n" \
    "v_exp_f32 v61, v61\n" \
    "v_cvt_pk_bf16_f32 v77, v58, v59\n" \
    "v_exp_f32 v62, v62\n" \
    "v_mfma_f32_16x16x32_bf16 v[100:103], v[96:99], v[68:71], v[100:103]\n" \
    "v_exp_f32 v63, v63\n" \
    "v_cvt_pk_bf16_f32 v78, v60, v61\n" \
    "v_cvt_pk_bf16_f32 v79, v62, v63\n" \
    "v_mfma_f32_32x32x16_bf16 v[48:63], v[108:111], v[88:91], 0\n" \
    "v_exp_f32 v32, v32\n" \
    "ds_read_b128 v[108:111], v125 offset:8192\n" \
    "v_exp_f32 v33, v33\n" \
    "v_exp_f32 v34, v34\n" \
    "v_exp_f32 v35, v35\n" \
    "v_mfma_f32_32x32x16_bf16 v[48:63], v[112:115], v[92:95], v[48:63]\n" \
    "v_cvt_pk_bf16_f32 v64, v32, v33\n" \
    "ds_read_b128 v[112:115], v125 offset:9216\n" \
    "v_cvt_pk_bf16_f32 v65, v34, v35\n" \
    "v_exp_f32 v36, v36\n" \
    "v_exp_f32 v37, v37\n" \
    "v_exp_f32 v38, v38\n" \
    "v_mfma_f32_32x32x16_bf16 v[16:31], v[116:119], v[72:75], v[16:31]\n" \
    "v_exp_f32 v39, v39\n" \
    "ds_read_b128 v[116:119], v125 offset:10240\n" \
    "v_cvt_pk_bf16_f32 v66, v36, v37\n" \
    "v_exp_f32 v40, v40\n" \
    "v_exp_f32 v41, v41\n" \
    "v_cvt_pk_bf16_f32 v67, v38, v39\n" \
    "v_mfma_f32_16x16x32_bf16 v[104:107], v[96:99], v[72:75], v[104:107]\n" \
    "v_exp_f32 v42, v42\n" \
    "v_exp_f32 v43, v43\n" \
    "v_mfma_f32_32x32x16_bf16 v[16:31], v[120:123], v[76:79], v[16:31]\n" \
    "v_cvt_pk_bf16_f32 v68, v40, v41\n" \
    "ds_read_b128 v[120:123], v125 offset:11264\n" \
    "v_exp_f32 v44, v44\n" \
    "v_exp_f32 v45, v45\n" \
    "v_cvt_pk_bf16_f32 v69, v42, v43\n" \
    "v_exp_f32 v46, v46\n" \
    "v_mfma_f32_16x16x32_bf16 v[104:107], v[96:99], v[76:79], v[104:107]\n" \
    "v_exp_f32 v47, v47\n" \
    "v_cvt_pk_bf16_f32 v70, v44, v45\n" \
    "v_cvt_pk_bf16_f32 v71, v46, v47\n" \
    "s_waitcnt lgkmcnt(2)\n" \
    "v_mfma_f32_32x32x16_bf16 v[32:47], v[108:111], v[80:83], 0\n" \
    "v_exp_f32 v48, v48\n" \
    "v_exp_f32 v49, v49\n" \
    "v_exp_f32 v50, v50\n" \
    "v_exp_f32 v51, v51\n" \
    "v_mfma_f32_32x32x16_bf16 v[32:47], v[112:115], v[84:87], v[32:47]\n" \
    "v_cvt_pk_bf16_f32 v72, v48, v49\n" \
    "v_cvt_pk_bf16_f32 v73, v50, v51\n" \
    "v_exp_f32 v52, v52\n" \
    "v_exp_f32 v53, v53\n" \
    "v_exp_f32 v54, v54\n" \
    "s_waitcnt lgkmcnt(1)\n" \
    "v_mfma_f32_32x32x16_bf16 v[0:15], v[116:119], v[64:67], v[0:15]\n" \
    "v_exp_f32 v55, v55\n" \
    "v_cvt_pk_bf16_f32 v74, v52, v53\n" \
    "v_exp_f32 v56, v56\n" \
    "v_exp_f32 v57, v57\n" \
    "v_cvt_pk_bf16_f32 v75, v54, v55\n" \
    "v_mfma_f32_16x16x32_bf16 v[100:103], v[96:99], v[64:67], v[100:103]\n" \
    "v_exp_f32 v58, v58\n" \
    "v_exp_f32 v59, v59\n" \
    "s_waitcnt lgkmcnt(0)\n" \
    "v_mfma_f32_32x32x16_bf16 v[0:15], v[120:123], v[68:71], v[0:15]\n" \
    "v_cvt_pk_bf16_f32 v76, v56, v57\n" \
    "v_exp_f32 v60, v60\n" \
    "v_exp_f32 v61, v61\n" \
    "v_cvt_pk_bf16_f32 v77, v58, v59\n" \
    "v_exp_f32 v62, v62\n" \
    "v_mfma_f32_16x16x32_bf16 v[100:103], v[96:99], v[68:71], v[100:103]\n" \
    "v_exp_f32 v63, v63\n" \
    "v_cvt_pk_bf16_f32 v78, v60, v61\n" \
    "v_cvt_pk_bf16_f32 v79, v62, v63\n" \
    "v_mfma_f32_32x32x16_bf16 v[48:63], v[108:111], v[88:91], 0\n" \
    "v_exp_f32 v32, v32\n" \
    "ds_read_b128 v[108:111], v125 offset:12288\n" \
    "v_exp_f32 v33, v33\n" \
    "v_exp_f32 v34, v34\n" \
    "v_exp_f32 v35, v35\n" \
    "v_mfma_f32_32x32x16_bf16 v[48:63], v[112:115], v[92:95], v[48:63]\n" \
    "v_cvt_pk_bf16_f32 v64, v32, v33\n" \
    "ds_read_b128 v[112:115], v125 offset:13312\n" \
    "v_cvt_pk_bf16_f32 v65, v34, v35\n" \
    "v_exp_f32 v36, v36\n" \
    "v_exp_f32 v37, v37\n" \
    "v_exp_f32 v38, v38\n" \
    "v_mfma_f32_32x32x16_bf16 v[16:31], v[116:119], v[72:75], v[16:31]\n" \
    "v_exp_f32 v39, v39\n" \
    "ds_read_b128 v[116:119], v125 offset:14336\n" \
    "v_cvt_pk_bf16_f32 v66, v36, v37\n" \
    "v_exp_f32 v40, v40\n" \
    "v_exp_f32 v41, v41\n" \
    "v_cvt_pk_bf16_f32 v67, v38, v39\n" \
    "v_mfma_f32_16x16x32_bf16 v[104:107], v[96:99], v[72:75], v[104:107]\n" \
    "v_exp_f32 v42, v42\n" \
    "v_exp_f32 v43, v43\n" \
    "v_mfma_f32_32x32x16_bf16 v[16:31], v[120:123], v[76:79], v[16:31]\n" \
    "v_cvt_pk_bf16_f32 v68, v40, v41\n" \
    "ds_read_b128 v[120:123], v125 offset:15360\n" \
    "v_exp_f32 v44, v44\n" \
    "v_exp_f32 v45, v45\n" \
    "v_cvt_pk_bf16_f32 v69, v42, v43\n" \
    "v_exp_f32 v46, v46\n" \
    "v_mfma_f32_16x16x32_bf16 v[104:107], v[96:99], v[76:79], v[104:107]\n" \
    "v_exp_f32 v47, v47\n" \
    "v_cvt_pk_bf16_f32 v70, v44, v45\n" \
    "v_cvt_pk_bf16_f32 v71, v46, v47\n" \
    "s_waitcnt lgkmcnt(2)\n" \
    "v_mfma_f32_32x32x16_bf16 v[32:47], v[108:111], v[80:83], 0\n" \
    "v_exp_f32 v48, v48\n" \
    "v_exp_f32 v49, v49\n" \
    "v_exp_f32 v50, v50\n" \
    "v_exp_f32 v51, v51\n" \
    "v_mfma_f32_32x32x16_bf16 v[32:47], v[112:115], v[84:87], v[32:47]\n" \
    "v_cvt_pk_bf16_f32 v72, v48, v49\n" \
    "v_cvt_pk_bf16_f32 v73, v50, v51\n" \
    "v_exp_f32 v52, v52\n" \
    "v_exp_f32 v53, v53\n" \
    "v_exp_f32 v54, v54\n" \
    "s_waitcnt lgkmcnt(1)\n" \
    "v_mfma_f32_32x32x16_bf16 v[0:15], v[116:119], v[64:67], v[0:15]\n" \
    "v_exp_f32 v55, v55\n" \
    "v_cvt_pk_bf16_f32 v74, v52, v53\n" \
    "v_exp_f32 v56, v56\n" \
    "v_exp_f32 v57, v57\n" \
    "v_cvt_pk_bf16_f32 v75, v54, v55\n" \
    "v_mfma_f32_16x16x32_bf16 v[100:103], v[96:99], v[64:67], v[100:103]\n" \
    "v_exp_f32 v58, v58\n" \
    "v_exp_f32 v59, v59\n" \
    "s_waitcnt lgkmcnt(0)\n" \
    "v_mfma_f32_32x32x16_bf16 v[0:15], v[120:123], v[68:71], v[0:15]\n" \
    "v_cvt_pk_bf16_f32 v76, v56, v57\n" \
    "v_exp_f32 v60, v60\n" \
    "v_exp_f32 v61, v61\n" \
    "v_cvt_pk_bf16_f32 v77, v58, v59\n" \
    "v_exp_f32 v62, v62\n" \
    "v_mfma_f32_16x16x32_bf16 v[100:103], v[96:99], v[68:71], v[100:103]\n" \
    "v_exp_f32 v63, v63\n" \
    "v_cvt_pk_bf16_f32 v78, v60, v61\n" \
    "v_cvt_pk_bf16_f32 v79, v62, v63\n" \
    "s_barrier\n" \
    "v_mfma_f32_32x32x16_bf16 v[48:63], v[108:111], v[88:91], 0\n" \
    "v_exp_f32 v32, v32\n" \
    "ds_read_b128 v[108:111], v124 offset:0\n" \
    "v_exp_f32 v33, v33\n" \
    "v_exp_f32 v34, v34\n" \
    "v_exp_f32 v35, v35\n" \
    "v_mfma_f32_32x32x16_bf16 v[48:63], v[112:115], v[92:95], v[48:63]\n" \
    "v_cvt_pk_bf16_f32 v64, v32, v33\n" \
    "ds_read_b128 v[112:115], v124 offset:1024\n" \
    "v_cvt_pk_bf16_f32 v65, v34, v35\n" \
    "v_exp_f32 v36, v36\n" \
    "v_exp_f32 v37, v37\n" \
    "v_exp_f32 v38, v38\n" \
    "v_mfma_f32_32x32x16_bf16 v[16:31], v[116:119], v[72:75], v[16:31]\n" \
    "v_exp_f32 v39, v39\n" \
    "ds_read_b128 v[116:119], v124 offset:2048\n" \
    "v_cvt_pk_bf16_f32 v66, v36, v37\n" \
    "v_exp_f32 v40, v40\n" \
    "v_exp_f32 v41, v41\n" \
    "v_cvt_pk_bf16_f32 v67, v38, v39\n" \
    "v_mfma_f32_16x16x32_bf16 v[104:107], v[96:99], v[72:75], v[104:107]\n" \
    "v_exp_f32 v42, v42\n" \
    "v_exp_f32 v43, v43\n" \
    "v_mfma_f32_32x32x16_bf16 v[16:31], v[120:123], v[76:79], v[16:31]\n" \
    "v_cvt_pk_bf16_f32 v68, v40, v41\n" \
    "ds_read_b128 v[120:123], v124 offset:3072\n" \
    "v_exp_f32 v44, v44\n" \
    "v_exp_f32 v45, v45\n" \
    "v_cvt_pk_bf16_f32 v69, v42, v43\n" \
    "v_exp_f32 v46, v46\n" \
    "v_mfma_f32_16x16x32_bf16 v[104:107], v[96:99], v[76:79], v[104:107]\n" \
    "v_exp_f32 v47, v47\n" \
    "v_cvt_pk_bf16_f32 v70, v44, v45\n" \
    "v_cvt_pk_bf16_f32 v71, v46, v47\n" \
    "s_waitcnt lgkmcnt(2)\n" \
    "v_mfma_f32_32x32x16_bf16 v[32:47], v[108:111], v[80:83], 0\n" \
    "v_exp_f32 v48, v48\n" \
    "v_exp_f32 v49, v49\n" \
    "v_exp_f32 v50, v50\n" \
    "v_exp_f32 v51, v51\n" \
    "v_mfma_f32_32x32x16_bf16 v[32:47], v[112:115], v[84:87], v[32:47]\n" \
    "v_cvt_pk_bf16_f32 v72, v48, v49\n" \
    "v_cvt_pk_bf16_f32 v73, v50, v51\n" \
    "v_exp_f32 v52, v52\n" \
    "v_exp_f32 v53, v53\n" \
    "v_exp_f32 v54, v54\n" \
    "s_waitcnt lgkmcnt(1)\n" \
    "v_mfma_f32_32x32x16_bf16 v[0:15], v[116:119], v[64:67], v[0:15]\n" \
    "v_exp_f32 v55, v55\n" \
    "v_cvt_pk_bf16_f32 v74, v52, v53\n" \
    "v_exp_f32 v56, v56\n" \
    "v_exp_f32 v57, v57\n" \
    "v_cvt_pk_bf16_f32 v75, v54, v55\n" \
    "v_mfma_f32_16x16x32_bf16 v[100:103], v[96:99], v[64:67], v[100:103]\n" \
    "v_exp_f32 v58, v58\n" \
    "v_exp_f32 v59, v59\n" \
    "s_waitcnt lgkmcnt(0)\n" \
    "v_mfma_f32_32x32x16_bf16 v[0:15], v[120:123], v[68:71], v[0:15]\n" \
    "v_cvt_pk_bf16_f32 v76, v56, v57\n" \
    "v_exp_f32 v60, v60\n" \
    "v_exp_f32 v61, v61\n" \
    "v_cvt_pk_bf16_f32 v77, v58, v59\n" \
    "v_exp_f32 v62, v62\n" \
    "v_mfma_f32_16x16x32_bf16 v[100:103], v[96:99], v[68:71], v[100:103]\n" \
    "v_exp_f32 v63, v63\n" \
    "v_cvt_pk_bf16_f32 v78, v60, v61\n" \
    "v_cvt_pk_bf16_f32 v79, v62, v63\n" \
    "v_mfma_f32_32x32x16_bf16 v[48:63], v[108:111], v[88:91], 0\n" \
    "v_exp_f32 v32, v32\n" \
    "ds_read_b128 v[108:111], v124 offset:4096\n" \
    "v_exp_f32 v33, v33\n" \
    "v_exp_f32 v34, v34\n" \
    "v_exp_f32 v35, v35\n" \
    "v_mfma_f32_32x32x16_bf16 v[48:63], v[112:115], v[92:95], v[48:63]\n" \
    "v_cvt_pk_bf16_f32 v64, v32, v33\n" \
    "ds_read_b128 v[112:115], v124 offset:5120\n" \
    "v_cvt_pk_bf16_f32 v65, v34, v35\n" \
    "v_exp_f32 v36, v36\n" \
    "v_exp_f32 v37, v37\n" \
    "v_exp_f32 v38, v38\n" \
    "v_mfma_f32_32x32x16_bf16 v[16:31], v[116:119], v[72:75], v[16:31]\n" \
    "v_exp_f32 v39, v39\n" \
    "ds_read_b128 v[116:119], v124 offset:6144\n" \
    "v_cvt_pk_bf16_f32 v66, v36, v37\n" \
    "v_exp_f32 v40, v40\n" \
    "v_exp_f32 v41, v41\n" \
    "v_cvt_pk_bf16_f32 v67, v38, v39\n" \
    "v_mfma_f32_16x16x32_bf16 v[104:107], v[96:99], v[72:75], v[104:107]\n" \
    "v_exp_f32 v42, v42\n" \
    "v_exp_f32 v43, v43\n" \
    "v_mfma_f32_32x32x16_bf16 v[16:31], v[120:123], v[76:79], v[16:31]\n" \
    "v_cvt_pk_bf16_f32 v68, v40, v41\n" \
    "ds_read_b128 v[120:123], v124 offset:7168\n" \
    "v_exp_f32 v44, v44\n" \
    "v_exp_f32 v45, v45\n" \
    "v_cvt_pk_bf16_f32 v69, v42, v43\n" \
    "v_exp_f32 v46, v46\n" \
    "v_mfma_f32_16x16x32_bf16 v[104:107], v[96:99], v[76:79], v[104:107]\n" \
    "v_exp_f32 v47, v47\n" \
    "v_cvt_pk_bf16_f32 v70, v44, v45\n" \
    "v_cvt_pk_bf16_f32 v71, v46, v47\n" \
    "s_waitcnt lgkmcnt(2)\n" \
    "v_mfma_f32_32x32x16_bf16 v[32:47], v[108:111], v[80:83], 0\n" \
    "v_exp_f32 v48, v48\n" \
    "v_exp_f32 v49, v49\n" \
    "v_exp_f32 v50, v50\n" \
    "v_exp_f32 v51, v51\n" \
    "v_mfma_f32_32x32x16_bf16 v[32:47], v[112:115], v[84:87], v[32:47]\n" \
    "v_cvt_pk_bf16_f32 v72, v48, v49\n" \
    "v_cvt_pk_bf16_f32 v73, v50, v51\n" \
    "v_exp_f32 v52, v52\n" \
    "v_exp_f32 v53, v53\n" \
    "v_exp_f32 v54, v54\n" \
    "s_waitcnt lgkmcnt(1)\n" \
    "v_mfma_f32_32x32x16_bf16 v[0:15], v[116:119], v[64:67], v[0:15]\n" \
    "v_exp_f32 v55, v55\n" \
    "v_cvt_pk_bf16_f32 v74, v52, v53\n" \
    "v_exp_f32 v56, v56\n" \
    "v_exp_f32 v57, v57\n" \
    "v_cvt_pk_bf16_f32 v75, v54, v55\n" \
    "v_mfma_f32_16x16x32_bf16 v[100:103], v[96:99], v[64:67], v[100:103]\n" \
    "v_exp_f32 v58, v58\n" \
    "v_exp_f32 v59, v59\n" \
    "s_waitcnt lgkmcnt(0)\n" \
    "v_mfma_f32_32x32x16_bf16 v[0:15], v[120:123], v[68:71], v[0:15]\n" \
    "v_cvt_pk_bf16_f32 v76, v56, v57\n" \
    "v_exp_f32 v60, v60\n" \
    "v_exp_f32 v61, v61\n" \
    "v_cvt_pk_bf16_f32 v77, v58, v59\n" \
    "v_exp_f32 v62, v62\n" \
    "v_mfma_f32_16x16x32_bf16 v[100:103], v[96:99], v[68:71], v[100:103]\n" \
    "v_exp_f32 v63, v63\n" \
    "v_cvt_pk_bf16_f32 v78, v60, v61\n" \
    "v_cvt_pk_bf16_f32 v79, v62, v63\n" \
    "v_mfma_f32_32x32x16_bf16 v[48:63], v[108:111], v[88:91], 0\n" \
    "v_exp_f32 v32, v32\n" \
    "ds_read_b128 v[108:111], v124 offset:8192\n" \
    "v_exp_f32 v33, v33\n" \
    "v_exp_f32 v34, v34\n" \
    "v_exp_f32 v35, v35\n" \
    "v_mfma_f32_32x32x16_bf16 v[48:63], v[112:115], v[92:95], v[48:63]\n" \
    "v_cvt_pk_bf16_f32 v64, v32, v33\n" \
    "ds_read_b128 v[112:115], v124 offset:9216\n" \
    "v_cvt_pk_bf16_f32 v65, v34, v35\n" \
    "v_exp_f32 v36, v36\n" \
    "v_exp_f32 v37, v37\n" \
    "v_exp_f32 v38, v38\n" \
    "v_mfma_f32_32x32x16_bf16 v[16:31], v[116:119], v[72:75], v[16:31]\n" \
    "v_exp_f32 v39, v39\n" \
    "ds_read_b128 v[116:119], v124 offset:10240\n" \
    "v_cvt_pk_bf16_f32 v66, v36, v37\n" \
    "v_exp_f32 v40, v40\n" \
    "v_exp_f32 v41, v41\n" \
    "v_cvt_pk_bf16_f32 v67, v38, v39\n" \
    "v_mfma_f32_16x16x32_bf16 v[104:107], v[96:99], v[72:75], v[104:107]\n" \
    "v_exp_f32 v42, v42\n" \
    "v_exp_f32 v43, v43\n" \
    "v_mfma_f32_32x32x16_bf16 v[16:31], v[120:123], v[76:79], v[16:31]\n" \
    "v_cvt_pk_bf16_f32 v68, v40, v41\n" \
    "ds_read_b128 v[120:123], v124 offset:11264\n" \
    "v_exp_f32 v44, v44\n" \
    "v_exp_f32 v45, v45\n" \
    "v_cvt_pk_bf16_f32 v69, v42, v43\n" \
    "v_exp_f32 v46, v46\n" \
    "v_mfma_f32_16x16x32_bf16 v[104:107], v[96:99], v[76:79], v[104:107]\n" \
    "v_exp_f32 v47, v47\n" \
    "v_cvt_pk_bf16_f32 v70, v44, v45\n" \
    "v_cvt_pk_bf16_f32 v71, v46, v47\n" \
    "s_waitcnt lgkmcnt(2)\n" \
    "v_mfma_f32_32x32x16_bf16 v[32:47], v[108:111], v[80:83], 0\n" \
    "v_exp_f32 v48, v48\n" \
    "v_exp_f32 v49, v49\n" \
    "v_exp_f32 v50, v50\n" \
    "v_exp_f32 v51, v51\n" \
    "v_mfma_f32_32x32x16_bf16 v[32:47], v[112:115], v[84:87], v[32:47]\n" \
    "v_cvt_pk_bf16_f32 v72, v48, v49\n" \
    "v_cvt_pk_bf16_f32 v73, v50, v51\n" \
    "v_exp_f32 v52, v52\n" \
    "v_exp_f32 v53, v53\n" \
    "v_exp_f32 v54, v54\n" \
    "s_waitcnt lgkmcnt(1)\n" \
    "v_mfma_f32_32x32x16_bf16 v[0:15], v[116:119], v[64:67], v[0:15]\n" \
    "v_exp_f32 v55, v55\n" \
    "v_cvt_pk_bf16_f32 v74, v52, v53\n" \
    "v_exp_f32 v56, v56\n" \
    "v_exp_f32 v57, v57\n" \
    "v_cvt_pk_bf16_f32 v75, v54, v55\n" \
    "v_mfma_f32_16x16x32_bf16 v[100:103], v[96:99], v[64:67], v[100:103]\n" \
    "v_exp_f32 v58, v58\n" \
    "v_exp_f32 v59, v59\n" \
    "s_waitcnt lgkmcnt(0)\n" \
    "v_mfma_f32_32x32x16_bf16 v[0:15], v[120:123], v[68:71], v[0:15]\n" \
    "v_cvt_pk_bf16_f32 v76, v56, v57\n" \
    "v_exp_f32 v60, v60\n" \
    "v_exp_f32 v61, v61\n" \
    "v_cvt_pk_bf16_f32 v77, v58, v59\n" \
    "v_exp_f32 v62, v62\n" \
    "v_mfma_f32_16x16x32_bf16 v[100:103], v[96:99], v[68:71], v[100:103]\n" \
    "v_exp_f32 v63, v63\n" \
    "v_cvt_pk_bf16_f32 v78, v60, v61\n" \
    "v_cvt_pk_bf16_f32 v79, v62, v63\n" \
    "v_mfma_f32_32x32x16_bf16 v[48:63], v[108:111], v[88:91], 0\n" \
    "v_exp_f32 v32, v32\n" \
    "ds_read_b128 v[108:111], v124 offset:12288\n" \
    "v_exp_f32 v33, v33\n" \
    "v_exp_f32 v34, v34\n" \
    "v_exp_f32 v35, v35\n" \
    "v_mfma_f32_32x32x16_bf16 v[48:63], v[112:115], v[92:95], v[48:63]\n" \
    "v_cvt_pk_bf16_f32 v64, v32, v33\n" \
    "ds_read_b128 v[112:115], v124 offset:13312\n" \
    "v_cvt_pk_bf16_f32 v65, v34, v35\n" \
    "v_exp_f32 v36, v36\n" \
    "v_exp_f32 v37, v37\n" \
    "v_exp_f32 v38, v38\n" \
    "v_mfma_f32_32x32x16_bf16 v[16:31], v[116:119], v[72:75], v[16:31]\n" \
    "v_exp_f32 v39, v39\n" \
    "ds_read_b128 v[116:119], v124 offset:14336\n" \
    "v_cvt_pk_bf16_f32 v66, v36, v37\n" \
    "v_exp_f32 v40, v40\n" \
    "v_exp_f32 v41, v41\n" \
    "v_cvt_pk_bf16_f32 v67, v38, v39\n" \
    "v_mfma_f32_16x16x32_bf16 v[104:107], v[96:99], v[72:75], v[104:107]\n" \
    "v_exp_f32 v42, v42\n" \
    "v_exp_f32 v43, v43\n" \
    "v_mfma_f32_32x32x16_bf16 v[16:31], v[120:123], v[76:79], v[16:31]\n" \
    "v_cvt_pk_bf16_f32 v68, v40, v41\n" \
    "ds_read_b128 v[120:123], v124 offset:15360\n" \
    "v_exp_f32 v44, v44\n" \
    "v_exp_f32 v45, v45\n" \
    "v_cvt_pk_bf16_f32 v69, v42, v43\n" \
    "v_exp_f32 v46, v46\n" \
    "v_mfma_f32_16x16x32_bf16 v[104:107], v[96:99], v[76:79], v[104:107]\n" \
    "v_exp_f32 v47, v47\n" \
    "v_cvt_pk_bf16_f32 v70, v44, v45\n" \
    "v_cvt_pk_bf16_f32 v71, v46, v47\n" \
    "s_waitcnt lgkmcnt(2)\n" \
    "v_mfma_f32_32x32x16_bf16 v[32:47], v[108:111], v[80:83], 0\n" \
    "v_exp_f32 v48, v48\n" \
    "v_exp_f32 v49, v49\n" \
    "v_exp_f32 v50, v50\n" \
    "v_exp_f32 v51, v51\n" \
    "v_mfma_f32_32x32x16_bf16 v[32:47], v[112:115], v[84:87], v[32:47]\n" \
    "v_cvt_pk_bf16_f32 v72, v48, v49\n" \
    "v_cvt_pk_bf16_f32 v73, v50, v51\n" \
    "v_exp_f32 v52, v52\n" \
    "v_exp_f32 v53, v53\n" \
    "v_exp_f32 v54, v54\n" \
    "s_waitcnt lgkmcnt(1)\n" \
    "v_mfma_f32_32x32x16_bf16 v[0:15], v[116:119], v[64:67], v[0:15]\n" \
    "v_exp_f32 v55, v55\n" \
    "v_cvt_pk_bf16_f32 v74, v52, v53\n" \
    "v_exp_f32 v56, v56\n" \
    "v_exp_f32 v57, v57\n" \
    "v_cvt_pk_bf16_f32 v75, v54, v55\n" \
    "v_mfma_f32_16x16x32_bf16 v[100:103], v[96:99], v[64:67], v[100:103]\n" \
    "v_exp_f32 v58, v58\n" \
    "v_exp_f32 v59, v59\n" \
    "s_waitcnt lgkmcnt(0)\n" \
    "v_mfma_f32_32x32x16_bf16 v[0:15], v[120:123], v[68:71], v[0:15]\n" \
    "v_cvt_pk_bf16_f32 v76, v56, v57\n" \
    "v_exp_f32 v60, v60\n" \
    "v_exp_f32 v61, v61\n" \
    "v_cvt_pk_bf16_f32 v77, v58, v59\n" \
    "v_exp_f32 v62, v62\n" \
    "v_mfma_f32_16x16x32_bf16 v[100:103], v[96:99], v[68:71], v[100:103]\n" \
    "v_exp_f32 v63, v63\n" \
    "v_cvt_pk_bf16_f32 v78, v60, v61\n" \
    "v_cvt_pk_bf16_f32 v79, v62, v63\n" \
    "v_mfma_f32_32x32x16_bf16 v[48:63], v[108:111], v[88:91], 0\n" \
    "v_exp_f32 v32, v32\n" \
    "ds_read_b128 v[108:111], v124 offset:16384\n" \
    "v_exp_f32 v33, v33\n" \
    "v_exp_f32 v34, v34\n" \
    "v_exp_f32 v35, v35\n" \
    "v_mfma_f32_32x32x16_bf16 v[48:63], v[112:115], v[92:95], v[48:63]\n" \
    "v_cvt_pk_bf16_f32 v64, v32, v33\n" \
    "ds_read_b128 v[112:115], v124 offset:17408\n" \
    "v_cvt_pk_bf16_f32 v65, v34, v35\n" \
    "v_exp_f32 v36, v36\n" \
    "v_exp_f32 v37, v37\n" \
    "v_exp_f32 v38, v38\n" \
    "v_mfma_f32_32x32x16_bf16 v[16:31], v[116:119], v[72:75], v[16:31]\n" \
    "v_exp_f32 v39, v39\n" \
    "ds_read_b128 v[116:119], v124 offset:18432\n" \
    "v_cvt_pk_bf16_f32 v66, v36, v37\n" \
    "v_exp_f32 v40, v40\n" \
    "v_exp_f32 v41, v41\n" \
    "v_cvt_pk_bf16_f32 v67, v38, v39\n" \
    "v_mfma_f32_16x16x32_bf16 v[104:107], v[96:99], v[72:75], v[104:107]\n" \
    "v_exp_f32 v42, v42\n" \
    "v_exp_f32 v43, v43\n" \
    "v_mfma_f32_32x32x16_bf16 v[16:31], v[120:123], v[76:79], v[16:31]\n" \
    "v_cvt_pk_bf16_f32 v68, v40, v41\n" \
    "ds_read_b128 v[120:123], v124 offset:19456\n" \
    "v_exp_f32 v44, v44\n" \
    "v_exp_f32 v45, v45\n" \
    "v_cvt_pk_bf16_f32 v69, v42, v43\n" \
    "v_exp_f32 v46, v46\n" \
    "v_mfma_f32_16x16x32_bf16 v[104:107], v[96:99], v[76:79], v[104:107]\n" \
    "v_exp_f32 v47, v47\n" \
    "v_cvt_pk_bf16_f32 v70, v44, v45\n" \
    "v_cvt_pk_bf16_f32 v71, v46, v47\n" \
    "s_waitcnt lgkmcnt(2)\n" \
    "v_mfma_f32_32x32x16_bf16 v[32:47], v[108:111], v[80:83], 0\n" \
    "v_exp_f32 v48, v48\n" \
    "v_exp_f32 v49, v49\n" \
    "v_exp_f32 v50, v50\n" \
    "v_exp_f32 v51, v51\n" \
    "v_mfma_f32_32x32x16_bf16 v[32:47], v[112:115], v[84:87], v[32:47]\n" \
    "v_cvt_pk_bf16_f32 v72, v48, v49\n" \
    "v_cvt_pk_bf16_f32 v73, v50, v51\n" \
    "v_exp_f32 v52, v52\n" \
    "v_exp_f32 v53, v53\n" \
    "v_exp_f32 v54, v54\n" \
    "s_waitcnt lgkmcnt(1)\n" \
    "v_mfma_f32_32x32x16_bf16 v[0:15], v[116:119], v[64:67], v[0:15]\n" \
    "v_exp_f32 v55, v55\n" \
    "v_cvt_pk_bf16_f32 v74, v52, v53\n" \
    "v_exp_f32 v56, v56\n" \
    "v_exp_f32 v57, v57\n" \
    "v_cvt_pk_bf16_f32 v75, v54, v55\n" \
    "v_mfma_f32_16x16x32_bf16 v[100:103], v[96:99], v[64:67], v[100:103]\n" \
    "v_exp_f32 v58, v58\n" \
    "v_exp_f32 v59, v59\n" \
    "s_waitcnt lgkmcnt(0)\n" \
    "v_mfma_f32_32x32x16_bf16 v[0:15], v[120:123], v[68:71], v[0:15]\n" \
    "v_cvt_pk_bf16_f32 v76, v56, v57\n" \
    "v_exp_f32 v60, v60\n" \
    "v_exp_f32 v61, v61\n" \
    "v_cvt_pk_bf16_f32 v77, v58, v59\n" \
    "v_exp_f32 v62, v62\n" \
    "v_mfma_f32_16x16x32_bf16 v[100:103], v[96:99], v[68:71], v[100:103]\n" \
    "v_exp_f32 v63, v63\n" \
    "v_cvt_pk_bf16_f32 v78, v60, v61\n" \
    "v_cvt_pk_bf16_f32 v79, v62, v63\n" \
    "v_mfma_f32_32x32x16_bf16 v[48:63], v[108:111], v[88:91], 0\n" \
    "v_exp_f32 v32, v32\n" \
    "ds_read_b128 v[108:111], v124 offset:20480\n" \
    "v_exp_f32 v33, v33\n" \
    "v_exp_f32 v34, v34\n" \
    "v_exp_f32 v35, v35\n" \
    "v_mfma_f32_32x32x16_bf16 v[48:63], v[112:115], v[92:95], v[48:63]\n" \
    "v_cvt_pk_bf16_f32 v64, v32, v33\n" \
    "ds_read_b128 v[112:115], v124 offset:21504\n" \
    "v_cvt_pk_bf16_f32 v65, v34, v35\n" \
    "v_exp_f32 v36, v36\n" \
    "v_exp_f32 v37, v37\n" \
    "v_exp_f32 v38, v38\n" \
    "v_mfma_f32_32x32x16_bf16 v[16:31], v[116:119], v[72:75], v[16:31]\n" \
    "v_exp_f32 v39, v39\n" \
    "ds_read_b128 v[116:119], v124 offset:22528\n" \
    "v_cvt_pk_bf16_f32 v66, v36, v37\n" \
    "v_exp_f32 v40, v40\n" \
    "v_exp_f32 v41, v41\n" \
    "v_cvt_pk_bf16_f32 v67, v38, v39\n" \
    "v_mfma_f32_16x16x32_bf16 v[104:107], v[96:99], v[72:75], v[104:107]\n" \
    "v_exp_f32 v42, v42\n" \
    "v_exp_f32 v43, v43\n" \
    "v_mfma_f32_32x32x16_bf16 v[16:31], v[120:123], v[76:79], v[16:31]\n" \
    "v_cvt_pk_bf16_f32 v68, v40, v41\n" \
    "ds_read_b128 v[120:123], v124 offset:23552\n" \
    "v_exp_f32 v44, v44\n" \
    "v_exp_f32 v45, v45\n" \
    "v_cvt_pk_bf16_f32 v69, v42, v43\n" \
    "v_exp_f32 v46, v46\n" \
    "v_mfma_f32_16x16x32_bf16 v[104:107], v[96:99], v[76:79], v[104:107]\n" \
    "v_exp_f32 v47, v47\n" \
    "v_cvt_pk_bf16_f32 v70, v44, v45\n" \
    "v_cvt_pk_bf16_f32 v71, v46, v47\n" \
    "s_waitcnt lgkmcnt(2)\n" \
    "v_mfma_f32_32x32x16_bf16 v[32:47], v[108:111], v[80:83], 0\n" \
    "v_exp_f32 v48, v48\n" \
    "v_exp_f32 v49, v49\n" \
    "v_exp_f32 v50, v50\n" \
    "v_exp_f32 v51, v51\n" \
    "v_mfma_f32_32x32x16_bf16 v[32:47], v[112:115], v[84:87], v[32:47]\n" \
    "v_cvt_pk_bf16_f32 v72, v48, v49\n" \
    "v_cvt_pk_bf16_f32 v73, v50, v51\n" \
    "v_exp_f32 v52, v52\n" \
    "v_exp_f32 v53, v53\n" \
    "v_exp_f32 v54, v54\n" \
    "s_waitcnt lgkmcnt(1)\n" \
    "v_mfma_f32_32x32x16_bf16 v[0:15], v[116:119], v[64:67], v[0:15]\n" \
    "v_exp_f32 v55, v55\n" \
    "v_cvt_pk_bf16_f32 v74, v52, v53\n" \
    "v_exp_f32 v56, v56\n" \
    "v_exp_f32 v57, v57\n" \
    "v_cvt_pk_bf16_f32 v75, v54, v55\n" \
    "v_mfma_f32_16x16x32_bf16 v[100:103], v[96:99], v[64:67], v[100:103]\n" \
    "v_exp_f32 v58, v58\n" \
    "v_exp_f32 v59, v59\n" \
    "s_waitcnt lgkmcnt(0)\n" \
    "v_mfma_f32_32x32x16_bf16 v[0:15], v[120:123], v[68:71], v[0:15]\n" \
    "v_cvt_pk_bf16_f32 v76, v56, v57\n" \
    "v_exp_f32 v60, v60\n" \
    "v_exp_f32 v61, v61\n" \
    "v_cvt_pk_bf16_f32 v77, v58, v59\n" \
    "v_exp_f32 v62, v62\n" \
    "v_mfma_f32_16x16x32_bf16 v[100:103], v[96:99], v[68:71], v[100:103]\n" \
    "v_exp_f32 v63, v63\n" \
    "v_cvt_pk_bf16_f32 v78, v60, v61\n" \
    "v_cvt_pk_bf16_f32 v79, v62, v63\n" \
    "v_mfma_f32_32x32x16_bf16 v[48:63], v[108:111], v[88:91], 0\n" \
    "v_exp_f32 v32, v32\n" \
    "ds_read_b128 v[108:111], v125 offset:16384\n" \
    "v_exp_f32 v33, v33\n" \
    "v_exp_f32 v34, v34\n" \
    "v_exp_f32 v35, v35\n" \
    "v_mfma_f32_32x32x16_bf16 v[48:63], v[112:115], v[92:95], v[48:63]\n" \
    "v_cvt_pk_bf16_f32 v64, v32, v33\n" \
    "ds_read_b128 v[112:115], v125 offset:17408\n" \
    "v_cvt_pk_bf16_f32 v65, v34, v35\n" \
    "v_exp_f32 v36, v36\n" \
    "v_exp_f32 v37, v37\n" \
    "v_exp_f32 v38, v38\n" \
    "v_mfma_f32_32x32x16_bf16 v[16:31], v[116:119], v[72:75], v[16:31]\n" \
    "v_exp_f32 v39, v39\n" \
    "ds_read_b128 v[116:119], v125 offset:18432\n" \
    "v_cvt_pk_bf16_f32 v66, v36, v37\n" \
    "v_exp_f32 v40, v40\n" \
    "v_exp_f32 v41, v41\n" \
    "v_cvt_pk_bf16_f32 v67, v38, v39\n" \
    "v_mfma_f32_16x16x32_bf16 v[104:107], v[96:99], v[72:75], v[104:107]\n" \
    "v_exp_f32 v42, v42\n" \
    "v_exp_f32 v43, v43\n" \
    "v_mfma_f32_32x32x16_bf16 v[16:31], v[120:123], v[76:79], v[16:31]\n" \
    "v_cvt_pk_bf16_f32 v68, v40, v41\n" \
    "ds_read_b128 v[120:123], v125 offset:19456\n" \
    "v_exp_f32 v44, v44\n" \
    "v_exp_f32 v45, v45\n" \
    "v_cvt_pk_bf16_f32 v69, v42, v43\n" \
    "v_exp_f32 v46, v46\n" \
    "v_mfma_f32_16x16x32_bf16 v[104:107], v[96:99], v[76:79], v[104:107]\n" \
    "v_exp_f32 v47, v47\n" \
    "v_cvt_pk_bf16_f32 v70, v44, v45\n" \
    "v_cvt_pk_bf16_f32 v71, v46, v47\n" \
    "s_waitcnt lgkmcnt(2)\n" \
    "v_mfma_f32_32x32x16_bf16 v[32:47], v[108:111], v[80:83], 0\n" \
    "v_exp_f32 v48, v48\n" \
    "v_exp_f32 v49, v49\n" \
    "v_exp_f32 v50, v50\n" \
    "v_exp_f32 v51, v51\n" \
    "v_mfma_f32_32x32x16_bf16 v[32:47], v[112:115], v[84:87], v[32:47]\n" \
    "v_cvt_pk_bf16_f32 v72, v48, v49\n" \
    "v_cvt_pk_bf16_f32 v73, v50, v51\n" \
    "v_exp_f32 v52, v52\n" \
    "v_exp_f32 v53, v53\n" \
    "v_exp_f32 v54, v54\n" \
    "s_waitcnt lgkmcnt(1)\n" \
    "v_mfma_f32_32x32x16_bf16 v[0:15], v[116:119], v[64:67], v[0:15]\n" \
    "v_exp_f32 v55, v55\n" \
    "v_cvt_pk_bf16_f32 v74, v52, v53\n" \
    "v_exp_f32 v56, v56\n" \
    "v_exp_f32 v57, v57\n" \
    "v_cvt_pk_bf16_f32 v75, v54, v55\n" \
    "v_mfma_f32_16x16x32_bf16 v[100:103], v[96:99], v[64:67], v[100:103]\n" \
    "v_exp_f32 v58, v58\n" \
    "v_exp_f32 v59, v59\n" \
    "s_waitcnt lgkmcnt(0)\n" \
    "v_mfma_f32_32x32x16_bf16 v[0:15], v[120:123], v[68:71], v[0:15]\n" \
    "v_cvt_pk_bf16_f32 v76, v56, v57\n" \
    "v_exp_f32 v60, v60\n" \
    "v_exp_f32 v61, v61\n" \
    "v_cvt_pk_bf16_f32 v77, v58, v59\n" \
    "v_exp_f32 v62, v62\n" \
    "v_mfma_f32_16x16x32_bf16 v[100:103], v[96:99], v[68:71], v[100:103]\n" \
    "v_exp_f32 v63, v63\n" \
    "v_cvt_pk_bf16_f32 v78, v60, v61\n" \
    "v_cvt_pk_bf16_f32 v79, v62, v63\n" \
    "v_mfma_f32_32x32x16_bf16 v[48:63], v[108:111], v[88:91], 0\n" \
    "v_exp_f32 v32, v32\n" \
    "ds_read_b128 v[108:111], v125 offset:20480\n" \
    "v_exp_f32 v33, v33\n" \
    "v_exp_f32 v34, v34\n" \
    "v_exp_f32 v35, v35\n" \
    "v_mfma_f32_32x32x16_bf16 v[48:63], v[112:115], v[92:95], v[48:63]\n" \
    "v_cvt_pk_bf16_f32 v64, v32, v33\n" \
    "ds_read_b128 v[112:115], v125 offset:21504\n" \
    "v_cvt_pk_bf16_f32 v65, v34, v35\n" \
    "v_exp_f32 v36, v36\n" \
    "v_exp_f32 v37, v37\n" \
    "v_exp_f32 v38, v38\n" \
    "v_mfma_f32_32x32x16_bf16 v[16:31], v[116:119], v[72:75], v[16:31]\n" \
    "v_exp_f32 v39, v39\n" \
    "ds_read_b128 v[116:119], v125 offset:22528\n" \
    "v_cvt_pk_bf16_f32 v66, v36, v37\n" \
    "v_exp_f32 v40, v40\n" \
    "v_exp_f32 v41, v41\n" \
    "v_cvt_pk_bf16_f32 v67, v38, v39\n" \
    "v_mfma_f32_16x16x32_bf16 v[104:107], v[96:99], v[72:75], v[104:107]\n" \
    "v_exp_f32 v42, v42\n" \
    "v_exp_f32 v43, v43\n" \
    "v_mfma_f32_32x32x16_bf16 v[16:31], v[120:123], v[76:79], v[16:31]\n" \
    "v_cvt_pk_bf16_f32 v68, v40, v41\n" \
    "ds_read_b128 v[120:123], v125 offset:23552\n" \
    "v_exp_f32 v44, v44\n" \
    "v_exp_f32 v45, v45\n" \
    "v_cvt_pk_bf16_f32 v69, v42, v43\n" \
    "v_exp_f32 v46, v46\n" \
    "v_mfma_f32_16x16x32_bf16 v[104:107], v[96:99], v[76:79], v[104:107]\n" \
    "v_exp_f32 v47, v47\n" \
    "v_cvt_pk_bf16_f32 v70, v44, v45\n" \
    "v_cvt_pk_bf16_f32 v71, v46, v47\n" \
    "s_waitcnt lgkmcnt(2)\n" \
    "v_mfma_f32_32x32x16_bf16 v[32:47], v[108:111], v[80:83], 0\n" \
    "v_exp_f32 v48, v48\n" \
    "v_exp_f32 v49, v49\n" \
    "v_exp_f32 v50, v50\n" \
    "v_exp_f32 v51, v51\n" \
    "v_mfma_f32_32x32x16_bf16 v[32:47], v[112:115], v[84:87], v[32:47]\n" \
    "v_cvt_pk_bf16_f32 v72, v48, v49\n" \
    "v_cvt_pk_bf16_f32 v73, v50, v51\n" \
    "v_exp_f32 v52, v52\n" \
    "v_exp_f32 v53, v53\n" \
    "v_exp_f32 v54, v54\n" \
    "s_waitcnt lgkmcnt(1)\n" \
    "v_mfma_f32_32x32x16_bf16 v[0:15], v[116:119], v[64:67], v[0:15]\n" \
    "v_exp_f32 v55, v55\n" \
    "v_cvt_pk_bf16_f32 v74, v52, v53\n" \
    "v_exp_f32 v56, v56\n" \
    "v_exp_f32 v57, v57\n" \
    "v_cvt_pk_bf16_f32 v75, v54, v55\n" \
    "v_mfma_f32_16x16x32_bf16 v[100:103], v[96:99], v[64:67], v[100:103]\n" \
    "v_exp_f32 v58, v58\n" \
    "v_exp_f32 v59, v59\n" \
    "s_waitcnt lgkmcnt(0)\n" \
    "v_mfma_f32_32x32x16_bf16 v[0:15], v[120:123], v[68:71], v[0:15]\n" \
    "v_cvt_pk_bf16_f32 v76, v56, v57\n" \
    "v_exp_f32 v60, v60\n" \
    "v_exp_f32 v61, v61\n" \
    "v_cvt_pk_bf16_f32 v77, v58, v59\n" \
    "v_exp_f32 v62, v62\n" \
    "v_mfma_f32_16x16x32_bf16 v[100:103], v[96:99], v[68:71], v[100:103]\n" \
    "v_exp_f32 v63, v63\n" \
    "v_cvt_pk_bf16_f32 v78, v60, v61\n" \
    "v_cvt_pk_bf16_f32 v79, v62, v63\n" \
    "s_barrier\n" \
    "v_mfma_f32_32x32x16_bf16 v[48:63], v[108:111], v[88:91], 0\n" \
    "v_exp_f32 v32, v32\n" \
    "ds_read_b128 v[108:111], v125 offset:40960\n" \
    "v_exp_f32 v33, v33\n" \
    "v_exp_f32 v34, v34\n" \
    "v_exp_f32 v35, v35\n" \
    "v_mfma_f32_32x32x16_bf16 v[48:63], v[112:115], v[92:95], v[48:63]\n" \
    "v_cvt_pk_bf16_f32 v64, v32, v33\n" \
    "ds_read_b128 v[112:115], v125 offset:41984\n" \
    "v_cvt_pk_bf16_f32 v65, v34, v35\n" \
    "v_exp_f32 v36, v36\n" \
    "v_exp_f32 v37, v37\n" \
    "v_exp_f32 v38, v38\n" \
    "v_mfma_f32_32x32x16_bf16 v[16:31], v[116:119], v[72:75], v[16:31]\n" \
    "v_exp_f32 v39, v39\n" \
    "ds_read_b128 v[116:119], v125 offset:43008\n" \
    "v_cvt_pk_bf16_f32 v66, v36, v37\n" \
    "v_exp_f32 v40, v40\n" \
    "v_exp_f32 v41, v41\n" \
    "v_cvt_pk_bf16_f32 v67, v38, v39\n" \
    "v_mfma_f32_16x16x32_bf16 v[104:107], v[96:99], v[72:75], v[104:107]\n" \
    "v_exp_f32 v42, v42\n" \
    "v_exp_f32 v43, v43\n" \
    "v_mfma_f32_32x32x16_bf16 v[16:31], v[120:123], v[76:79], v[16:31]\n" \
    "v_cvt_pk_bf16_f32 v68, v40, v41\n" \
    "ds_read_b128 v[120:123], v125 offset:44032\n" \
    "v_exp_f32 v44, v44\n" \
    "v_exp_f32 v45, v45\n" \
    "v_cvt_pk_bf16_f32 v69, v42, v43\n" \
    "v_exp_f32 v46, v46\n" \
    "v_mfma_f32_16x16x32_bf16 v[104:107], v[96:99], v[76:79], v[104:107]\n" \
    "v_exp_f32 v47, v47\n" \
    "v_cvt_pk_bf16_f32 v70, v44, v45\n" \
    "v_cvt_pk_bf16_f32 v71, v46, v47\n" \
    "s_sub_u32 %[cnt], %[cnt], 1\n" \
    "s_cmp_lg_u32 %[cnt], 0\n" \
    "s_cbranch_scc1 1b\n" \
    "s_waitcnt lgkmcnt(0)\n" \
    "s_nop 15\n"
#define ASM_MIX1 \
    "s_mov_b32 %[cnt], 16\n" \
    "1:\n" \
    "s_waitcnt lgkmcnt(0)\n" \
    "s_barrier\n" \
    "v_mfma_f32_32x32x16_bf16 v[0:15], v[80:83], v[88:91], v[0:15]\n" \
    "ds_read_b64_tr_b16 v[64:65], v96 offset:0\n" \
    "ds_read_b64_tr_b16 v[66:67], v96 offset:1024\n" \
    "v_mfma_f32_32x32x16_bf16 v[16:31], v[80:83], v[92:95], v[16:31]\n" \
    "ds_read_b128 v[72:75], v98 offset:0\n" \
    "ds_read_b128 v[76:79], v98 offset:512\n" \
    "v_mfma_f32_32x32x16_bf16 v[32:47], v[84:87], v[88:91], v[32:47]\n" \
    "ds_read_b64_tr_b16 v[68:69], v97 offset:0\n" \
    "ds_read_b64_tr_b16 v[70:71], v97 offset:1024\n" \
    "v_mfma_f32_32x32x16_bf16 v[48:63], v[84:87], v[92:95], v[48:63]\n" \
    "s_waitcnt lgkmcnt(0)\n" \
    "v_mfma_f32_32x32x16_bf16 v[0:15], v[64:67], v[72:75], v[0:15]\n" \
    "ds_read_b64_tr_b16 v[80:81], v96 offset:4096\n" \
    "ds_read_b64_tr_b16 v[82:83], v96 offset:5120\n" \
    "v_mfma_f32_32x32x16_bf16 v[16:31], v[64:67], v[76:79], v[16:31]\n" \
    "ds_read_b128 v[88:91], v98 offset:4096\n" \
    "ds_read_b128 v[92:95], v98 offset:4608\n" \
    "v_mfma_f32_32x32x16_bf16 v[32:47], v[68:71], v[72:75], v[32:47]\n" \
    "ds_read_b64_tr_b16 v[84:85], v97 offset:4096\n" \
    "ds_read_b64_tr_b16 v[86:87], v97 offset:5120\n" \
    "v_mfma_f32_32x32x16_bf16 v[48:63], v[68:71], v[76:79], v[48:63]\n" \
    "s_waitcnt lgkmcnt(0)\n" \
    "s_barrier\n" \
    "v_mfma_f32_32x32x16_bf16 v[0:15], v[80:83], v[88:91], v[0:15]\n" \
    "ds_read_b64_tr_b16 v[64:65], v96 offset:16384\n" \
    "ds_read_b64_tr_b16 v[66:67], v96 offset:17408\n" \
    "v_mfma_f32_32x32x16_bf16 v[16:31], v[80:83], v[92:95], v[16:31]\n" \
    "ds_read_b128 v[72:75], v98 offset:16384\n" \
    "ds_read_b128 v[76:79], v98 offset:16896\n" \
    "v_mfma_f32_32x32x16_bf16 v[32:47], v[84:87], v[88:91], v[32:47]\n" \
    "ds_read_b64_tr_b16 v[68:69], v97 offset:16384\n" \
    "ds_read_b64_tr_b16 v[70:71], v97 offset:17408\n" \
    "v_mfma_f32_32x32x16_bf16 v[48:63], v[84:87], v[92:95], v[48:63]\n" \
    "s_waitcnt lgkmcnt(0)\n" \
    "v_mfma_f32_32x32x16_bf16 v[0:15], v[64:67], v[72:75], v[0:15]\n" \
    "ds_read_b64_tr_b16 v[80:81], v96 offset:20480\n" \
    "ds_read_b64_tr_b16 v[82:83], v96 offset:21504\n" \
    "v_mfma_f32_32x32x16_bf16 v[16:31], v[64:67], v[76:79], v[16:31]\n" \
    "ds_read_b128 v[88:91], v98 offset:20480\n" \
    "ds_read_b128 v[92:95], v98 offset:20992\n" \
    "v_mfma_f32_32x32x16_bf16 v[32:47], v[68:71], v[72:75], v[32:47]\n" \
    "ds_read_b64_tr_b16 v[84:85], v97 offset:20480\n" \
    "ds_read_b64_tr_b16 v[86:87], v97 offset:21504\n" \
    "v_mfma_f32_32x32x16_bf16 v[48:63], v[68:71], v[76:79], v[48:63]\n" \
    "s_waitcnt lgkmcnt(0)\n" \
    "s_barrier\n" \
    "v_mfma_f32_32x32x16_bf16 v[0:15], v[80:83], v[88:91], v[0:15]\n" \
    "ds_read_b64_tr_b16 v[64:65], v96 offset:32768\n" \
    "ds_read_b64_tr_b16 v[66:67], v96 offset:33792\n" \
    "v_mfma_f32_32x32x16_bf16 v[16:31], v[80:83], v[92:95], v[16:31]\n" \
    "ds_read_b128 v[72:75], v98 offset:32768\n" \
    "ds_read_b128 v[76:79], v98 offset:33280\n" \
    "v_mfma_f32_32x32x16_bf16 v[32:47], v[84:87], v[88:91], v[32:47]\n" \
    "ds_read_b64_tr_b16 v[68:69], v97 offset:32768\n" \
    "ds_read_b64_tr_b16 v[70:71], v97 offset:33792\n" \
    "v_mfma_f32_32x32x16_bf16 v[48:63], v[84:87], v[92:95], v[48:63]\n" \
    "s_waitcnt lgkmcnt(0)\n" \
    "v_mfma_f32_32x32x16_bf16 v[0:15], v[64:67], v[72:75], v[0:15]\n" \
    "ds_read_b64_tr_b16 v[80:81], v96 offset:36864\n" \
    "ds_read_b64_tr_b16 v[82:83], v96 offset:37888\n" \
    "v_mfma_f32_32x32x16_bf16 v[16:31], v[64:67], v[76:79], v[16:31]\n" \
    "ds_read_b128 v[88:91], v98 offset:36864\n" \
    "ds_read_b128 v[92:95], v98 offset:37376\n" \
    "v_mfma_f32_32x32x16_bf16 v[32:47], v[68:71], v[72:75], v[32:47]\n" \
    "ds_read_b64_tr_b16 v[84:85], v97 offset:36864\n" \
    "ds_read_b64_tr_b16 v[86:87], v97 offset:37888\n" \
    "v_mfma_f32_32x32x16_bf16 v[48:63], v[68:71], v[76:79], v[48:63]\n" \
    "s_waitcnt lgkmcnt(0)\n" \
    "s_barrier\n" \
    "v_mfma_f32_32x32x16_bf16 v[0:15], v[80:83], v[88:91], v[0:15]\n" \
    "ds_read_b64_tr_b16 v[64:65], v96 offset:49152\n" \
    "ds_read_b64_tr_b16 v[66:67], v96 offset:50176\n" \
    "v_mfma_f32_32x32x16_bf16 v[16:31], v[80:83], v[92:95], v[16:31]\n" \
    "ds_read_b128 v[72:75], v98 offset:49152\n" \
    "ds_read_b128 v[76:79], v98 offset:49664\n" \
    "v_mfma_f32_32x32x16_bf16 v[32:47], v[84:87], v[88:91], v[32:47]\n" \
    "ds_read_b64_tr_b16 v[68:69], v97 offset:49152\n" \
    "ds_read_b64_tr_b16 v[70:71], v97 offset:50176\n" \
    "v_mfma_f32_32x32x16_bf16 v[48:63], v[84:87], v[92:95], v[48:63]\n" \
    "s_waitcnt lgkmcnt(0)\n" \
    "v_mfma_f32_32x32x16_bf16 v[0:15], v[64:67], v[72:75], v[0:15]\n" \
    "ds_read_b64_tr_b16 v[80:81], v96 offset:53248\n" \
    "ds_read_b64_tr_b16 v[82:83], v96 offset:54272\n" \
    "v_mfma_f32_32x32x16_bf16 v[16:31], v[64:67], v[76:79], v[16:31]\n" \
    "ds_read_b128 v[88:91], v98 offset:53248\n" \
    "ds_read_b128 v[92:95], v98 offset:53760\n" \
    "v_mfma_f32_32x32x16_bf16 v[32:47], v[68:71], v[72:75], v[32:47]\n" \
    "ds_read_b64_tr_b16 v[84:85], v97 offset:53248\n" \
    "ds_read_b64_tr_b16 v[86:87], v97 offset:54272\n" \
    "v_mfma_f32_32x32x16_bf16 v[48:63], v[68:71], v[76:79], v[48:63]\n" \
    "s_sub_u32 %[cnt], %[cnt], 1\n" \
    "s_cmp_lg_u32 %[cnt], 0\n" \
    "s_cbranch_scc1 1b\n" \
    "s_waitcnt lgkmcnt(0)\n" \
    "v_mfma_f32_32x32x16_bf16 v[0:15], v[80:83], v[88:91], v[0:15]\n" \
    "v_mfma_f32_32x32x16_bf16 v[16:31], v[80:83], v[92:95], v[16:31]\n" \
    "v_mfma_f32_32x32x16_bf16 v[32:47], v[84:87], v[88:91], v[32:47]\n" \
    "v_mfma_f32_32x32x16_bf16 v[48:63], v[84:87], v[92:95], v[48:63]\n" \
    "s_nop 15\n"
#define ASM_MIX2 \
    "s_mov_b32 %[cnt], 14\n" \
    "1:\n" \
    "s_waitcnt lgkmcnt(0)\n" \
    "s_barrier\n" \
    "v_mfma_f32_32x32x16_bf16 v[0:15], v[88:91], v[80:83], v[0:15]\n" \
    "ds_read_b64_tr_b16 v[64:65], v96 offset:0\n" \
    "ds_read_b64_tr_b16 v[66:67], v96 offset:2048\n" \
    "v_mfma_f32_32x32x16_bf16 v[32:47], v[92:95], v[80:83], v[32:47]\n" \
    "ds_read_b128 v[72:75], v98 offset:0\n" \
    "ds_read_b128 v[76:79], v98 offset:512\n" \
    "v_mfma_f32_32x32x16_bf16 v[16:31], v[88:91], v[84:87], v[16:31]\n" \
    "ds_read_b64_tr_b16 v[68:69], v97 offset:0\n" \
    "ds_read_b64_tr_b16 v[70:71], v97 offset:2048\n" \
    "v_mfma_f32_32x32x16_bf16 v[48:63], v[92:95], v[84:87], v[48:63]\n" \
    "s_waitcnt lgkmcnt(0)\n" \
    "v_mfma_f32_32x32x16_bf16 v[0:15], v[72:75], v[64:67], v[0:15]\n" \
    "ds_read_b64_tr_b16 v[80:81], v96 offset:4096\n" \
    "ds_read_b64_tr_b16 v[82:83], v96 offset:6144\n" \
    "v_mfma_f32_32x32x16_bf16 v[32:47], v[76:79], v[64:67], v[32:47]\n" \
    "ds_read_b128 v[88:91], v98 offset:4096\n" \
    "ds_read_b128 v[92:95], v98 offset:4608\n" \
    "v_mfma_f32_32x32x16_bf16 v[16:31], v[72:75], v[68:71], v[16:31]\n" \
    "ds_read_b64_tr_b16 v[84:85], v97 offset:4096\n" \
    "ds_read_b64_tr_b16 v[86:87], v97 offset:6144\n" \
    "v_mfma_f32_32x32x16_bf16 v[48:63], v[76:79], v[68:71], v[48:63]\n" \
    "s_waitcnt lgkmcnt(0)\n" \
    "s_barrier\n" \
    "v_mfma_f32_32x32x16_bf16 v[0:15], v[88:91], v[80:83], v[0:15]\n" \
    "ds_read_b64_tr_b16 v[64:65], v96 offset:16384\n" \
    "ds_read_b64_tr_b16 v[66:67], v96 offset:18432\n" \
    "v_mfma_f32_32x32x16_bf16 v[32:47], v[92:95], v[80:83], v[32:47]\n" \
    "ds_read_b128 v[72:75], v98 offset:16384\n" \
    "ds_read_b128 v[76:79], v98 offset:16896\n" \
    "v_mfma_f32_32x32x16_bf16 v[16:31], v[88:91], v[84:87], v[16:31]\n" \
    "ds_read_b64_tr_b16 v[68:69], v97 offset:16384\n" \
    "ds_read_b64_tr_b16 v[70:71], v97 offset:18432\n" \
    "v_mfma_f32_32x32x16_bf16 v[48:63], v[92:95], v[84:87], v[48:63]\n" \
    "s_waitcnt lgkmcnt(0)\n" \
    "v_mfma_f32_32x32x16_bf16 v[0:15], v[72:75], v[64:67], v[0:15]\n" \
    "ds_read_b64_tr_b16 v[80:81], v96 offset:20480\n" \
    "ds_read_b64_tr_b16 v[82:83], v96 offset:22528\n" \
    "v_mfma_f32_32x32x16_bf16 v[32:47], v[76:79], v[64:67], v[32:47]\n" \
    "ds_read_b128 v[88:91], v98 offset:20480\n" \
    "ds_read_b128 v[92:95], v98 offset:20992\n" \
    "v_mfma_f32_32x32x16_bf16 v[16:31], v[72:75], v[68:71], v[16:31]\n" \
    "ds_read_b64_tr_b16 v[84:85], v97 offset:20480\n" \
    "ds_read_b64_tr_b16 v[86:87], v97 offset:22528\n" \
    "v_mfma_f32_32x32x16_bf16 v[48:63], v[76:79], v[68:71], v[48:63]\n" \
    "s_waitcnt lgkmcnt(0)\n" \
    "s_barrier\n" \
    "v_mfma_f32_32x32x16_bf16 v[0:15], v[88:91], v[80:83], v[0:15]\n" \
    "ds_read_b64_tr_b16 v[64:65], v96 offset:32768\n" \
    "ds_read_b64_tr_b16 v[66:67], v96 offset:34816\n" \
    "v_mfma_f32_32x32x16_bf16 v[32:47], v[92:95], v[80:83], v[32:47]\n" \
    "ds_read_b128 v[72:75], v98 offset:32768\n" \
    "ds_read_b128 v[76:79], v98 offset:33280\n" \
    "v_mfma_f32_32x32x16_bf16 v[16:31], v[88:91], v[84:87], v[16:31]\n" \
    "ds_read_b64_tr_b16 v[68:69], v97 offset:32768\n" \
    "ds_read_b64_tr_b16 v[70:71], v97 offset:34816\n" \
    "v_mfma_f32_32x32x16_bf16 v[48:63], v[92:95], v[84:87], v[48:63]\n" \
    "s_waitcnt lgkmcnt(0)\n" \
    "v_mfma_f32_32x32x16_bf16 v[0:15], v[72:75], v[64:67], v[0:15]\n" \
    "ds_read_b64_tr_b16 v[80:81], v96 offset:36864\n" \
    "ds_read_b64_tr_b16 v[82:83], v96 offset:38912\n" \
    "v_mfma_f32_32x32x16_bf16 v[32:47], v[76:79], v[64:67], v[32:47]\n" \
    "ds_read_b128 v[88:91], v98 offset:36864\n" \
    "ds_read_b128 v[92:95], v98 offset:37376\n" \
    "v_mfma_f32_32x32x16_bf16 v[16:31], v[72:75], v[68:71], v[16:31]\n" \
    "ds_read_b64_tr_b16 v[84:85], v97 offset:36864\n" \
    "ds_read_b64_tr_b16 v[86:87], v97 offset:38912\n" \
    "v_mfma_f32_32x32x16_bf16 v[48:63], v[76:79], v[68:71], v[48:63]\n" \
    "s_waitcnt lgkmcnt(0)\n" \
    "s_barrier\n" \
    "v_mfma_f32_32x32x16_bf16 v[0:15], v[88:91], v[80:83], v[0:15]\n" \
    "ds_read_b64_tr_b16 v[64:65], v96 offset:49152\n" \
    "ds_read_b64_tr_b16 v[66:67], v96 offset:51200\n" \
    "v_mfma_f32_32x32x16_bf16 v[32:47], v[92:95], v[80:83], v[32:47]\n" \
    "ds_read_b128 v[72:75], v98 offset:49152\n" \
    "ds_read_b128 v[76:79], v98 offset:49664\n" \
    "v_mfma_f32_32x32x16_bf16 v[16:31], v[88:91], v[84:87], v[16:31]\n" \
    "ds_read_b64_tr_b16 v[68:69], v97 offset:49152\n" \
    "ds_read_b64_tr_b16 v[70:71], v97 offset:51200\n" \
    "v_mfma_f32_32x32x16_bf16 v[48:63], v[92:95], v[84:87], v[48:63]\n" \
    "s_waitcnt lgkmcnt(0)\n" \
    "v_mfma_f32_32x32x16_bf16 v[0:15], v[72:75], v[64:67], v[0:15]\n" \
    "ds_read_b64_tr_b16 v[80:81], v96 offset:53248\n" \
    "ds_read_b64_tr_b16 v[82:83], v96 offset:55296\n" \
    "v_mfma_f32_32x32x16_bf16 v[32:47], v[76:79], v[64:67], v[32:47]\n" \
    "ds_read_b128 v[88:91], v98 offset:53248\n" \
    "ds_read_b128 v[92:95], v98 offset:53760\n" \
    "v_mfma_f32_32x32x16_bf16 v[16:31], v[72:75], v[68:71], v[16:31]\n" \
    "ds_read_b64_tr_b16 v[84:85], v97 offset:53248\n" \
    "ds_read_b64_tr_b16 v[86:87], v97 offset:55296\n" \
    "v_mfma_f32_32x32x16_bf16 v[48:63], v[76:79], v[68:71], v[48:63]\n" \
    "s_sub_u32 %[cnt], %[cnt], 1\n" \
    "s_cmp_lg_u32 %[cnt], 0\n" \
    "s_cbranch_scc1 1b\n" \
    "s_waitcnt lgkmcnt(0)\n" \
    "v_mfma_f32_32x32x16_bf16 v[0:15], v[88:91], v[80:83], v[0:15]\n" \
    "v_mfma_f32_32x32x16_bf16 v[32:47], v[92:95], v[80:83], v[32:47]\n" \
    "v_mfma_f32_32x32x16_bf16 v[16:31], v[88:91], v[84:87], v[16:31]\n" \
    "v_mfma_f32_32x32x16_bf16 v[48:63], v[92:95], v[84:87], v[48:63]\n" \
    "s_nop 15\n"
#define ASM_QPROJ \
    "s_mov_b32 %[cnt], 5\n" \
    "1:\n" \
    "s_waitcnt lgkmcnt(0)\n" \
    "s_barrier\n" \
    "v_mfma_f32_32x32x16_bf16 v[0:15], v[56:59], v[60:63], v[0:15]\n" \
    "ds_read_b64_tr_b16 v[32:33], v80 offset:0\n" \
    "ds_read_b64_tr_b16 v[34:35], v80 offset:512\n" \
    "ds_read_b128 v[36:39], v81 offset:0\n" \
    "v_mfma_f32_32x32x16_bf16 v[16:31], v[56:59], v[64:67], v[16:31]\n" \
    "ds_read_b128 v[40:43], v81 offset:4096\n" \
    "ds_read_b64_tr_b16 v[44:45], v80 offset:2048\n" \
    "ds_read_b64_tr_b16 v[46:47], v80 offset:2560\n" \
    "v_mfma_f32_32x32x16_bf16 v[0:15], v[68:71], v[72:75], v[0:15]\n" \
    "ds_read_b128 v[48:51], v82 offset:0\n" \
    "ds_read_b128 v[52:55], v82 offset:4096\n" \
    "v_mfma_f32_32x32x16_bf16 v[16:31], v[68:71], v[76:79], v[16:31]\n" \
    "s_waitcnt lgkmcnt(0)\n" \
    "v_mfma_f32_32x32x16_bf16 v[0:15], v[32:35], v[36:39], v[0:15]\n" \
    "ds_read_b64_tr_b16 v[56:57], v80 offset:4096\n" \
    "ds_read_b64_tr_b16 v[58:59], v80 offset:4608\n" \
    "ds_read_b128 v[60:63], v83 offset:0\n" \
    "v_mfma_f32_32x32x16_bf16 v[16:31], v[32:35], v[40:43], v[16:31]\n" \
    "ds_read_b128 v[64:67], v83 offset:4096\n" \
    "ds_read_b64_tr_b16 v[68:69], v80 offset:6144\n" \
    "ds_read_b64_tr_b16 v[70:71], v80 offset:6656\n" \
    "v_mfma_f32_32x32x16_bf16 v[0:15], v[44:47], v[48:51], v[0:15]\n" \
    "ds_read_b128 v[72:75], v84 offset:0\n" \
    "ds_read_b128 v[76:79], v84 offset:4096\n" \
    "v_mfma_f32_32x32x16_bf16 v[16:31], v[44:47], v[52:55], v[16:31]\n" \
    "s_waitcnt lgkmcnt(0)\n" \
    "s_barrier\n" \
    "v_mfma_f32_32x32x16_bf16 v[0:15], v[56:59], v[60:63], v[0:15]\n" \
    "ds_read_b64_tr_b16 v[32:33], v80 offset:24576\n" \
    "ds_read_b64_tr_b16 v[34:35], v80 offset:25088\n" \
    "ds_read_b128 v[36:39], v81 offset:24576\n" \
    "v_mfma_f32_32x32x16_bf16 v[16:31], v[56:59], v[64:67], v[16:31]\n" \
    "ds_read_b128 v[40:43], v81 offset:28672\n" \
    "ds_read_b64_tr_b16 v[44:45], v80 offset:26624\n" \
    "ds_read_b64_tr_b16 v[46:47], v80 offset:27136\n" \
    "v_mfma_f32_32x32x16_bf16 v[0:15], v[68:71], v[72:75], v[0:15]\n" \
    "ds_read_b128 v[48:51], v82 offset:24576\n" \
    "ds_read_b128 v[52:55], v82 offset:28672\n" \
    "v_mfma_f32_32x32x16_bf16 v[16:31], v[68:71], v[76:79], v[16:31]\n" \
    "s_waitcnt lgkmcnt(0)\n" \
    "v_mfma_f32_32x32x16_bf16 v[0:15], v[32:35], v[36:39], v[0:15]\n" \
    "ds_read_b64_tr_b16 v[56:57], v80 offset:28672\n" \
    "ds_read_b64_tr_b16 v[58:59], v80 offset:29184\n" \
    "ds_read_b128 v[60:63], v83 offset:24576\n" \
    "v_mfma_f32_32x32x16_bf16 v[16:31], v[32:35], v[40:43], v[16:31]\n" \
    "ds_read_b128 v[64:67], v83 offset:28672\n" \
    "ds_read_b64_tr_b16 v[68:69], v80 offset:30720\n" \
    "ds_read_b64_tr_b16 v[70:71], v80 offset:31232\n" \
    "v_mfma_f32_32x32x16_bf16 v[0:15], v[44:47], v[48:51], v[0:15]\n" \
    "ds_read_b128 v[72:75], v84 offset:24576\n" \
    "ds_read_b128 v[76:79], v84 offset:28672\n" \
    "v_mfma_f32_32x32x16_bf16 v[16:31], v[44:47], v[52:55], v[16:31]\n" \
    "s_waitcnt lgkmcnt(0)\n" \
    "s_barrier\n" \
    "v_mfma_f32_32x32x16_bf16 v[0:15], v[56:59], v[60:63], v[0:15]\n" \
    "ds_read_b64_tr_b16 v[32:33], v80 offset:49152\n" \
    "ds_read_b64_tr_b16 v[34:35], v80 offset:49664\n" \
    "ds_read_b128 v[36:39], v81 offset:49152\n" \
    "v_mfma_f32_32x32x16_bf16 v[16:31], v[56:59], v[64:67], v[16:31]\n" \
    "ds_read_b128 v[40:43], v81 offset:53248\n" \
    "ds_read_b64_tr_b16 v[44:45], v80 offset:51200\n" \
    "ds_read_b64_tr_b16 v[46:47], v80 offset:51712\n" \
    "v_mfma_f32_32x32x16_bf16 v[0:15], v[68:71], v[72:75], v[0:15]\n" \
    "ds_read_b128 v[48:51], v82 offset:49152\n" \
    "ds_read_b128 v[52:55], v82 offset:53248\n" \
    "v_mfma_f32_32x32x16_bf16 v[16:31], v[68:71], v[76:79], v[16:31]\n" \
    "s_waitcnt lgkmcnt(0)\n" \
    "v_mfma_f32_32x32x16_bf16 v[0:15], v[32:35], v[36:39], v[0:15]\n" \
    "ds_read_b64_tr_b16 v[56:57], v80 offset:53248\n" \
    "ds_read_b64_tr_b16 v[58:59], v80 offset:53760\n" \
    "ds_read_b128 v[60:63], v83 offset:49152\n" \
    "v_mfma_f32_32x32x16_bf16 v[16:31], v[32:35], v[40:43], v[16:31]\n" \
    "ds_read_b128 v[64:67], v83 offset:53248\n" \
    "ds_read_b64_tr_b16 v[68:69], v80 offset:55296\n" \
    "ds_read_b64_tr_b16 v[70:71], v80 offset:55808\n" \
    "v_mfma_f32_32x32x16_bf16 v[0:15], v[44:47], v[48:51], v[0:15]\n" \
    "ds_read_b128 v[72:75], v84 offset:49152\n" \
    "ds_read_b128 v[76:79], v84 offset:53248\n" \
    "v_mfma_f32_32x32x16_bf16 v[16:31], v[44:47], v[52:55], v[16:31]\n" \
    "s_sub_u32 %[cnt], %[cnt], 1\n" \
    "s_cmp_lg_u32 %[cnt], 0\n" \
    "s_cbranch_scc1 1b\n" \
    "s_waitcnt lgkmcnt(0)\n" \
    "s_barrier\n" \
    "v_mfma_f32_32x32x16_bf16 v[0:15], v[56:59], v[60:63], v[0:15]\n" \
    "ds_read_b64_tr_b16 v[32:33], v80 offset:0\n" \
    "ds_read_b64_tr_b16 v[34:35], v80 offset:512\n" \
    "ds_read_b128 v[36:39], v81 offset:0\n" \
    "v_mfma_f32_32x32x16_bf16 v[16:31], v[56:59], v[64:67], v[16:31]\n" \
    "ds_read_b128 v[40:43], v81 offset:4096\n" \
    "ds_read_b64_tr_b16 v[44:45], v80 offset:2048\n" \
    "ds_read_b64_tr_b16 v[46:47], v80 offset:2560\n" \
    "v_mfma_f32_32x32x16_bf16 v[0:15], v[68:71], v[72:75], v[0:15]\n" \
    "ds_read_b128 v[48:51], v82 offset:0\n" \
    "ds_read_b128 v[52:55], v82 offset:4096\n" \
    "v_mfma_f32_32x32x16_bf16 v[16:31], v[68:71], v[76:79], v[16:31]\n" \
    "s_waitcnt lgkmcnt(0)\n" \
    "v_mfma_f32_32x32x16_bf16 v[0:15], v[32:35], v[36:39], v[0:15]\n" \
    "ds_read_b64_tr_b16 v[56:57], v80 offset:4096\n" \
    "ds_read_b64_tr_b16 v[58:59], v80 offset:4608\n" \
    "ds_read_b128 v[60:63], v83 offset:0\n" \
    "v_mfma_f32_32x32x16_bf16 v[16:31], v[32:35], v[40:43], v[16:31]\n" \
    "ds_read_b128 v[64:67], v83 offset:4096\n" \
    "ds_read_b64_tr_b16 v[68:69], v80 offset:6144\n" \
    "ds_read_b64_tr_b16 v[70:71], v80 offset:6656\n" \
    "v_mfma_f32_32x32x16_bf16 v[0:15], v[44:47], v[48:51], v[0:15]\n" \
    "ds_read_b128 v[72:75], v84 offset:0\n" \
    "ds_read_b128 v[76:79], v84 offset:4096\n" \
    "v_mfma_f32_32x32x16_bf16 v[16:31], v[44:47], v[52:55], v[16:31]\n" \
    "s_waitcnt lgkmcnt(0)\n" \
    "v_mfma_f32_32x32x16_bf16 v[0:15], v[56:59], v[60:63], v[0:15]\n" \
    "v_mfma_f32_32x32x16_bf16 v[16:31], v[56:59], v[64:67], v[16:31]\n" \
    "v_mfma_f32_32x32x16_bf16 v[0:15], v[68:71], v[72:75], v[0:15]\n" \
    "v_mfma_f32_32x32x16_bf16 v[16:31], v[68:71], v[76:79], v[16:31]\n" \
    "s_nop 15\n"
#define ASM_OPROJ_A \
    "s_mov_b32 %[cnt], 4\n" \
    "1:\n" \
    "s_waitcnt lgkmcnt(0)\n" \
    "s_barrier\n" \
    "v_mfma_f32_32x32x16_bf16 v[0:15], v[56:59], v[60:63], v[0:15]\n" \
    "ds_read_b64_tr_b16 v[32:33], v80 offset:0\n" \
    "ds_read_b64_tr_b16 v[34:35], v80 offset:2048\n" \
    "ds_read_b128 v[36:39], v81 offset:0\n" \
    "v_mfma_f32_32x32x16_bf16 v[16:31], v[56:59], v[64:67], v[16:31]\n" \
    "ds_read_b128 v[40:43], v81 offset:512\n" \
    "ds_read_b64_tr_b16 v[44:45], v80 offset:4096\n" \
    "ds_read_b64_tr_b16 v[46:47], v80 offset:6144\n" \
    "v_mfma_f32_32x32x16_bf16 v[0:15], v[68:71], v[72:75], v[0:15]\n" \
    "ds_read_b128 v[48:51], v81 offset:2048\n" \
    "ds_read_b128 v[52:55], v81 offset:2560\n" \
    "v_mfma_f32_32x32x16_bf16 v[16:31], v[68:71], v[76:79], v[16:31]\n" \
    "s_waitcnt lgkmcnt(0)\n" \
    "v_mfma_f32_32x32x16_bf16 v[0:15], v[32:35], v[36:39], v[0:15]\n" \
    "ds_read_b64_tr_b16 v[56:57], v80 offset:8192\n" \
    "ds_read_b64_tr_b16 v[58:59], v80 offset:10240\n" \
    "ds_read_b128 v[60:63], v81 offset:4096\n" \
    "v_mfma_f32_32x32x16_bf16 v[16:31], v[32:35], v[40:43], v[16:31]\n" \
    "ds_read_b128 v[64:67], v81 offset:4608\n" \
    "ds_read_b64_tr_b16 v[68:69], v80 offset:12288\n" \
    "ds_read_b64_tr_b16 v[70:71], v80 offset:14336\n" \
    "v_mfma_f32_32x32x16_bf16 v[0:15], v[44:47], v[48:51], v[0:15]\n" \
    "ds_read_b128 v[72:75], v81 offset:6144\n" \
    "ds_read_b128 v[76:79], v81 offset:6656\n" \
    "v_mfma_f32_32x32x16_bf16 v[16:31], v[44:47], v[52:55], v[16:31]\n" \
    "s_waitcnt lgkmcnt(0)\n" \
    "s_barrier\n" \
    "v_mfma_f32_32x32x16_bf16 v[0:15], v[56:59], v[60:63], v[0:15]\n" \
    "ds_read_b64_tr_b16 v[32:33], v80 offset:24576\n" \
    "ds_read_b64_tr_b16 v[34:35], v80 offset:26624\n" \
    "ds_read_b128 v[36:39], v81 offset:24576\n" \
    "v_mfma_f32_32x32x16_bf16 v[16:31], v[56:59], v[64:67], v[16:31]\n" \
    "ds_read_b128 v[40:43], v81 offset:25088\n" \
    "ds_read_b64_tr_b16 v[44:45], v80 offset:28672\n" \
    "ds_read_b64_tr_b16 v[46:47], v80 offset:30720\n" \
    "v_mfma_f32_32x32x16_bf16 v[0:15], v[68:71], v[72:75], v[0:15]\n" \
    "ds_read_b128 v[48:51], v81 offset:26624\n" \
    "ds_read_b128 v[52:55], v81 offset:27136\n" \
    "v_mfma_f32_32x32x16_bf16 v[16:31], v[68:71], v[76:79], v[16:31]\n" \
    "s_waitcnt lgkmcnt(0)\n" \
    "v_mfma_f32_32x32x16_bf16 v[0:15], v[32:35], v[36:39], v[0:15]\n" \
    "ds_read_b64_tr_b16 v[56:57], v80 offset:32768\n" \
    "ds_read_b64_tr_b16 v[58:59], v80 offset:34816\n" \
    "ds_read_b128 v[60:63], v81 offset:28672\n" \
    "v_mfma_f32_32x32x16_bf16 v[16:31], v[32:35], v[40:43], v[16:31]\n" \
    "ds_read_b128 v[64:67], v81 offset:29184\n" \
    "ds_read_b64_tr_b16 v[68:69], v80 offset:36864\n" \
    "ds_read_b64_tr_b16 v[70:71], v80 offset:38912\n" \
    "v_mfma_f32_32x32x16_bf16 v[0:15], v[44:47], v[48:51], v[0:15]\n" \
    "ds_read_b128 v[72:75], v81 offset:30720\n" \
    "ds_read_b128 v[76:79], v81 offset:31232\n" \
    "v_mfma_f32_32x32x16_bf16 v[16:31], v[44:47], v[52:55], v[16:31]\n" \
    "s_waitcnt lgkmcnt(0)\n" \
    "s_barrier\n" \
    "v_mfma_f32_32x32x16_bf16 v[0:15], v[56:59], v[60:63], v[0:15]\n" \
    "ds_read_b64_tr_b16 v[32:33], v80 offset:49152\n" \
    "ds_read_b64_tr_b16 v[34:35], v80 offset:51200\n" \
    "ds_read_b128 v[36:39], v81 offset:49152\n" \
    "v_mfma_f32_32x32x16_bf16 v[16:31], v[56:59], v[64:67], v[16:31]\n" \
    "ds_read_b128 v[40:43], v81 offset:49664\n" \
    "ds_read_b64_tr_b16 v[44:45], v80 offset:53248\n" \
    "ds_read_b64_tr_b16 v[46:47], v80 offset:55296\n" \
    "v_mfma_f32_32x32x16_bf16 v[0:15], v[68:71], v[72:75], v[0:15]\n" \
    "ds_read_b128 v[48:51], v81 offset:51200\n" \
    "ds_read_b128 v[52:55], v81 offset:51712\n" \
    "v_mfma_f32_32x32x16_bf16 v[16:31], v[68:71], v[76:79], v[16:31]\n" \
    "s_waitcnt lgkmcnt(0)\n" \
    "v_mfma_f32_32x32x16_bf16 v[0:15], v[32:35], v[36:39], v[0:15]\n" \
    "ds_read_b64_tr_b16 v[56:57], v80 offset:57344\n" \
    "ds_read_b64_tr_b16 v[58:59], v80 offset:59392\n" \
    "ds_read_b128 v[60:63], v81 offset:53248\n" \
    "v_mfma_f32_32x32x16_bf16 v[16:31], v[32:35], v[40:43], v[16:31]\n" \
    "ds_read_b128 v[64:67], v81 offset:53760\n" \
    "ds_read_b64_tr_b16 v[68:69], v80 offset:61440\n" \
    "ds_read_b64_tr_b16 v[70:71], v80 offset:63488\n" \
    "v_mfma_f32_32x32x16_bf16 v[0:15], v[44:47], v[48:51], v[0:15]\n" \
    "ds_read_b128 v[72:75], v81 offset:55296\n" \
    "ds_read_b128 v[76:79], v81 offset:55808\n" \
    "v_mfma_f32_32x32x16_bf16 v[16:31], v[44:47], v[52:55], v[16:31]\n" \
    "s_sub_u32 %[cnt], %[cnt], 1\n" \
    "s_cmp_lg_u32 %[cnt], 0\n" \
    "s_cbranch_scc1 1b\n" \
    "s_waitcnt lgkmcnt(0)\n"
#define ASM_OPROJ_B \
    "v_mfma_f32_32x32x16_bf16 v[0:15], v[56:59], v[60:63], v[0:15]\n" \
    "ds_read_b64_tr_b16 v[32:33], v80 offset:0\n" \
    "ds_read_b64_tr_b16 v[34:35], v80 offset:2048\n" \
    "ds_read_b128 v[36:39], v81 offset:0\n" \
    "v_mfma_f32_32x32x16_bf16 v[16:31], v[56:59], v[64:67], v[16:31]\n" \
    "ds_read_b128 v[40:43], v81 offset:512\n" \
    "ds_read_b64_tr_b16 v[44:45], v80 offset:4096\n" \
    "ds_read_b64_tr_b16 v[46:47], v80 offset:6144\n" \
    "v_mfma_f32_32x32x16_bf16 v[0:15], v[68:71], v[72:75], v[0:15]\n" \
    "ds_read_b128 v[48:51], v81 offset:2048\n" \
    "ds_read_b128 v[52:55], v81 offset:2560\n" \
    "v_mfma_f32_32x32x16_bf16 v[16:31], v[68:71], v[76:79], v[16:31]\n" \
    "s_waitcnt lgkmcnt(0)\n" \
    "v_mfma_f32_32x32x16_bf16 v[0:15], v[32:35], v[36:39], v[0:15]\n" \
    "ds_read_b64_tr_b16 v[56:57], v80 offset:8192\n" \
    "ds_read_b64_tr_b16 v[58:59], v80 offset:10240\n" \
    "ds_read_b128 v[60:63], v81 offset:4096\n" \
    "v_mfma_f32_32x32x16_bf16 v[16:31], v[32:35], v[40:43], v[16:31]\n" \
    "ds_read_b128 v[64:67], v81 offset:4608\n" \
    "ds_read_b64_tr_b16 v[68:69], v80 offset:12288\n" \
    "ds_read_b64_tr_b16 v[70:71], v80 offset:14336\n" \
    "v_mfma_f32_32x32x16_bf16 v[0:15], v[44:47], v[48:51], v[0:15]\n" \
    "ds_read_b128 v[72:75], v81 offset:6144\n" \
    "ds_read_b128 v[76:79], v81 offset:6656\n" \
    "v_mfma_f32_32x32x16_bf16 v[16:31], v[44:47], v[52:55], v[16:31]\n" \
    "s_waitcnt lgkmcnt(0)\n" \
    "s_barrier\n" \
    "v_mfma_f32_32x32x16_bf16 v[0:15], v[56:59], v[60:63], v[0:15]\n" \
    "ds_read_b64_tr_b16 v[32:33], v80 offset:24576\n" \
    "ds_read_b64_tr_b16 v[34:35], v80 offset:26624\n" \
    "ds_read_b128 v[36:39], v81 offset:24576\n" \
    "v_mfma_f32_32x32x16_bf16 v[16:31], v[56:59], v[64:67], v[16:31]\n" \
    "ds_read_b128 v[40:43], v81 offset:25088\n" \
    "ds_read_b64_tr_b16 v[44:45], v80 offset:28672\n" \
    "ds_read_b64_tr_b16 v[46:47], v80 offset:30720\n" \
    "v_mfma_f32_32x32x16_bf16 v[0:15], v[68:71], v[72:75], v[0:15]\n" \
    "ds_read_b128 v[48:51], v81 offset:26624\n" \
    "ds_read_b128 v[52:55], v81 offset:27136\n" \
    "v_mfma_f32_32x32x16_bf16 v[16:31], v[68:71], v[76:79], v[16:31]\n" \
    "s_waitcnt lgkmcnt(0)\n" \
    "v_mfma_f32_32x32x16_bf16 v[0:15], v[32:35], v[36:39], v[0:15]\n" \
    "ds_read_b64_tr_b16 v[56:57], v80 offset:32768\n" \
    "ds_read_b64_tr_b16 v[58:59], v80 offset:34816\n" \
    "ds_read_b128 v[60:63], v81 offset:28672\n" \
    "v_mfma_f32_32x32x16_bf16 v[16:31], v[32:35], v[40:43], v[16:31]\n" \
    "ds_read_b128 v[64:67], v81 offset:29184\n" \
    "ds_read_b64_tr_b16 v[68:69], v80 offset:36864\n" \
    "ds_read_b64_tr_b16 v[70:71], v80 offset:38912\n" \
    "v_mfma_f32_32x32x16_bf16 v[0:15], v[44:47], v[48:51], v[0:15]\n" \
    "ds_read_b128 v[72:75], v81 offset:30720\n" \
    "ds_read_b128 v[76:79], v81 offset:31232\n" \
    "v_mfma_f32_32x32x16_bf16 v[16:31], v[44:47], v[52:55], v[16:31]\n" \
    "s_waitcnt lgkmcnt(0)\n" \
    "s_barrier\n" \
    "v_mfma_f32_32x32x16_bf16 v[0:15], v[56:59], v[60:63], v[0:15]\n" \
    "ds_read_b64_tr_b16 v[32:33], v80 offset:49152\n" \
    "ds_read_b64_tr_b16 v[34:35], v80 offset:51200\n" \
    "ds_read_b128 v[36:39], v81 offset:49152\n" \
    "v_mfma_f32_32x32x16_bf16 v[16:31], v[56:59], v[64:67], v[16:31]\n" \
    "ds_read_b128 v[40:43], v81 offset:49664\n" \
    "ds_read_b64_tr_b16 v[44:45], v80 offset:53248\n" \
    "ds_read_b64_tr_b16 v[46:47], v80 offset:55296\n" \
    "v_mfma_f32_32x32x16_bf16 v[0:15], v[68:71], v[72:75], v[0:15]\n" \
    "ds_read_b128 v[48:51], v81 offset:51200\n" \
    "ds_read_b128 v[52:55], v81 offset:51712\n" \
    "v_mfma_f32_32x32x16_bf16 v[16:31], v[68:71], v[76:79], v[16:31]\n" \
    "s_waitcnt lgkmcnt(0)\n" \
    "v_mfma_f32_32x32x16_bf16 v[0:15], v[32:35], v[36:39], v[0:15]\n" \
    "ds_read_b64_tr_b16 v[56:57], v80 offset:57344\n" \
    "ds_read_b64_tr_b16 v[58:59], v80 offset:59392\n" \
    "ds_read_b128 v[60:63], v81 offset:53248\n" \
    "v_mfma_f32_32x32x16_bf16 v[16:31], v[32:35], v[40:43], v[16:31]\n" \
    "ds_read_b128 v[64:67], v81 offset:53760\n" \
    "ds_read_b64_tr_b16 v[68:69], v80 offset:61440\n" \
    "ds_read_b64_tr_b16 v[70:71], v80 offset:63488\n" \
    "v_mfma_f32_32x32x16_bf16 v[0:15], v[44:47], v[48:51], v[0:15]\n" \
    "ds_read_b128 v[72:75], v81 offset:55296\n" \
    "ds_read_b128 v[76:79], v81 offset:55808\n" \
    "v_mfma_f32_32x32x16_bf16 v[16:31], v[44:47], v[52:55], v[16:31]\n" \
    "s_waitcnt lgkmcnt(0)\n" \
    "s_barrier\n" \
    "v_mfma_f32_32x32x16_bf16 v[0:15], v[56:59], v[60:63], v[0:15]\n" \
    "ds_read_b64_tr_b16 v[32:33], v80 offset:0\n" \
    "ds_read_b64_tr_b16 v[34:35], v80 offset:2048\n" \
    "ds_read_b128 v[36:39], v81 offset:0\n" \
    "v_mfma_f32_32x32x16_bf16 v[16:31], v[56:59], v[64:67], v[16:31]\n" \
    "ds_read_b128 v[40:43], v81 offset:512\n" \
    "ds_read_b64_tr_b16 v[44:45], v80 offset:4096\n" \
    "ds_read_b64_tr_b16 v[46:47], v80 offset:6144\n" \
    "v_mfma_f32_32x32x16_bf16 v[0:15], v[68:71], v[72:75], v[0:15]\n" \
    "ds_read_b128 v[48:51], v81 offset:2048\n" \
    "ds_read_b128 v[52:55], v81 offset:2560\n" \
    "v_mfma_f32_32x32x16_bf16 v[16:31], v[68:71], v[76:79], v[16:31]\n" \
    "s_waitcnt lgkmcnt(0)\n" \
    "v_mfma_f32_32x32x16_bf16 v[0:15], v[32:35], v[36:39], v[0:15]\n" \
    "ds_read_b64_tr_b16 v[56:57], v80 offset:8192\n" \
    "ds_read_b64_tr_b16 v[58:59], v80 offset:10240\n" \
    "ds_read_b128 v[60:63], v81 offset:4096\n" \
    "v_mfma_f32_32x32x16_bf16 v[16:31], v[32:35], v[40:43], v[16:31]\n" \
    "ds_read_b128 v[64:67], v81 offset:4608\n" \
    "ds_read_b64_tr_b16 v[68:69], v80 offset:12288\n" \
    "ds_read_b64_tr_b16 v[70:71], v80 offset:14336\n" \
    "v_mfma_f32_32x32x16_bf16 v[0:15], v[44:47], v[48:51], v[0:15]\n" \
    "ds_read_b128 v[72:75], v81 offset:6144\n" \
    "ds_read_b128 v[76:79], v81 offset:6656\n" \
    "v_mfma_f32_32x32x16_bf16 v[16:31], v[44:47], v[52:55], v[16:31]\n" \
    "s_waitcnt lgkmcnt(0)\n" \
    "v_mfma_f32_32x32x16_bf16 v[0:15], v[56:59], v[60:63], v[0:15]\n" \
    "v_mfma_f32_32x32x16_bf16 v[16:31], v[56:59], v[64:67], v[16:31]\n" \
    "v_mfma_f32_32x32x16_bf16 v[0:15], v[68:71], v[72:75], v[0:15]\n" \
    "v_mfma_f32_32x32x16_bf16 v[16:31], v[68:71], v[76:79], v[16:31]\n" \
    "s_nop 15\n"

struct Args {
    const char *x, *wq, *wo, *w1, *b1, *w2, *b2;
    char *ws, *out;
};

__global__ __launch_bounds__(512) void k_pre(const char* __restrict__ x, const char* __restrict__ wk, const char* __restrict__ wv,
                                              const char* __restrict__ gw, char* __restrict__ ws) {
    __shared__ __attribute__((aligned(1024))) char lds[131072];
    typedef __attribute__((address_space(3))) char* lds_ptr;
    const int tid = threadIdx.x, lane = tid & 63, wid = __builtin_amdgcn_readfirstlane(tid >> 6), b = blockIdx.x;
    if (b == 0 && wid == 0) LSTAMP(15);
    if (tid < 32) *(unsigned*)(ws + b * 128 + tid * 4) = 0u;
    if (b < 128) {
        const int T = b >> 1, h = lane >> 5, r32 = lane & 31;
        const bool isv = b & 1;
        const unsigned base = (unsigned)(uintptr_t)lds + wid * 16384;
        const unsigned xe = (lane >> 3) * 2048 + (((lane & 7) ^ (lane >> 4)) * 16), xo = xe ^ 64;
        const char* xb = x + (size_t)T * 32 * 2048 + wid * 256;
        const char* wb = (isv ? wv : wk) + wid * 8192;
#pragma unroll
        for (int i = 0; i < 4; ++i) dma_s2(lane * 16, wb + i * 2048, base + 8192 + i * 2048);
#pragma unroll
        for (int i = 0; i < 8; ++i) dma_s((i & 1) ? xo : xe, xb + (i >> 2) * 128 + (i & 3) * 8 * 2048, base + i * 1024);
        asm volatile("s_waitcnt vmcnt(0)" ::: "memory");
        lds_cptr wp = (lds_cptr)lds + wid * 16384 + 8192 + (8 * h + ((lane & 15) >> 2)) * 64 + ((lane >> 4) & 1) * 32 + (lane & 3) * 8;
        const unsigned xa = wid * 16384 + r32 * 128 + ((h ^ ((r32 >> 1) & 7)) * 16);
        f32x16 a = {};
#pragma unroll
        for (int ks = 0; ks < 8; ++ks) {
            const bf16x8 wf = __builtin_shufflevector(ldstr(wp + ks * 1024), ldstr(wp + ks * 1024 + 256), 0, 1, 2, 3, 4, 5, 6, 7);
            const bf16x8 xf = lds128((lds_cptr)lds + ((xa ^ ((ks & 3) * 32)) + (ks >> 2) * 4096));
            a = isv ? MFMA32(xf, wf, a) : MFMA32(wf, xf, a);
        }
#pragma unroll
        for (int k = 0; k < 4; ++k)
            *(__attribute__((address_space(3))) f32x4*)((lds_ptr)lds + wid * 16384 + k * 1024 + lane * 16) = (f32x4){a[4 * k], a[4 * k + 1], a[4 * k + 2], a[4 * k + 3]};
        LGKM_BAR();
        if (wid) return;
#pragma unroll
        for (int w = 1; w < 8; ++w)
#pragma unroll
            for (int k = 0; k < 4; ++k) {
                const f32x4 v = *(const __attribute__((address_space(3))) f32x4*)((lds_cptr)lds + w * 16384 + k * 1024 + lane * 16);
                a[4 * k] += v[0]; a[4 * k + 1] += v[1]; a[4 * k + 2] += v[2]; a[4 * k + 3] += v[3];
            }
        const u32x4 u0 = pack8(a, 0, 1.f), u1 = pack8(a, 1, 1.f);
        char* dst = ws + WS_KV + (isv ? T * 4096 + 2048 : ((T + 63) & 63) * 4096) + lane * 16;
        *(u32x4*)dst = u0;
        *(u32x4*)(dst + 1024) = u1;
        if (!isv) {
            float n = sumsq8(u0) + sumsq8(u1);
            n += __shfl_xor(n, 32);
            n = wave_max(n < 3.0e38f ? n : __builtin_inff());
            if (lane == 0) ((float*)(ws + WS_KN))[T] = n;
        }
        return;
    }
    const int gi = (b - 128) * 2 + (wid >> 2), w4 = wid & 3, t4 = tid & 255;
    const char* xr = x + (size_t)(gi * 8 + w4 * 2) * 2048 + lane * 16;
    const u32x4 a00 = *(const u32x4*)xr, a01 = *(const u32x4*)(xr + 1024), a10 = *(const u32x4*)(xr + 2048), a11 = *(const u32x4*)(xr + 3072);
    const u32x4 g0 = *(const u32x4*)(gw + lane * 16), g1 = *(const u32x4*)(gw + 1024 + lane * 16);
    u32x2 xt[8];
#pragma unroll
    for (int r = 0; r < 8; ++r) xt[r] = *(const u32x2*)(x + (size_t)(gi * 8 + r) * 2048 + t4 * 8);
    float v0[16], v1[16], gv[16];
#pragma unroll
    for (int i = 0; i < 4; ++i) {
        v0[2 * i] = bflo(a00[i]); v0[2 * i + 1] = bfhi(a00[i]); v0[8 + 2 * i] = bflo(a01[i]); v0[9 + 2 * i] = bfhi(a01[i]);
        v1[2 * i] = bflo(a10[i]); v1[2 * i + 1] = bfhi(a10[i]); v1[8 + 2 * i] = bflo(a11[i]); v1[9 + 2 * i] = bfhi(a11[i]);
        gv[2 * i] = bflo(g0[i]); gv[2 * i + 1] = bfhi(g0[i]); gv[8 + 2 * i] = bflo(g1[i]); gv[9 + 2 * i] = bfhi(g1[i]);
    }
    float s0 = 0, s1 = 0, d0 = 0, d1 = 0;
#pragma unroll
    for (int i = 0; i < 16; ++i) { s0 += v0[i]; s1 += v1[i]; d0 = fmaf(v0[i], gv[i], d0); d1 = fmaf(v1[i], gv[i], d1); }
    s0 = wave_sum(s0) * (1.f / DM); s1 = wave_sum(s1) * (1.f / DM);
    float q0 = 0, q1 = 0;
#pragma unroll
    for (int i = 0; i < 16; ++i) { const float e0 = v0[i] - s0, e1 = v1[i] - s1; q0 = fmaf(e0, e0, q0); q1 = fmaf(e1, e1, q1); }
    q0 = wave_sum(q0) * (1.f / DM); q1 = wave_sum(q1) * (1.f / DM); d0 = wave_sum(d0); d1 = wave_sum(d1);
    float* st = (float*)lds + (wid >> 2) * 16;
    if (lane == 0) {
        st[w4 * 4] = s0; st[w4 * 4 + 1] = 1.f / sqrtf(q0 + 1e-5f); st[w4 * 4 + 2] = s1; st[w4 * 4 + 3] = 1.f / sqrtf(q1 + 1e-5f);
        float* gp = (float*)(ws + WS_GATE) + gi * 8 + w4 * 2;
        gp[0] = 1.f / (1.f + __expf(-d0)); gp[1] = 1.f / (1.f + __expf(-d1));
    }
    LGKM_BAR();
    float hv[8][4];
#pragma unroll
    for (int r = 0; r < 8; ++r) {
        const float m = st[2 * r], rs = st[2 * r + 1];
        hv[r][0] = (bflo(xt[r][0]) - m) * rs; hv[r][1] = (bfhi(xt[r][0]) - m) * rs;
        hv[r][2] = (bflo(xt[r][1]) - m) * rs; hv[r][3] = (bfhi(xt[r][1]) - m) * rs;
    }
    char* hp = ws + WS_HT + (size_t)gi * 16384 + t4 * 64;
#pragma unroll
    for (int c = 0; c < 4; ++c)
        *(u32x4*)(hp + c * 16) = (u32x4){cvtpk(hv[0][c], hv[1][c]), cvtpk(hv[2][c], hv[3][c]), cvtpk(hv[4][c], hv[5][c]), cvtpk(hv[6][c], hv[7][c])};
}

__device__ __forceinline__ void attn_store(rsrc_t rw, const f32x16& o, float inv, int head, int s, int h) {
    const unsigned off = WS_OP + ((head * 4 + h) * SEQ + s) * 16;
    st16(rw, off, pack8(o, 0, inv));
    st16(rw, off + 2 * SEQ * 16, pack8(o, 1, inv));
}

__device__ __forceinline__ void attn_online(rsrc_t rw, bf16x8 q0, bf16x8 q1, int lane, f32x16& o, float& inv) {
    float m = -__builtin_inff(), l = 0.f;
    o = f32x16{};
    for (int t = 0; t < 64; ++t) {
        const unsigned kp = WS_KV + ((t + 63) & 63) * 4096, vp = WS_KV + t * 4096 + 2048;
        const bf16x8 k0 = BC8(__builtin_amdgcn_raw_buffer_load_b128(rw, lane * 16, kp, 0));
        const bf16x8 k1 = BC8(__builtin_amdgcn_raw_buffer_load_b128(rw, lane * 16 + 1024, kp, 0));
        const bf16x8 v0 = BC8(__builtin_amdgcn_raw_buffer_load_b128(rw, lane * 16, vp, 0));
        const bf16x8 v1 = BC8(__builtin_amdgcn_raw_buffer_load_b128(rw, lane * 16 + 1024, vp, 0));
        f32x16 s = MFMA32(k0, q0, f32x16{});
        s = MFMA32(k1, q1, s);
        float tm = s[0];
#pragma unroll
        for (int r = 1; r < 16; ++r) tm = fmaxf(tm, s[r]);
        tm = fmaxf(tm, __shfl_xor(tm, 32));
        const float mn = fmaxf(m, tm), al = __builtin_amdgcn_exp2f(m - mn);
        m = mn;
#pragma unroll
        for (int r = 0; r < 16; ++r) { s[r] = __builtin_amdgcn_exp2f(s[r] - mn); o[r] *= al; }
        const u32x4 p0 = pack8(s, 0, 1.f), p1 = pack8(s, 1, 1.f);
        float ts = 0.f;
#pragma unroll
        for (int r = 0; r < 4; ++r) ts += bflo(p0[r]) + bfhi(p0[r]) + bflo(p1[r]) + bfhi(p1[r]);
        l = l * al + ts;
        o = MFMA32(v0, BC8(p0), o);
        o = MFMA32(v1, BC8(p1), o);
    }
    l += __shfl_xor(l, 32);
    inv = 1.f / l;
}

__device__ __forceinline__ void side_a_loader(const Args& A, unsigned lds0, int lane) {
    char* ws = A.ws;
    const int xcd = blockIdx.x & 7, r = blockIdx.x >> 3;
    const unsigned lin = lane * 16;
    {
        const int rb = (xcd >> 1) * 4 + (r & 3), hp = (xcd & 1) * 8 + (r >> 2), rot = ((r & 3) + (r >> 2)) & 3;
        const unsigned xe = (lane >> 3) * 2048 + (((lane & 7) ^ (lane >> 4)) * 16);
        const unsigned we = (lane >> 3) * 2048 + (((((lane & 7) >> 2) ^ ((lane >> 4) & 1)) << 2) | (lane & 3)) * 16;
        unsigned xv[16], wv[8];
#pragma unroll
        for (int i = 0; i < 16; ++i) xv[i] = off4((xe ^ ((i & 1) * 64)) + i * 8 * 2048, i);
#pragma unroll
        for (int i = 0; i < 8; ++i) wv[i] = off4(we + i * 8 * 2048, i);
        const char* xb = A.x + (size_t)rb * 128 * 2048;
        const char* wb = A.wq + hp * 128;
        auto stage = [&](int j, int slot) {
            const int st = (j & ~3) | ((j + rot) & 3);
            const unsigned dst = lds0 + slot * 24576;
#pragma unroll
            for (int i = 0; i < 2; ++i) dma4(wv + 4 * i, wb + (size_t)st * 64 * 2048, dst + 16384 + i * 4096);
#pragma unroll
            for (int i = 0; i < 4; ++i) dma4(xv + 4 * i, xb + st * 128, dst + i * 4096);
        };
        const char* kv = ws + WS_KV;
        const unsigned l4[4] = {lin + 3072, lin + 3072, lin + 3072, lin + 3072};
        auto part = [&](int c, int u0, int n, unsigned dst, bool pace) {
            for (int i = 0; i < n; ++i) {
                dma4(l4, kv + c * 32768 + (u0 + i) * 4096, lds0 + dst + i * 4096);
                if (pace) __builtin_amdgcn_s_sleep(4);
            }
        };
        stage(0, 0); part(0, 0, 2, LDS_X, false); stage(1, 1);
        int slot = 2;
        for (int j = 0; j < 14; ++j) { WAITV_BAR(24); if (j == 0) LSTAMP(8); if (j == 4) LSTAMP(9); if (j == 8) LSTAMP(10); stage(j + 2, slot); slot = slot == 2 ? 0 : slot + 1; }
        WAITV_BAR(24);
        part(0, 2, 6, 24576, false);
        WAITV_BAR(24);
        WAITV_BAR(0);
        part(1, 0, 6, 0, true); part(1, 6, 2, 49152, true);
        for (int c = 0; c < 6; c += 2) {
            WAITV_BAR(0); part(c + 2, 0, 2, LDS_X, true); part(c + 2, 2, 6, 24576, true);
            WAITV_BAR(0); part(c + 3, 0, 6, 0, true); part(c + 3, 6, 2, 49152, true);
        }
        WAITV_BAR(0); BARRIER();
    }
    {
        const int rb = (xcd >> 1) * 4 + (r & 3), hp = (xcd & 1) * 8 + (r >> 2);
        const int sb = (xcd >> 1) * 8 + (r & 7), dt = (xcd & 1) * 4 + (r >> 3), rot = (((r & 7) + (r >> 3)) & 3) * 4;
        const unsigned woff = (lane >> 4) * 2048 + dt * 256 + (((((lane & 15) >> 2) ^ (lane >> 4)) << 2) | (lane & 3)) * 16;
        unsigned wv[16], ov[8];
#pragma unroll
        for (int i = 0; i < 16; ++i) wv[i] = off4(woff + i * 4 * 2048, i);
#pragma unroll
        for (int i = 0; i < 8; ++i) ov[i] = off4(lin + i * (SEQ * 16), i);
        const char* ob = ws + WS_OP + sb * 1024;
        const unsigned foff = ((CT_O + (sb >> 1) * 4 + (dt & 3)) * 32 + (lane & 15)) * 4;
        unsigned todo = 0xFFFFu, pv;
        auto poll = [&]() { asm volatile("global_load_dword %0, %1, %2 sc1" : "=v"(pv) : "v"(foff), "s"(ws) : "memory"); };
        auto ready = [&]() { return (unsigned)__ballot(pv != 0u) & todo; };
        auto stage = [&](unsigned avail, int slot) {
            const int st = (__builtin_ctz(((avail | (avail << 16)) >> rot) & 0xFFFFu) + rot) & 15;
            todo &= ~(1u << st);
#pragma unroll
            for (int i = 0; i < 2; ++i) dma4c(ov + 4 * i, ob + (size_t)st * 8 * (SEQ * 16), lds0 + slot * 24576 + 16384 + i * 4096);
#pragma unroll
            for (int i = 0; i < 4; ++i) dma4(wv + 4 * i, A.wo + (size_t)st * 64 * 2048, lds0 + slot * 24576 + i * 4096);
        };
        auto wait_ready = [&](unsigned avail) {
            for (unsigned spins = 0; !avail && spins < (1u << 20); ++spins) {
                poll();
                asm volatile("s_waitcnt vmcnt(0)" : "+v"(pv)::"memory");
                avail = ready();
                if (!avail) __builtin_amdgcn_s_sleep(POLL_SLEEP);
            }
            return avail ? avail : todo;
        };
        poll();
        asm volatile("s_waitcnt vmcnt(0)" : "+v"(pv)::"memory");
        int n0 = 0;
        if (ready()) { stage(ready(), 0); n0 = 1; }
        if (n0 && ready()) { stage(ready(), 1); n0 = 2; }
        BARRIER();
        if (lane < 4) __hip_atomic_store((gu32*)ws + (CT_O + rb * 4 + lane) * 32 + hp, 1u, __ATOMIC_RELAXED, __HIP_MEMORY_SCOPE_AGENT);
        LSTAMP(12);
        if (n0 < 1) stage(wait_ready(0), 0);
        if (n0 < 2) stage(wait_ready(ready()), 1);
        LSTAMP(13);
        int slot = 2;
        for (int j = 0; j < 14; ++j) {
            if (j == 12) wait_count(ws, CT_Y + (sb >> 1) * 8 + dt, 2);
            asm volatile("s_waitcnt vmcnt(24)\n\ts_barrier" : "+v"(pv)::"memory");
            if (j == 0) LSTAMP(14);
            const unsigned avail = wait_ready(ready());
            poll();
            stage(avail, slot);
            slot = slot == 2 ? 0 : slot + 1;
        }
        asm volatile("s_waitcnt vmcnt(24)\n\ts_barrier" : "+v"(pv)::"memory");
        WAITV_BAR(0);
    }
}

__device__ __forceinline__ void side_a(const Args& A, char* lds, int wid) {
    char* ws = A.ws;
    const unsigned lds0 = (unsigned)(uintptr_t)lds;
    const int xcd = blockIdx.x & 7, r = blockIdx.x >> 3;
    const rsrc_t rw = __builtin_amdgcn_make_buffer_rsrc(ws, 0, WS_END, 0x00020000);
    unsigned cnt;
    bf16x8 qA0, qA1, qB0, qB1, k0, k1;
    float qn, kn;
    __builtin_amdgcn_s_setprio(PRIO_A);
    {
        const int lane = fresh_lane(), h = lane >> 5, r32 = lane & 31, q4 = (lane & 15) >> 2;
        const unsigned tb = lds0 + 16384 + (8 * h + q4) * 128 + (((wid & 1) ^ (q4 >> 1)) * 64) + ((lane >> 4) & 1) * 32 + (lane & 3) * 8;
        const unsigned xb = lds0 + ((wid >> 1) * 64 + r32) * 128 + ((h ^ ((r32 >> 1) & 7)) * 16);
        k0 = *(const bf16x8*)(ws + WS_KV + 63 * 4096 + lane * 16);
        k1 = *(const bf16x8*)(ws + WS_KV + 63 * 4096 + 1024 + lane * 16);
        kn = wave_max(((const float*)(ws + WS_KN))[lane]);
        f32x16 a0 = {}, a1 = {}, y0 = {};
        f32x8 y1 = {};
        asm volatile("s_nop 1\n" ASM_QPROJ
                     : [cnt] "=&s"(cnt), "+{v[0:15]}"(a0), "+{v[16:31]}"(a1), "+{v[56:71]}"(y0), "+{v[72:79]}"(y1)
                     : "{v80}"(tb), "{v81}"(xb), "{v82}"(xb ^ 32), "{v83}"(xb ^ 64), "{v84}"(xb ^ 96)
                     : "memory", "scc", CLOB16(32, 33, 34, 35, 36, 37, 38, 39, 40, 41, 42, 43, 44, 45, 46, 47), CLOB8(48, 49, 50, 51, 52, 53, 54, 55));
        STAMP(1);
        const u32x4 u0 = pack8(a0, 0, QSCALE), u1 = pack8(a0, 1, QSCALE), w0 = pack8(a1, 0, QSCALE), w1 = pack8(a1, 1, QSCALE);
        float nA = sumsq8(u0) + sumsq8(u1), nB = sumsq8(w0) + sumsq8(w1);
        const auto sw = __builtin_amdgcn_permlane32_swap(__float_as_uint(nA), __float_as_uint(nB), false, false);
        qn = __uint_as_float(sw[0]) + __uint_as_float(sw[1]);
        qA0 = BC8(u0); qA1 = BC8(u1); qB0 = BC8(w0); qB1 = BC8(w1);
    }
    const int head = ((xcd & 1) * 8 + (r >> 2)) * 2 + (wid & 1), s0 = ((xcd >> 1) * 4 + (r & 3)) * 128 + (wid >> 1) * 64;
    BARRIER();
    STAMP(2);
    {
        const int lane = fresh_lane();
        lds_cptr p = (lds_cptr)lds + lane * 16;
        if (!__all(qn * kn <= SCORE_BOUND * SCORE_BOUND)) {
            f32x16 o; float inv;
            attn_online(rw, qA0, qA1, lane, o, inv); attn_store(rw, o, inv, head, s0 + (lane & 31), lane >> 5);
            attn_online(rw, qB0, qB1, lane, o, inv); attn_store(rw, o, inv, head, s0 + 32 + (lane & 31), lane >> 5);
            for (int c = 0; c < 8; ++c) BARRIER();
        } else {
            const short one = ((lane & 3) == 0 && ((lane >> 2) & 1) == ((lane >> 4) & 1)) ? (short)0x3F80 : (short)0;
            const bf16x8 ones = {one, one, one, one, one, one, one, one};
            f32x16 oA = {}, oB = {};
            f32x4 lA = {}, lB = {};
            f32x16 sA = MFMA32(k0, qA0, f32x16{}), sB = MFMA32(k0, qB0, f32x16{});
            sA = MFMA32(k1, qA1, sA); sB = MFMA32(k1, qB1, sB);
            k0 = lds128(p + LDS_X); k1 = lds128(p + LDS_X + 1024);
            bf16x8 v0 = lds128(p + LDS_X + 2048), v1 = lds128(p + LDS_X + 3072);
#pragma unroll
            for (int i = 0; i < 16; ++i) sA[i] = __builtin_amdgcn_exp2f(sA[i]);
            u32x4 pA0 = pack8(sA, 0, 1.f), pA1 = pack8(sA, 1, 1.f);
            asm volatile("s_nop 15\n" ASM_ATTN
                         : [cnt] "=&s"(cnt), "+{v[0:15]}"(oA), "+{v[16:31]}"(oB), "+{v[48:63]}"(sB), "+{v[64:67]}"(pA0), "+{v[68:71]}"(pA1),
                           "+{v[100:103]}"(lA), "+{v[104:107]}"(lB), "+{v[108:111]}"(k0), "+{v[112:115]}"(k1), "+{v[116:119]}"(v0), "+{v[120:123]}"(v1)
                         : "{v[80:83]}"(qA0), "{v[84:87]}"(qA1), "{v[88:91]}"(qB0), "{v[92:95]}"(qB1), "{v[96:99]}"(ones),
                           "{v124}"((unsigned)(uintptr_t)p), "{v125}"((unsigned)(uintptr_t)p + 32768)
                         : "memory", "scc", CLOB16(32, 33, 34, 35, 36, 37, 38, 39, 40, 41, 42, 43, 44, 45, 46, 47), CLOB8(72, 73, 74, 75, 76, 77, 78, 79));
            const int ln = fresh_lane();
            attn_store(rw, oA, __builtin_amdgcn_rcpf(lA[0]), head, s0 + (ln & 31), ln >> 5);
            attn_store(rw, oB, __builtin_amdgcn_rcpf(lB[0]), head, s0 + 32 + (ln & 31), ln >> 5);
        }
    }
    STAMP(3);
    WAITV_BAR(0);
    {
        const int lane = fresh_lane(), h = lane >> 5, r32 = lane & 31, q4 = (lane & 15) >> 2;
        const int sb = (xcd >> 1) * 8 + (r & 7), dt = (xcd & 1) * 4 + (r >> 3);
        const unsigned tb = lds0 + (4 * h + q4) * 256 + ((wid ^ q4) * 64) + ((lane >> 4) & 1) * 32 + (lane & 3) * 8;
        const unsigned ob = lds0 + 16384 + h * 1024 + r32 * 16;
        f32x16 a0 = {}, a1 = {}, y0 = {};
        f32x8 y1 = {};
#define OPROJ(TXT) asm volatile(TXT : [cnt] "=&s"(cnt), "+{v[0:15]}"(a0), "+{v[16:31]}"(a1), "+{v[56:71]}"(y0), "+{v[72:79]}"(y1) : "{v80}"(tb), "{v81}"(ob) \
                                : "memory", "scc", CLOB16(32, 33, 34, 35, 36, 37, 38, 39, 40, 41, 42, 43, 44, 45, 46, 47), CLOB8(48, 49, 50, 51, 52, 53, 54, 55))
        OPROJ("s_nop 1\n" ASM_OPROJ_A);
        STAMP(4);
        BARRIER();
        STAMP(5);
        const int s = sb * 64 + r32;
        u32x4 yp[2][2][2];
        float gate[2], bias[2];
#pragma unroll
        for (int si = 0; si < 2; ++si) {
#pragma unroll
            for (int kh = 0; kh < 2; ++kh)
#pragma unroll
                for (int c = 0; c < 2; ++c)
                    yp[si][kh][c] = __builtin_amdgcn_raw_buffer_load_b128(
                        rw, lane * 16 + c * 1024, WS_Y + ((((sb >> 1) * 8 + dt) * 2 + kh) * 2 + (sb & 1)) * 16384 + (si * 4 + wid) * 2048, SC1);
            gate[si] = __uint_as_float(__builtin_amdgcn_raw_buffer_load_b32(rw, (s + si * 32) * 4, WS_GATE, 0));
            bias[si] = bflo(*(const unsigned short*)(A.b2 + (s + si * 32) * 2));
        }
        OPROJ(ASM_OPROJ_B);
#undef OPROJ
        STAMP(6);
#pragma unroll
        for (int si = 0; si < 2; ++si) {
            const f32x16& a = si ? a1 : a0;
            const float g = gate[si], og = 1.f - g;
            unsigned pk[8];
#pragma unroll
            for (int k = 0; k < 8; ++k) {
                const unsigned p0 = yp[si][0][k >> 2][k & 3], p1 = yp[si][1][k >> 2][k & 3];
                pk[k] = cvtpk(g * a[2 * k] + og * (bflo(p0) + bflo(p1) + bias[si]), g * a[2 * k + 1] + og * (bfhi(p0) + bfhi(p1) + bias[si]));
            }
            char* op = A.out + (size_t)(s + si * 32) * 2048 + (dt * 128 + wid * 32) * 2 + h * 16;
#pragma unroll
            for (int k = 0; k < 2; ++k) {
                const auto x0 = __builtin_amdgcn_permlane32_swap(pk[4 * k], pk[4 * k + 2], false, false);
                const auto x1 = __builtin_amdgcn_permlane32_swap(pk[4 * k + 1], pk[4 * k + 3], false, false);
                *(u32x4*)(op + k * 32) = (u32x4){x0[0], x1[0], x0[1], x1[1]};
            }
        }
    }
    STAMP(7);
}

__device__ __forceinline__ void side_b_loader(const Args& A, unsigned lds0, int lane) {
    char* ws = A.ws;
    const int gi = blockIdx.x - 256, xcd = gi & 7, r = gi >> 3;
    const unsigned sw256 = (((((lane & 15) >> 2) ^ (lane >> 4)) << 2) | (lane & 3)) * 16;
    if (r >= 28) {
        const int id = (r - 28) * 8 + xcd;
        for (int i = id; i < 14336 + 2048; i += 32) {
            asm volatile("s_waitcnt vmcnt(24)" ::: "memory");
            dma_s(lane * 16, i < 14336 ? A.w2 + (size_t)i * 1024 : A.wo + (size_t)(i - 14336) * 1024, lds0 + 65536 + 1024);
        }
    } else {
        const int dt = (xcd & 1) * 4 + (r & 3), ft = (xcd >> 1) * 7 + (r >> 2), rot = 2 * (((r & 3) + (r >> 2)) & 3);
        unsigned av[8], bv[8];
#pragma unroll
        for (int i = 0; i < 8; ++i) {
            av[i] = off4((i * 4 + (lane >> 4)) * (DF * 2) + ft * 256 + sw256, i);
            bv[i] = off4((i >> 1) * 16384 + (dt * 128 + (i & 1) * 64 + lane) * 16, i);
        }
        auto sta = [&](int j) {
            const int st = (j & ~7) | ((j + rot) & 7);
#pragma unroll
            for (int i = 0; i < 2; ++i) dma4(av + 4 * i, A.w1 + (size_t)st * 32 * (DF * 2), lds0 + (j & 3) * 16384 + i * 4096);
        };
        auto stb = [&](int j) {
            const int st = (j & ~7) | ((j + rot) & 7);
#pragma unroll
            for (int i = 0; i < 2; ++i) dma4(bv + 4 * i, ws + WS_HT + (size_t)st * 4 * 16384, lds0 + (j & 3) * 16384 + 8192 + i * 4096);
        };
        for (int i = 0; i < HEAD0; ++i) __builtin_amdgcn_s_sleep(16);
        stb(0); sta(0); stb(1); sta(1); stb(2); sta(2);
        for (int i = 0; i < HEAD1; ++i) __builtin_amdgcn_s_sleep(16);
        for (int j = 0; j < 61; ++j) { WAITV_BAR(32); if (j == 0) LSTAMP(8); if (j == 16) LSTAMP(11); if (j == 32) LSTAMP(12); stb(j + 3); sta(j + 3); }
        WAITV_BAR(32); WAITV_BAR(16); WAITV_BAR(0);
    }
    {
        const int kh = xcd >> 2, st_ = (xcd & 1) * 8 + (r & 7), dt = ((xcd >> 1) & 1) * 4 + (r >> 3), rot = 2 * (((r & 7) + (r >> 3)) & 3);
        unsigned av[8], bv[8];
#pragma unroll
        for (int i = 0; i < 8; ++i) {
            av[i] = off4((i * 4 + (lane >> 4)) * (SEQ * 2) + st_ * 256 + sw256, i);
            bv[i] = off4((i >> 1) * 16384 + (dt * 128 + (i & 1) * 64 + lane) * 16, i);
        }
        const char* ab = A.w2 + (size_t)kh * 1792 * (SEQ * 2);
        const char* bb = ws + WS_G + (size_t)kh * 224 * 16384;
        auto sta = [&](int j) {
            const int st = (j & ~7) | ((j + rot) & 7);
#pragma unroll
            for (int i = 0; i < 2; ++i) dma4(av + 4 * i, ab + (size_t)st * 32 * (SEQ * 2), lds0 + (j & 3) * 16384 + i * 4096);
        };
        auto stb = [&](int j) {
            const int st = (j & ~7) | ((j + rot) & 7);
#pragma unroll
            for (int i = 0; i < 2; ++i) dma4c(bv + 4 * i, bb + (size_t)st * 4 * 16384, lds0 + (j & 3) * 16384 + 8192 + i * 4096);
        };
        sta(0); sta(1); sta(2);
        if (r < 28) {
            BARRIER();
            arrive(ws, CT_G + (((xcd & 1) * 4 + (r & 3)) * 2 + (xcd >> 2)) * 4, 4, lane);
        }
        LSTAMP(13);
        wait_count(ws, CT_G + (dt * 2 + kh) * 4 + (r & 3), 14);
        LSTAMP(14);
        stb(0); stb(1); stb(2);
        WAITV_BAR(16); stb(3); sta(3);
        WAITV_BAR(24); stb(4); sta(4);
        for (int j = 2; j < 53; ++j) { WAITV_BAR(32); stb(j + 3); sta(j + 3); }
        WAITV_BAR(32); WAITV_BAR(16); WAITV_BAR(0);
        BARRIER();
        arrive(ws, CT_Y + st_ * 8 + dt, 1, lane);
    }
}

__device__ __forceinline__ float gelu_tanh(float u) {
    const float z = u * fmaf(u * u, -0.044715f * 2.302208198f, -2.302208198f);
    return u * __builtin_amdgcn_rcpf(1.f + __builtin_amdgcn_exp2f(z));
}

__device__ __forceinline__ void side_b(const Args& A, char* lds, int wid) {
    char* ws = A.ws;
    const unsigned lds0 = (unsigned)(uintptr_t)lds;
    const int gi = blockIdx.x - 256, xcd = gi & 7, r = gi >> 3;
    const rsrc_t rw = __builtin_amdgcn_make_buffer_rsrc(ws, 0, WS_END, 0x00020000);
    unsigned cnt;
    __builtin_amdgcn_s_setprio(PRIO_B);
#define MIX(TXT) asm volatile("s_nop 1\n" TXT \
        : [cnt] "=&s"(cnt), "+{v[0:15]}"(acc[0][0]), "+{v[16:31]}"(acc[0][1]), "+{v[32:47]}"(acc[1][0]), "+{v[48:63]}"(acc[1][1]), "+{v[80:95]}"(fy) \
        : "{v96}"(tb), "{v97}"(tb ^ 64), "{v98}"(rb) : "memory", "scc", CLOB16(64, 65, 66, 67, 68, 69, 70, 71, 72, 73, 74, 75, 76, 77, 78, 79))
    if (r < 28) {
        const int lane = fresh_lane(), h = lane >> 5, q4 = (lane & 15) >> 2, wf = wid >> 1, wd = wid & 1;
        const int dt = (xcd & 1) * 4 + (r & 3), ft = (xcd >> 1) * 7 + (r >> 2);
        const unsigned tb = lds0 + (8 * h + q4) * 256 + (((2 * wf) ^ q4) * 64) + ((lane >> 4) & 1) * 32 + (lane & 3) * 8;
        const unsigned rb = lds0 + 8192 + h * 2048 + (wd * 64 + (lane & 31)) * 16;
        f32x16 acc[2][2] = {}, fy = {};
        MIX(ASM_MIX1);
        __builtin_amdgcn_s_setprio(PRIO_EPI);
        STAMP(2);
#pragma unroll
        for (int fi = 0; fi < 2; ++fi) {
            const int fb = ft * 4 + wf * 2 + fi;
            float bias[16];
#pragma unroll
            for (int k = 0; k < 4; ++k) {
                const u32x2 bv = *(const u32x2*)(A.b1 + (fb * 32 + 8 * k + 4 * h) * 2);
                bias[4 * k] = bflo(bv[0]); bias[4 * k + 1] = bfhi(bv[0]); bias[4 * k + 2] = bflo(bv[1]); bias[4 * k + 3] = bfhi(bv[1]);
            }
#pragma unroll
            for (int di = 0; di < 2; ++di) {
                f32x16 u;
#pragma unroll
                for (int k = 0; k < 16; ++k) u[k] = gelu_tanh(acc[fi][di][k] + bias[k]);
                const unsigned off = WS_G + ((fb * 4 + h) * DM + dt * 128 + wd * 64 + di * 32 + (lane & 31)) * 16;
                st16(rw, off, pack8(u, 0, 1.f));
                st16(rw, off + 2 * DM * 16, pack8(u, 1, 1.f));
            }
        }
        WAITV_BAR(0);
    }
    __builtin_amdgcn_s_setprio(PRIO_B);
    STAMP(3);
    {
        const int lane = fresh_lane(), h = lane >> 5, q4 = (lane & 15) >> 2, wsq = wid >> 1, wd = wid & 1;
        const int kh = xcd >> 2, st_ = (xcd & 1) * 8 + (r & 7), dt = ((xcd >> 1) & 1) * 4 + (r >> 3);
        const unsigned tb = lds0 + (4 * h + q4) * 256 + (((2 * wsq) ^ q4) * 64) + ((lane >> 4) & 1) * 32 + (lane & 3) * 8;
        const unsigned rb = lds0 + 8192 + h * 2048 + (wd * 64 + (lane & 31)) * 16;
        f32x16 acc[2][2] = {}, fy = {};
        MIX(ASM_MIX2);
        __builtin_amdgcn_s_setprio(PRIO_EPI);
        STAMP(4);
        const unsigned dst = WS_Y + (((st_ * 8 + dt) * 2 + kh) * 2 + wsq) * 16384;
#pragma unroll
        for (int di = 0; di < 2; ++di)
#pragma unroll
            for (int si = 0; si < 2; ++si)
#pragma unroll
                for (int c = 0; c < 2; ++c)
                    st16(rw, dst + (si * 4 + wd * 2 + di) * 2048 + c * 1024 + lane * 16, pack8(acc[di][si], c, 1.f));
        WAITV_BAR(0);
    }
#undef MIX
    STAMP(7);
}

__global__ __launch_bounds__(512, 4) void k_main(const char* x, const char* wq, const char* wo, const char* w1, const char* b1, const char* w2,
                                                  const char* b2, char* ws, char* out) {
    __shared__ __attribute__((aligned(1024))) char lds[LDS_BYTES];
    const int wid = __builtin_amdgcn_readfirstlane(threadIdx.x >> 6);
    if (wid > 4) return;
    const Args A = {x, wq, wo, w1, b1, w2, b2, ws, out};
    STAMP(0);
    if (wid == 4) {
        __builtin_amdgcn_s_setprio(3);
        if (blockIdx.x < 256) side_a_loader(A, (unsigned)(uintptr_t)lds, threadIdx.x & 63);
        else side_b_loader(A, (unsigned)(uintptr_t)lds, threadIdx.x & 63);
    } else {
        if (blockIdx.x < 256) side_a(A, lds, wid);
        else side_b(A, lds, wid);
    }
}

extern "C" void kernel_launch(void* const* d_in, const int* in_sizes, int n_in, void* d_out, int out_size, void* d_ws, size_t ws_size,
                              hipStream_t stream) {
    k_pre<<<256, 512, 0, stream>>>((const char*)d_in[0], (const char*)d_in[2], (const char*)d_in[3], (const char*)d_in[10], (char*)d_ws);
    k_main<<<512, 512, 0, stream>>>((const char*)d_in[0], (const char*)d_in[1], (const char*)d_in[4], (const char*)d_in[6], (const char*)d_in[7],
                                    (const char*)d_in[8], (const char*)d_in[9], (char*)d_ws, (char*)d_out);
}
```

```cpp
#include <hip/hip_runtime.h>
#include <hip/hip_bf16.h>
#include <stdint.h>

typedef short bf16x8 __attribute__((ext_vector_type(8)));
typedef float f32x16 __attribute__((ext_vector_type(16)));
typedef float f32x8 __attribute__((ext_vector_type(8)));
typedef float f32x4 __attribute__((ext_vector_type(4)));
typedef unsigned u32x4 __attribute__((ext_vector_type(4)));
typedef unsigned u32x2 __attribute__((ext_vector_type(2)));
typedef __attribute__((address_space(3))) const char* lds_cptr;
typedef __attribute__((address_space(1))) unsigned gu32;
typedef __amdgpu_buffer_rsrc_t rsrc_t;

constexpr int SEQ = 2048, DM = 1024, DF = 3584;
constexpr unsigned CT_G = 0, CT_O = 64, CT_Y = 128, CT_KV = 256;
constexpr unsigned WS_KV = 1u << 20;
constexpr unsigned WS_GATE = WS_KV + (256u << 10);
constexpr unsigned WS_HT = 2u << 20;
constexpr unsigned WS_OP = 6u << 20;
constexpr unsigned WS_G = 10u << 20;
constexpr unsigned WS_Y = 18u << 20;
constexpr unsigned WS_END = 26u << 20;
constexpr int SC1 = 16, NT = 2;
constexpr int LDS_K0 = 57344, LDS_KN = 59392, LDS_PF = 66560, LDS_X = 73728, LDS_BYTES = 81920;
constexpr float QSCALE = 0.17677669529663687f * 1.4426950408889634f;
constexpr float SCORE_BOUND = 60.f;

constexpr int POLL_SLEEP = 2;
constexpr int KV_HEAD = 11;
constexpr unsigned TOUCH_PLAN = 0x0222200;
constexpr int PRIO_EPI = 2;
#define MFMA32(a, b, c) __builtin_amdgcn_mfma_f32_32x32x16_bf16(a, b, c, 0, 0, 0)
#define BC8(x) __builtin_bit_cast(bf16x8, x)
#define BARRIER() asm volatile("s_barrier" ::: "memory")
#define WAITV_BAR(N) asm volatile("s_waitcnt vmcnt(%0)\n\ts_barrier" ::"i"(N) : "memory")
#define LGKM_BAR() asm volatile("s_waitcnt lgkmcnt(0)\n\ts_barrier" ::: "memory")
#define CLOB8(a, b, c, d, e, f, g, h) "v" #a, "v" #b, "v" #c, "v" #d, "v" #e, "v" #f, "v" #g, "v" #h
#define CLOB16(a, b, c, d, e, f, g, h, i, j, k, l, m, n, o, p) CLOB8(a, b, c, d, e, f, g, h), CLOB8(i, j, k, l, m, n, o, p)

__device__ __forceinline__ unsigned cvtpk(float lo, float hi) {
    typedef __bf16 bf2 __attribute__((ext_vector_type(2)));
    typedef float f2 __attribute__((ext_vector_type(2)));
    return __builtin_bit_cast(unsigned, __builtin_convertvector((f2){lo, hi}, bf2));
}
__device__ __forceinline__ float bflo(unsigned u) { return __uint_as_float(u << 16); }
__device__ __forceinline__ float bfhi(unsigned u) { return __uint_as_float(u & 0xffff0000u); }
__device__ __forceinline__ u32x4 pack8(const f32x16& a, int c, float m) {
    return (u32x4){cvtpk(a[8 * c] * m, a[8 * c + 1] * m), cvtpk(a[8 * c + 2] * m, a[8 * c + 3] * m),
                   cvtpk(a[8 * c + 4] * m, a[8 * c + 5] * m), cvtpk(a[8 * c + 6] * m, a[8 * c + 7] * m)};
}
__device__ __forceinline__ float sumsq8(u32x4 v) {
    float n = 0.f;
#pragma unroll
    for (int i = 0; i < 4; ++i) n = fmaf(bflo(v[i]), bflo(v[i]), fmaf(bfhi(v[i]), bfhi(v[i]), n));
    return n;
}
#define DMA4_TXT(SC) "s_mov_b32 m0, %5\n\ts_nop 0\n\tglobal_load_lds_dwordx4 %0, %4" SC "\n\tglobal_load_lds_dwordx4 %1, %4 offset:1024" SC \
                     "\n\tglobal_load_lds_dwordx4 %2, %4 offset:2048" SC "\n\tglobal_load_lds_dwordx4 %3, %4 offset:3072" SC
__device__ __forceinline__ void dma4(const unsigned* v, const char* sbase, unsigned lds) {
    asm volatile(DMA4_TXT("") ::"v"(v[0]), "v"(v[1]), "v"(v[2]), "v"(v[3]), "s"(sbase - 3072), "s"(lds) : "memory");
}
__device__ __forceinline__ void dma4c(const unsigned* v, const char* sbase, unsigned lds) {
    asm volatile(DMA4_TXT(" sc1") ::"v"(v[0]), "v"(v[1]), "v"(v[2]), "v"(v[3]), "s"(sbase - 3072), "s"(lds) : "memory");
}
__device__ __forceinline__ unsigned off4(unsigned want, int piece) { return want + 3072 - (piece & 3) * 1024; }
__device__ __forceinline__ void st16(rsrc_t rw, unsigned off, u32x4 v) { __builtin_amdgcn_raw_buffer_store_b128(v, rw, off, 0, SC1); }
typedef short s16x4 __attribute__((ext_vector_type(4)));
__device__ __forceinline__ s16x4 ldstr(lds_cptr p) {
    return __builtin_bit_cast(s16x4, __builtin_amdgcn_ds_read_tr16_b64_v4i16((__attribute__((address_space(3))) s16x4*)p));
}
__device__ __forceinline__ bf16x8 lds128(lds_cptr p) { return *(const __attribute__((address_space(3))) bf16x8*)p; }
__device__ __forceinline__ int fresh_lane() {
    int l;
    asm volatile("v_mbcnt_lo_u32_b32 %0, -1, 0\n\tv_mbcnt_hi_u32_b32 %0, -1, %0" : "=v"(l));
    return l;
}
template <int N>
__device__ __forceinline__ unsigned row_ror(unsigned v) { return __builtin_amdgcn_update_dpp(0u, v, 0x120 + N, 0xf, 0xf, false); }
template <int N>
__device__ __forceinline__ float row_ror(float v) { return __uint_as_float(row_ror<N>(__float_as_uint(v))); }
__device__ __forceinline__ float wave_sum(float v) {
    v += row_ror<8>(v); v += row_ror<4>(v); v += row_ror<2>(v); v += row_ror<1>(v);
    const auto a = __builtin_amdgcn_permlane16_swap(__float_as_uint(v), __float_as_uint(v), false, false);
    v = __uint_as_float(a[0]) + __uint_as_float(a[1]);
    const auto b = __builtin_amdgcn_permlane32_swap(__float_as_uint(v), __float_as_uint(v), false, false);
    return __uint_as_float(b[0]) + __uint_as_float(b[1]);
}
__device__ __forceinline__ unsigned wave_umax(unsigned v) {
    v = max(v, row_ror<8>(v)); v = max(v, row_ror<4>(v)); v = max(v, row_ror<2>(v)); v = max(v, row_ror<1>(v));
    const auto a = __builtin_amdgcn_permlane16_swap(v, v, false, false);
    v = max(a[0], a[1]);
    const auto b = __builtin_amdgcn_permlane32_swap(v, v, false, false);
    return max(b[0], b[1]);
}
__device__ __forceinline__ void touch(const char* p, int lane, unsigned lds) {
    asm volatile("s_mov_b32 m0, %2\n\ts_nop 0\n\tglobal_load_lds_dword %0, %1" ::"v"(lane * 128), "s"(p), "s"(lds) : "memory");
}
__device__ __forceinline__ void wait_count(char* ws, unsigned line, unsigned n) {
    for (unsigned spins = 0; spins < (1u << 20); ++spins) {
        if (__builtin_amdgcn_readfirstlane(__hip_atomic_load((gu32*)ws + line * 32, __ATOMIC_RELAXED, __HIP_MEMORY_SCOPE_AGENT)) >= n) break;
        __builtin_amdgcn_s_sleep(POLL_SLEEP);
    }
    asm volatile("" ::: "memory");
}
#define ASM_ATTN \
    "s_mov_b32 %[cnt], 4\n" \
    "1:\n" \
    "s_waitcnt lgkmcnt(2)\n" \
    "v_mfma_f32_32x32x16_bf16 v[32:47], v[108:111], v[80:83], 0\n" \
    "v_exp_f32 v48, v48\n" \
    "v_exp_f32 v49, v49\n" \
    "v_exp_f32 v50, v50\n" \
    "v_exp_f32 v51, v51\n" \
    "v_cvt_pk_bf16_f32 v72, v48, v49\n" \
    "v_mfma_f32_32x32x16_bf16 v[32:47], v[112:115], v[84:87], v[32:47]\n" \
    "v_exp_f32 v52, v52\n" \
    "v_exp_f32 v53, v53\n" \
    "v_cvt_pk_bf16_f32 v73, v50, v51\n" \
    "v_exp_f32 v54, v54\n" \
    "v_exp_f32 v55, v55\n" \
    "s_waitcnt lgkmcnt(1)\n" \
    "v_mfma_f32_32x32x16_bf16 v[0:15], v[116:119], v[64:67], v[0:15]\n" \
    "v_cvt_pk_bf16_f32 v74, v52, v53\n" \
    "v_exp_f32 v56, v56\n" \
    "v_exp_f32 v57, v57\n" \
    "v_cvt_pk_bf16_f32 v75, v54, v55\n" \
    "v_exp_f32 v58, v58\n" \
    "s_waitcnt lgkmcnt(0)\n" \
    "v_mfma_f32_32x32x16_bf16 v[0:15], v[120:123], v[68:71], v[0:15]\n" \
    "v_exp_f32 v59, v59\n" \
    "v_cvt_pk_bf16_f32 v76, v56, v57\n" \
    "v_exp_f32 v60, v60\n" \
    "v_exp_f32 v61, v61\n" \
    "v_cvt_pk_bf16_f32 v77, v58, v59\n" \
    "v_exp_f32 v62, v62\n" \
    "v_smfmac_f32_16x16x64_bf16 v[100:103], v[96:99], v[64:71], v126\n" \
    "v_exp_f32 v63, v63\n" \
    "v_cvt_pk_bf16_f32 v78, v60, v61\n" \
    "v_cvt_pk_bf16_f32 v79, v62, v63\n" \
    "v_mfma_f32_32x32x16_bf16 v[48:63], v[108:111], v[88:91], 0\n" \
    "v_exp_f32 v32, v32\n" \
    "ds_read_b128 v[108:111], v125 offset:45056\n" \
    "v_exp_f32 v33, v33\n" \
    "v_exp_f32 v34, v34\n" \
    "v_exp_f32 v35, v35\n" \
    "v_cvt_pk_bf16_f32 v64, v32, v33\n" \
    "v_mfma_f32_32x32x16_bf16 v[48:63], v[112:115], v[92:95], v[48:63]\n" \
    "v_exp_f32 v36, v36\n" \
    "ds_read_b128 v[112:115], v125 offset:46080\n" \
    "v_exp_f32 v37, v37\n" \
    "v_cvt_pk_bf16_f32 v65, v34, v35\n" \
    "v_exp_f32 v38, v38\n" \
    "v_exp_f32 v39, v39\n" \
    "v_mfma_f32_32x32x16_bf16 v[16:31], v[116:119], v[72:75], v[16:31]\n" \
    "v_cvt_pk_bf16_f32 v66, v36, v37\n" \
    "ds_read_b128 v[116:119], v125 offset:47104\n" \
    "v_exp_f32 v40, v40\n" \
    "v_exp_f32 v41, v41\n" \
    "v_cvt_pk_bf16_f32 v67, v38, v39\n" \
    "v_exp_f32 v42, v42\n" \
    "v_mfma_f32_32x32x16_bf16 v[16:31], v[120:123], v[76:79], v[16:31]\n" \
    "v_exp_f32 v43, v43\n" \
    "ds_read_b128 v[120:123], v125 offset:48128\n" \
    "v_cvt_pk_bf16_f32 v68, v40, v41\n" \
    "v_exp_f32 v44, v44\n" \
    "v_exp_f32 v45, v45\n" \
    "v_cvt_pk_bf16_f32 v69, v42, v43\n" \
    "v_exp_f32 v46, v46\n" \
    "v_smfmac_f32_16x16x64_bf16 v[104:107], v[96:99], v[72:79], v126\n" \
    "v_exp_f32 v47, v47\n" \
    "v_cvt_pk_bf16_f32 v70, v44, v45\n" \
    "v_cvt_pk_bf16_f32 v71, v46, v47\n" \
    "s_waitcnt lgkmcnt(2)\n" \
    "v_mfma_f32_32x32x16_bf16 v[32:47], v[108:111], v[80:83], 0\n" \
    "v_exp_f32 v48, v48\n" \
    "v_exp_f32 v49, v49\n" \
    "v_exp_f32 v50, v50\n" \
    "v_exp_f32 v51, v51\n" \
    "v_cvt_pk_bf16_f32 v72, v48, v49\n" \
    "v_mfma_f32_32x32x16_bf16 v[32:47], v[112:115], v[84:87], v[32:47]\n" \
    "v_exp_f32 v52, v52\n" \
    "v_exp_f32 v53, v53\n" \
    "v_cvt_pk_bf16_f32 v73, v50, v51\n" \
    "v_exp_f32 v54, v54\n" \
    "v_exp_f32 v55, v55\n" \
    "s_waitcnt lgkmcnt(1)\n" \
    "v_mfma_f32_32x32x16_bf16 v[0:15], v[116:119], v[64:67], v[0:15]\n" \
    "v_cvt_pk_bf16_f32 v74, v52, v53\n" \
    "v_exp_f32 v56, v56\n" \
    "v_exp_f32 v57, v57\n" \
    "v_cvt_pk_bf16_f32 v75, v54, v55\n" \
    "v_exp_f32 v58, v58\n" \
    "s_waitcnt lgkmcnt(0)\n" \
    "v_mfma_f32_32x32x16_bf16 v[0:15], v[120:123], v[68:71], v[0:15]\n" \
    "v_exp_f32 v59, v59\n" \
    "v_cvt_pk_bf16_f32 v76, v56, v57\n" \
    "v_exp_f32 v60, v60\n" \
    "v_exp_f32 v61, v61\n" \
    "v_cvt_pk_bf16_f32 v77, v58, v59\n" \
    "v_exp_f32 v62, v62\n" \
    "v_smfmac_f32_16x16x64_bf16 v[100:103], v[96:99], v[64:71], v126\n" \
    "v_exp_f32 v63, v63\n" \
    "v_cvt_pk_bf16_f32 v78, v60, v61\n" \
    "v_cvt_pk_bf16_f32 v79, v62, v63\n" \
    "v_mfma_f32_32x32x16_bf16 v[48:63], v[108:111], v[88:91], 0\n" \
    "v_exp_f32 v32, v32\n" \
    "ds_read_b128 v[108:111], v124 offset:24576\n" \
    "v_exp_f32 v33, v33\n" \
    "v_exp_f32 v34, v34\n" \
    "v_exp_f32 v35, v35\n" \
    "v_cvt_pk_bf16_f32 v64, v32, v33\n" \
    "v_mfma_f32_32x32x16_bf16 v[48:63], v[112:115], v[92:95], v[48:63]\n" \
    "v_exp_f32 v36, v36\n" \
    "ds_read_b128 v[112:115], v124 offset:25600\n" \
    "v_exp_f32 v37, v37\n" \
    "v_cvt_pk_bf16_f32 v65, v34, v35\n" \
    "v_exp_f32 v38, v38\n" \
    "v_exp_f32 v39, v39\n" \
    "v_mfma_f32_32x32x16_bf16 v[16:31], v[116:119], v[72:75], v[16:31]\n" \
    "v_cvt_pk_bf16_f32 v66, v36, v37\n" \
    "ds_read_b128 v[116:119], v124 offset:26624\n" \
    "v_exp_f32 v40, v40\n" \
    "v_exp_f32 v41, v41\n" \
    "v_cvt_pk_bf16_f32 v67, v38, v39\n" \
    "v_exp_f32 v42, v42\n" \
    "v_mfma_f32_32x32x16_bf16 v[16:31], v[120:123], v[76:79], v[16:31]\n" \
    "v_exp_f32 v43, v43\n" \
    "ds_read_b128 v[120:123], v124 offset:27648\n" \
    "v_cvt_pk_bf16_f32 v68, v40, v41\n" \
    "v_exp_f32 v44, v44\n" \
    "v_exp_f32 v45, v45\n" \
    "v_cvt_pk_bf16_f32 v69, v42, v43\n" \
    "v_exp_f32 v46, v46\n" \
    "v_smfmac_f32_16x16x64_bf16 v[104:107], v[96:99], v[72:79], v126\n" \
    "v_exp_f32 v47, v47\n" \
    "v_cvt_pk_bf16_f32 v70, v44, v45\n" \
    "v_cvt_pk_bf16_f32 v71, v46, v47\n" \
    "s_waitcnt lgkmcnt(2)\n" \
    "v_mfma_f32_32x32x16_bf16 v[32:47], v[108:111], v[80:83], 0\n" \
    "v_exp_f32 v48, v48\n" \
    "v_exp_f32 v49, v49\n" \
    "v_exp_f32 v50, v50\n" \
    "v_exp_f32 v51, v51\n" \
    "v_cvt_pk_bf16_f32 v72, v48, v49\n" \
    "v_mfma_f32_32x32x16_bf16 v[32:47], v[112:115], v[84:87], v[32:47]\n" \
    "v_exp_f32 v52, v52\n" \
    "v_exp_f32 v53, v53\n" \
    "v_cvt_pk_bf16_f32 v73, v50, v51\n" \
    "v_exp_f32 v54, v54\n" \
    "v_exp_f32 v55, v55\n" \
    "s_waitcnt lgkmcnt(1)\n" \
    "v_mfma_f32_32x32x16_bf16 v[0:15], v[116:119], v[64:67], v[0:15]\n" \
    "v_cvt_pk_bf16_f32 v74, v52, v53\n" \
    "v_exp_f32 v56, v56\n" \
    "v_exp_f32 v57, v57\n" \
    "v_cvt_pk_bf16_f32 v75, v54, v55\n" \
    "v_exp_f32 v58, v58\n" \
    "s_waitcnt lgkmcnt(0)\n" \
    "v_mfma_f32_32x32x16_bf16 v[0:15], v[120:123], v[68:71], v[0:15]\n" \
    "v_exp_f32 v59, v59\n" \
    "v_cvt_pk_bf16_f32 v76, v56, v57\n" \
    "v_exp_f32 v60, v60\n" \
    "v_exp_f32 v61, v61\n" \
    "v_cvt_pk_bf16_f32 v77, v58, v59\n" \
    "v_exp_f32 v62, v62\n" \
    "v_smfmac_f32_16x16x64_bf16 v[100:103], v[96:99], v[64:71], v126\n" \
    "v_exp_f32 v63, v63\n" \
    "v_cvt_pk_bf16_f32 v78, v60, v61\n" \
    "v_cvt_pk_bf16_f32 v79, v62, v63\n" \
    "v_mfma_f32_32x32x16_bf16 v[48:63], v[108:111], v[88:91], 0\n" \
    "v_exp_f32 v32, v32\n" \
    "ds_read_b128 v[108:111], v124 offset:28672\n" \
    "v_exp_f32 v33, v33\n" \
    "v_exp_f32 v34, v34\n" \
    "v_exp_f32 v35, v35\n" \
    "v_cvt_pk_bf16_f32 v64, v32, v33\n" \
    "v_mfma_f32_32x32x16_bf16 v[48:63], v[112:115], v[92:95], v[48:63]\n" \
    "v_exp_f32 v36, v36\n" \
    "ds_read_b128 v[112:115], v124 offset:29696\n" \
    "v_exp_f32 v37, v37\n" \
    "v_cvt_pk_bf16_f32 v65, v34, v35\n" \
    "v_exp_f32 v38, v38\n" \
    "v_exp_f32 v39, v39\n" \
    "v_mfma_f32_32x32x16_bf16 v[16:31], v[116:119], v[72:75], v[16:31]\n" \
    "v_cvt_pk_bf16_f32 v66, v36, v37\n" \
    "ds_read_b128 v[116:119], v124 offset:30720\n" \
    "v_exp_f32 v40, v40\n" \
    "v_exp_f32 v41, v41\n" \
    "v_cvt_pk_bf16_f32 v67, v38, v39\n" \
    "v_exp_f32 v42, v42\n" \
    "v_mfma_f32_32x32x16_bf16 v[16:31], v[120:123], v[76:79], v[16:31]\n" \
    "v_exp_f32 v43, v43\n" \
    "ds_read_b128 v[120:123], v124 offset:31744\n" \
    "v_cvt_pk_bf16_f32 v68, v40, v41\n" \
    "v_exp_f32 v44, v44\n" \
    "v_exp_f32 v45, v45\n" \
    "v_cvt_pk_bf16_f32 v69, v42, v43\n" \
    "v_exp_f32 v46, v46\n" \
    "v_smfmac_f32_16x16x64_bf16 v[104:107], v[96:99], v[72:79], v126\n" \
    "v_exp_f32 v47, v47\n" \
    "v_cvt_pk_bf16_f32 v70, v44, v45\n" \
    "v_cvt_pk_bf16_f32 v71, v46, v47\n" \
    "s_waitcnt lgkmcnt(2)\n" \
    "v_mfma_f32_32x32x16_bf16 v[32:47], v[108:111], v[80:83], 0\n" \
    "v_exp_f32 v48, v48\n" \
    "v_exp_f32 v49, v49\n" \
    "v_exp_f32 v50, v50\n" \
    "v_exp_f32 v51, v51\n" \
    "v_cvt_pk_bf16_f32 v72, v48, v49\n" \
    "v_mfma_f32_32x32x16_bf16 v[32:47], v[112:115], v[84:87], v[32:47]\n" \
    "v_exp_f32 v52, v52\n" \
    "v_exp_f32 v53, v53\n" \
    "v_cvt_pk_bf16_f32 v73, v50, v51\n" \
    "v_exp_f32 v54, v54\n" \
    "v_exp_f32 v55, v55\n" \
    "s_waitcnt lgkmcnt(1)\n" \
    "v_mfma_f32_32x32x16_bf16 v[0:15], v[116:119], v[64:67], v[0:15]\n" \
    "v_cvt_pk_bf16_f32 v74, v52, v53\n" \
    "v_exp_f32 v56, v56\n" \
    "v_exp_f32 v57, v57\n" \
    "v_cvt_pk_bf16_f32 v75, v54, v55\n" \
    "v_exp_f32 v58, v58\n" \
    "s_waitcnt lgkmcnt(0)\n" \
    "v_mfma_f32_32x32x16_bf16 v[0:15], v[120:123], v[68:71], v[0:15]\n" \
    "v_exp_f32 v59, v59\n" \
    "v_cvt_pk_bf16_f32 v76, v56, v57\n" \
    "v_exp_f32 v60, v60\n" \
    "v_exp_f32 v61, v61\n" \
    "v_cvt_pk_bf16_f32 v77, v58, v59\n" \
    "v_exp_f32 v62, v62\n" \
    "v_smfmac_f32_16x16x64_bf16 v[100:103], v[96:99], v[64:71], v126\n" \
    "v_exp_f32 v63, v63\n" \
    "v_cvt_pk_bf16_f32 v78, v60, v61\n" \
    "v_cvt_pk_bf16_f32 v79, v62, v63\n" \
    "v_mfma_f32_32x32x16_bf16 v[48:63], v[108:111], v[88:91], 0\n" \
    "v_exp_f32 v32, v32\n" \
    "ds_read_b128 v[108:111], v125 offset:0\n" \
    "v_exp_f32 v33, v33\n" \
    "v_exp_f32 v34, v34\n" \
    "v_exp_f32 v35, v35\n" \
    "v_cvt_pk_bf16_f32 v64, v32, v33\n" \
    "v_mfma_f32_32x32x16_bf16 v[48:63], v[112:115], v[92:95], v[48:63]\n" \
    "v_exp_f32 v36, v36\n" \
    "ds_read_b128 v[112:115], v125 offset:1024\n" \
    "v_exp_f32 v37, v37\n" \
    "v_cvt_pk_bf16_f32 v65, v34, v35\n" \
    "v_exp_f32 v38, v38\n" \
    "v_exp_f32 v39, v39\n" \
    "v_mfma_f32_32x32x16_bf16 v[16:31], v[116:119], v[72:75], v[16:31]\n" \
    "v_cvt_pk_bf16_f32 v66, v36, v37\n" \
    "ds_read_b128 v[116:119], v125 offset:2048\n" \
    "v_exp_f32 v40, v40\n" \
    "v_exp_f32 v41, v41\n" \
    "v_cvt_pk_bf16_f32 v67, v38, v39\n" \
    "v_exp_f32 v42, v42\n" \
    "v_mfma_f32_32x32x16_bf16 v[16:31], v[120:123], v[76:79], v[16:31]\n" \
    "v_exp_f32 v43, v43\n" \
    "ds_read_b128 v[120:123], v125 offset:3072\n" \
    "v_cvt_pk_bf16_f32 v68, v40, v41\n" \
    "v_exp_f32 v44, v44\n" \
    "v_exp_f32 v45, v45\n" \
    "v_cvt_pk_bf16_f32 v69, v42, v43\n" \
    "v_exp_f32 v46, v46\n" \
    "v_smfmac_f32_16x16x64_bf16 v[104:107], v[96:99], v[72:79], v126\n" \
    "v_exp_f32 v47, v47\n" \
    "v_cvt_pk_bf16_f32 v70, v44, v45\n" \
    "v_cvt_pk_bf16_f32 v71, v46, v47\n" \
    "s_waitcnt lgkmcnt(2)\n" \
    "v_mfma_f32_32x32x16_bf16 v[32:47], v[108:111], v[80:83], 0\n" \
    "v_exp_f32 v48, v48\n" \
    "v_exp_f32 v49, v49\n" \
    "v_exp_f32 v50, v50\n" \
    "v_exp_f32 v51, v51\n" \
    "v_cvt_pk_bf16_f32 v72, v48, v49\n" \
    "v_mfma_f32_32x32x16_bf16 v[32:47], v[112:115], v[84:87], v[32:47]\n" \
    "v_exp_f32 v52, v52\n" \
    "v_exp_f32 v53, v53\n" \
    "v_cvt_pk_bf16_f32 v73, v50, v51\n" \
    "v_exp_f32 v54, v54\n" \
    "v_exp_f32 v55, v55\n" \
    "s_waitcnt lgkmcnt(1)\n" \
    "v_mfma_f32_32x32x16_bf16 v[0:15], v[116:119], v[64:67], v[0:15]\n" \
    "v_cvt_pk_bf16_f32 v74, v52, v53\n" \
    "v_exp_f32 v56, v56\n" \
    "v_exp_f32 v57, v57\n" \
    "v_cvt_pk_bf16_f32 v75, v54, v55\n" \
    "v_exp_f32 v58, v58\n" \
    "s_waitcnt lgkmcnt(0)\n" \
    "v_mfma_f32_32x32x16_bf16 v[0:15], v[120:123], v[68:71], v[0:15]\n" \
    "v_exp_f32 v59, v59\n" \
    "v_cvt_pk_bf16_f32 v76, v56, v57\n" \
    "v_exp_f32 v60, v60\n" \
    "v_exp_f32 v61, v61\n" \
    "v_cvt_pk_bf16_f32 v77, v58, v59\n" \
    "v_exp_f32 v62, v62\n" \
    "v_smfmac_f32_16x16x64_bf16 v[100:103], v[96:99], v[64:71], v126\n" \
    "v_exp_f32 v63, v63\n" \
    "v_cvt_pk_bf16_f32 v78, v60, v61\n" \
    "v_cvt_pk_bf16_f32 v79, v62, v63\n" \
    "v_mfma_f32_32x32x16_bf16 v[48:63], v[108:111], v[88:91], 0\n" \
    "v_exp_f32 v32, v32\n" \
    "ds_read_b128 v[108:111], v125 offset:4096\n" \
    "v_exp_f32 v33, v33\n" \
    "v_exp_f32 v34, v34\n" \
    "v_exp_f32 v35, v35\n" \
    "v_cvt_pk_bf16_f32 v64, v32, v33\n" \
    "v_mfma_f32_32x32x16_bf16 v[48:63], v[112:115], v[92:95], v[48:63]\n" \
    "v_exp_f32 v36, v36\n" \
    "ds_read_b128 v[112:115], v125 offset:5120\n" \
    "v_exp_f32 v37, v37\n" \
    "v_cvt_pk_bf16_f32 v65, v34, v35\n" \
    "v_exp_f32 v38, v38\n" \
    "v_exp_f32 v39, v39\n" \
    "v_mfma_f32_32x32x16_bf16 v[16:31], v[116:119], v[72:75], v[16:31]\n" \
    "v_cvt_pk_bf16_f32 v66, v36, v37\n" \
    "ds_read_b128 v[116:119], v125 offset:6144\n" \
    "v_exp_f32 v40, v40\n" \
    "v_exp_f32 v41, v41\n" \
    "v_cvt_pk_bf16_f32 v67, v38, v39\n" \
    "v_exp_f32 v42, v42\n" \
    "v_mfma_f32_32x32x16_bf16 v[16:31], v[120:123], v[76:79], v[16:31]\n" \
    "v_exp_f32 v43, v43\n" \
    "ds_read_b128 v[120:123], v125 offset:7168\n" \
    "v_cvt_pk_bf16_f32 v68, v40, v41\n" \
    "v_exp_f32 v44, v44\n" \
    "v_exp_f32 v45, v45\n" \
    "v_cvt_pk_bf16_f32 v69, v42, v43\n" \
    "v_exp_f32 v46, v46\n" \
    "v_smfmac_f32_16x16x64_bf16 v[104:107], v[96:99], v[72:79], v126\n" \
    "v_exp_f32 v47, v47\n" \
    "v_cvt_pk_bf16_f32 v70, v44, v45\n" \
    "v_cvt_pk_bf16_f32 v71, v46, v47\n" \
    "s_waitcnt lgkmcnt(2)\n" \
    "v_mfma_f32_32x32x16_bf16 v[32:47], v[108:111], v[80:83], 0\n" \
    "v_exp_f32 v48, v48\n" \
    "v_exp_f32 v49, v49\n" \
    "v_exp_f32 v50, v50\n" \
    "v_exp_f32 v51, v51\n" \
    "v_cvt_pk_bf16_f32 v72, v48, v49\n" \
    "v_mfma_f32_32x32x16_bf16 v[32:47], v[112:115], v[84:87], v[32:47]\n" \
    "v_exp_f32 v52, v52\n" \
    "v_exp_f32 v53, v53\n" \
    "v_cvt_pk_bf16_f32 v73, v50, v51\n" \
    "v_exp_f32 v54, v54\n" \
    "v_exp_f32 v55, v55\n" \
    "s_waitcnt lgkmcnt(1)\n" \
    "v_mfma_f32_32x32x16_bf16 v[0:15], v[116:119], v[64:67], v[0:15]\n" \
    "v_cvt_pk_bf16_f32 v74, v52, v53\n" \
    "v_exp_f32 v56, v56\n" \
    "v_exp_f32 v57, v57\n" \
    "v_cvt_pk_bf16_f32 v75, v54, v55\n" \
    "v_exp_f32 v58, v58\n" \
    "s_waitcnt lgkmcnt(0)\n" \
    "v_mfma_f32_32x32x16_bf16 v[0:15], v[120:123], v[68:71], v[0:15]\n" \
    "v_exp_f32 v59, v59\n" \
    "v_cvt_pk_bf16_f32 v76, v56, v57\n" \
    "v_exp_f32 v60, v60\n" \
    "v_exp_f32 v61, v61\n" \
    "v_cvt_pk_bf16_f32 v77, v58, v59\n" \
    "v_exp_f32 v62, v62\n" \
    "v_smfmac_f32_16x16x64_bf16 v[100:103], v[96:99], v[64:71], v126\n" \
    "v_exp_f32 v63, v63\n" \
    "v_cvt_pk_bf16_f32 v78, v60, v61\n" \
    "v_cvt_pk_bf16_f32 v79, v62, v63\n" \
    "v_mfma_f32_32x32x16_bf16 v[48:63], v[108:111], v[88:91], 0\n" \
    "v_exp_f32 v32, v32\n" \
    "ds_read_b128 v[108:111], v125 offset:8192\n" \
    "v_exp_f32 v33, v33\n" \
    "v_exp_f32 v34, v34\n" \
    "v_exp_f32 v35, v35\n" \
    "v_cvt_pk_bf16_f32 v64, v32, v33\n" \
    "v_mfma_f32_32x32x16_bf16 v[48:63], v[112:115], v[92:95], v[48:63]\n" \
    "v_exp_f32 v36, v36\n" \
    "ds_read_b128 v[112:115], v125 offset:9216\n" \
    "v_exp_f32 v37, v37\n" \
    "v_cvt_pk_bf16_f32 v65, v34, v35\n" \
    "v_exp_f32 v38, v38\n" \
    "v_exp_f32 v39, v39\n" \
    "v_mfma_f32_32x32x16_bf16 v[16:31], v[116:119], v[72:75], v[16:31]\n" \
    "v_cvt_pk_bf16_f32 v66, v36, v37\n" \
    "ds_read_b128 v[116:119], v125 offset:10240\n" \
    "v_exp_f32 v40, v40\n" \
    "v_exp_f32 v41, v41\n" \
    "v_cvt_pk_bf16_f32 v67, v38, v39\n" \
    "v_exp_f32 v42, v42\n" \
    "v_mfma_f32_32x32x16_bf16 v[16:31], v[120:123], v[76:79], v[16:31]\n" \
    "v_exp_f32 v43, v43\n" \
    "ds_read_b128 v[120:123], v125 offset:11264\n" \
    "v_cvt_pk_bf16_f32 v68, v40, v41\n" \
    "v_exp_f32 v44, v44\n" \
    "v_exp_f32 v45, v45\n" \
    "v_cvt_pk_bf16_f32 v69, v42, v43\n" \
    "v_exp_f32 v46, v46\n" \
    "v_smfmac_f32_16x16x64_bf16 v[104:107], v[96:99], v[72:79], v126\n" \
    "v_exp_f32 v47, v47\n" \
    "v_cvt_pk_bf16_f32 v70, v44, v45\n" \
    "v_cvt_pk_bf16_f32 v71, v46, v47\n" \
    "s_waitcnt lgkmcnt(2)\n" \
    "v_mfma_f32_32x32x16_bf16 v[32:47], v[108:111], v[80:83], 0\n" \
    "v_exp_f32 v48, v48\n" \
    "v_exp_f32 v49, v49\n" \
    "v_exp_f32 v50, v50\n" \
    "v_exp_f32 v51, v51\n" \
    "v_cvt_pk_bf16_f32 v72, v48, v49\n" \
    "v_mfma_f32_32x32x16_bf16 v[32:47], v[112:115], v[84:87], v[32:47]\n" \
    "v_exp_f32 v52, v52\n" \
    "v_exp_f32 v53, v53\n" \
    "v_cvt_pk_bf16_f32 v73, v50, v51\n" \
    "v_exp_f32 v54, v54\n" \
    "v_exp_f32 v55, v55\n" \
    "s_waitcnt lgkmcnt(1)\n" \
    "v_mfma_f32_32x32x16_bf16 v[0:15], v[116:119], v[64:67], v[0:15]\n" \
    "v_cvt_pk_bf16_f32 v74, v52, v53\n" \
    "v_exp_f32 v56, v56\n" \
    "v_exp_f32 v57, v57\n" \
    "v_cvt_pk_bf16_f32 v75, v54, v55\n" \
    "v_exp_f32 v58, v58\n" \
    "s_waitcnt lgkmcnt(0)\n" \
    "v_mfma_f32_32x32x16_bf16 v[0:15], v[120:123], v[68:71], v[0:15]\n" \
    "v_exp_f32 v59, v59\n" \
    "v_cvt_pk_bf16_f32 v76, v56, v57\n" \
    "v_exp_f32 v60, v60\n" \
    "v_exp_f32 v61, v61\n" \
    "v_cvt_pk_bf16_f32 v77, v58, v59\n" \
    "v_exp_f32 v62, v62\n" \
    "v_smfmac_f32_16x16x64_bf16 v[100:103], v[96:99], v[64:71], v126\n" \
    "v_exp_f32 v63, v63\n" \
    "v_cvt_pk_bf16_f32 v78, v60, v61\n" \
    "v_cvt_pk_bf16_f32 v79, v62, v63\n" \
    "v_mfma_f32_32x32x16_bf16 v[48:63], v[108:111], v[88:91], 0\n" \
    "v_exp_f32 v32, v32\n" \
    "ds_read_b128 v[108:111], v125 offset:12288\n" \
    "v_exp_f32 v33, v33\n" \
    "v_exp_f32 v34, v34\n" \
    "v_exp_f32 v35, v35\n" \
    "v_cvt_pk_bf16_f32 v64, v32, v33\n" \
    "v_mfma_f32_32x32x16_bf16 v[48:63], v[112:115], v[92:95], v[48:63]\n" \
    "v_exp_f32 v36, v36\n" \
    "ds_read_b128 v[112:115], v125 offset:13312\n" \
    "v_exp_f32 v37, v37\n" \
    "v_cvt_pk_bf16_f32 v65, v34, v35\n" \
    "v_exp_f32 v38, v38\n" \
    "v_exp_f32 v39, v39\n" \
    "v_mfma_f32_32x32x16_bf16 v[16:31], v[116:119], v[72:75], v[16:31]\n" \
    "v_cvt_pk_bf16_f32 v66, v36, v37\n" \
    "ds_read_b128 v[116:119], v125 offset:14336\n" \
    "v_exp_f32 v40, v40\n" \
    "v_exp_f32 v41, v41\n" \
    "v_cvt_pk_bf16_f32 v67, v38, v39\n" \
    "v_exp_f32 v42, v42\n" \
    "v_mfma_f32_32x32x16_bf16 v[16:31], v[120:123], v[76:79], v[16:31]\n" \
    "v_exp_f32 v43, v43\n" \
    "ds_read_b128 v[120:123], v125 offset:15360\n" \
    "v_cvt_pk_bf16_f32 v68, v40, v41\n" \
    "v_exp_f32 v44, v44\n" \
    "v_exp_f32 v45, v45\n" \
    "v_cvt_pk_bf16_f32 v69, v42, v43\n" \
    "v_exp_f32 v46, v46\n" \
    "v_smfmac_f32_16x16x64_bf16 v[104:107], v[96:99], v[72:79], v126\n" \
    "v_exp_f32 v47, v47\n" \
    "v_cvt_pk_bf16_f32 v70, v44, v45\n" \
    "v_cvt_pk_bf16_f32 v71, v46, v47\n" \
    "s_waitcnt lgkmcnt(2)\n" \
    "v_mfma_f32_32x32x16_bf16 v[32:47], v[108:111], v[80:83], 0\n" \
    "v_exp_f32 v48, v48\n" \
    "v_exp_f32 v49, v49\n" \
    "v_exp_f32 v50, v50\n" \
    "v_exp_f32 v51, v51\n" \
    "v_cvt_pk_bf16_f32 v72, v48, v49\n" \
    "v_mfma_f32_32x32x16_bf16 v[32:47], v[112:115], v[84:87], v[32:47]\n" \
    "v_exp_f32 v52, v52\n" \
    "v_exp_f32 v53, v53\n" \
    "v_cvt_pk_bf16_f32 v73, v50, v51\n" \
    "v_exp_f32 v54, v54\n" \
    "v_exp_f32 v55, v55\n" \
    "s_waitcnt lgkmcnt(1)\n" \
    "v_mfma_f32_32x32x16_bf16 v[0:15], v[116:119], v[64:67], v[0:15]\n" \
    "v_cvt_pk_bf16_f32 v74, v52, v53\n" \
    "v_exp_f32 v56, v56\n" \
    "v_exp_f32 v57, v57\n" \
    "v_cvt_pk_bf16_f32 v75, v54, v55\n" \
    "v_exp_f32 v58, v58\n" \
    "s_waitcnt lgkmcnt(0)\n" \
    "v_mfma_f32_32x32x16_bf16 v[0:15], v[120:123], v[68:71], v[0:15]\n" \
    "v_exp_f32 v59, v59\n" \
    "v_cvt_pk_bf16_f32 v76, v56, v57\n" \
    "v_exp_f32 v60, v60\n" \
    "v_exp_f32 v61, v61\n" \
    "v_cvt_pk_bf16_f32 v77, v58, v59\n" \
    "v_exp_f32 v62, v62\n" \
    "v_smfmac_f32_16x16x64_bf16 v[100:103], v[96:99], v[64:71], v126\n" \
    "v_exp_f32 v63, v63\n" \
    "v_cvt_pk_bf16_f32 v78, v60, v61\n" \
    "v_cvt_pk_bf16_f32 v79, v62, v63\n" \
    "s_barrier\n" \
    "v_mfma_f32_32x32x16_bf16 v[48:63], v[108:111], v[88:91], 0\n" \
    "v_exp_f32 v32, v32\n" \
    "ds_read_b128 v[108:111], v124 offset:0\n" \
    "v_exp_f32 v33, v33\n" \
    "v_exp_f32 v34, v34\n" \
    "v_exp_f32 v35, v35\n" \
    "v_cvt_pk_bf16_f32 v64, v32, v33\n" \
    "v_mfma_f32_32x32x16_bf16 v[48:63], v[112:115], v[92:95], v[48:63]\n" \
    "v_exp_f32 v36, v36\n" \
    "ds_read_b128 v[112:115], v124 offset:1024\n" \
    "v_exp_f32 v37, v37\n" \
    "v_cvt_pk_bf16_f32 v65, v34, v35\n" \
    "v_exp_f32 v38, v38\n" \
    "v_exp_f32 v39, v39\n" \
    "v_mfma_f32_32x32x16_bf16 v[16:31], v[116:119], v[72:75], v[16:31]\n" \
    "v_cvt_pk_bf16_f32 v66, v36, v37\n" \
    "ds_read_b128 v[116:119], v124 offset:2048\n" \
    "v_exp_f32 v40, v40\n" \
    "v_exp_f32 v41, v41\n" \
    "v_cvt_pk_bf16_f32 v67, v38, v39\n" \
    "v_exp_f32 v42, v42\n" \
    "v_mfma_f32_32x32x16_bf16 v[16:31], v[120:123], v[76:79], v[16:31]\n" \
    "v_exp_f32 v43, v43\n" \
    "ds_read_b128 v[120:123], v124 offset:3072\n" \
    "v_cvt_pk_bf16_f32 v68, v40, v41\n" \
    "v_exp_f32 v44, v44\n" \
    "v_exp_f32 v45, v45\n" \
    "v_cvt_pk_bf16_f32 v69, v42, v43\n" \
    "v_exp_f32 v46, v46\n" \
    "v_smfmac_f32_16x16x64_bf16 v[104:107], v[96:99], v[72:79], v126\n" \
    "v_exp_f32 v47, v47\n" \
    "v_cvt_pk_bf16_f32 v70, v44, v45\n" \
    "v_cvt_pk_bf16_f32 v71, v46, v47\n" \
    "s_waitcnt lgkmcnt(2)\n" \
    "v_mfma_f32_32x32x16_bf16 v[32:47], v[108:111], v[80:83], 0\n" \
    "v_exp_f32 v48, v48\n" \
    "v_exp_f32 v49, v49\n" \
    "v_exp_f32 v50, v50\n" \
    "v_exp_f32 v51, v51\n" \
    "v_cvt_pk_bf16_f32 v72, v48, v49\n" \
    "v_mfma_f32_32x32x16_bf16 v[32:47], v[112:115], v[84:87], v[32:47]\n" \
    "v_exp_f32 v52, v52\n" \
    "v_exp_f32 v53, v53\n" \
    "v_cvt_pk_bf16_f32 v73, v50, v51\n" \
    "v_exp_f32 v54, v54\n" \
    "v_exp_f32 v55, v55\n" \
    "s_waitcnt lgkmcnt(1)\n" \
    "v_mfma_f32_32x32x16_bf16 v[0:15], v[116:119], v[64:67], v[0:15]\n" \
    "v_cvt_pk_bf16_f32 v74, v52, v53\n" \
    "v_exp_f32 v56, v56\n" \
    "v_exp_f32 v57, v57\n" \
    "v_cvt_pk_bf16_f32 v75, v54, v55\n" \
    "v_exp_f32 v58, v58\n" \
    "s_waitcnt lgkmcnt(0)\n" \
    "v_mfma_f32_32x32x16_bf16 v[0:15], v[120:123], v[68:71], v[0:15]\n" \
    "v_exp_f32 v59, v59\n" \
    "v_cvt_pk_bf16_f32 v76, v56, v57\n" \
    "v_exp_f32 v60, v60\n" \
    "v_exp_f32 v61, v61\n" \
    "v_cvt_pk_bf16_f32 v77, v58, v59\n" \
    "v_exp_f32 v62, v62\n" \
    "v_smfmac_f32_16x16x64_bf16 v[100:103], v[96:99], v[64:71], v126\n" \
    "v_exp_f32 v63, v63\n" \
    "v_cvt_pk_bf16_f32 v78, v60, v61\n" \
    "v_cvt_pk_bf16_f32 v79, v62, v63\n" \
    "v_mfma_f32_32x32x16_bf16 v[48:63], v[108:111], v[88:91], 0\n" \
    "v_exp_f32 v32, v32\n" \
    "ds_read_b128 v[108:111], v124 offset:4096\n" \
    "v_exp_f32 v33, v33\n" \
    "v_exp_f32 v34, v34\n" \
    "v_exp_f32 v35, v35\n" \
    "v_cvt_pk_bf16_f32 v64, v32, v33\n" \
    "v_mfma_f32_32x32x16_bf16 v[48:63], v[112:115], v[92:95], v[48:63]\n" \
    "v_exp_f32 v36, v36\n" \
    "ds_read_b128 v[112:115], v124 offset:5120\n" \
    "v_exp_f32 v37, v37\n" \
    "v_cvt_pk_bf16_f32 v65, v34, v35\n" \
    "v_exp_f32 v38, v38\n" \
    "v_exp_f32 v39, v39\n" \
    "v_mfma_f32_32x32x16_bf16 v[16:31], v[116:119], v[72:75], v[16:31]\n" \
    "v_cvt_pk_bf16_f32 v66, v36, v37\n" \
    "ds_read_b128 v[116:119], v124 offset:6144\n" \
    "v_exp_f32 v40, v40\n" \
    "v_exp_f32 v41, v41\n" \
    "v_cvt_pk_bf16_f32 v67, v38, v39\n" \
    "v_exp_f32 v42, v42\n" \
    "v_mfma_f32_32x32x16_bf16 v[16:31], v[120:123], v[76:79], v[16:31]\n" \
    "v_exp_f32 v43, v43\n" \
    "ds_read_b128 v[120:123], v124 offset:7168\n" \
    "v_cvt_pk_bf16_f32 v68, v40, v41\n" \
    "v_exp_f32 v44, v44\n" \
    "v_exp_f32 v45, v45\n" \
    "v_cvt_pk_bf16_f32 v69, v42, v43\n" \
    "v_exp_f32 v46, v46\n" \
    "v_smfmac_f32_16x16x64_bf16 v[104:107], v[96:99], v[72:79], v126\n" \
    "v_exp_f32 v47, v47\n" \
    "v_cvt_pk_bf16_f32 v70, v44, v45\n" \
    "v_cvt_pk_bf16_f32 v71, v46, v47\n" \
    "s_waitcnt lgkmcnt(2)\n" \
    "v_mfma_f32_32x32x16_bf16 v[32:47], v[108:111], v[80:83], 0\n" \
    "v_exp_f32 v48, v48\n" \
    "v_exp_f32 v49, v49\n" \
    "v_exp_f32 v50, v50\n" \
    "v_exp_f32 v51, v51\n" \
    "v_cvt_pk_bf16_f32 v72, v48, v49\n" \
    "v_mfma_f32_32x32x16_bf16 v[32:47], v[112:115], v[84:87], v[32:47]\n" \
    "v_exp_f32 v52, v52\n" \
    "v_exp_f32 v53, v53\n" \
    "v_cvt_pk_bf16_f32 v73, v50, v51\n" \
    "v_exp_f32 v54, v54\n" \
    "v_exp_f32 v55, v55\n" \
    "s_waitcnt lgkmcnt(1)\n" \
    "v_mfma_f32_32x32x16_bf16 v[0:15], v[116:119], v[64:67], v[0:15]\n" \
    "v_cvt_pk_bf16_f32 v74, v52, v53\n" \
    "v_exp_f32 v56, v56\n" \
    "v_exp_f32 v57, v57\n" \
    "v_cvt_pk_bf16_f32 v75, v54, v55\n" \
    "v_exp_f32 v58, v58\n" \
    "s_waitcnt lgkmcnt(0)\n" \
    "v_mfma_f32_32x32x16_bf16 v[0:15], v[120:123], v[68:71], v[0:15]\n" \
    "v_exp_f32 v59, v59\n" \
    "v_cvt_pk_bf16_f32 v76, v56, v57\n" \
    "v_exp_f32 v60, v60\n" \
    "v_exp_f32 v61, v61\n" \
    "v_cvt_pk_bf16_f32 v77, v58, v59\n" \
    "v_exp_f32 v62, v62\n" \
    "v_smfmac_f32_16x16x64_bf16 v[100:103], v[96:99], v[64:71], v126\n" \
    "v_exp_f32 v63, v63\n" \
    "v_cvt_pk_bf16_f32 v78, v60, v61\n" \
    "v_cvt_pk_bf16_f32 v79, v62, v63\n" \
    "v_mfma_f32_32x32x16_bf16 v[48:63], v[108:111], v[88:91], 0\n" \
    "v_exp_f32 v32, v32\n" \
    "ds_read_b128 v[108:111], v124 offset:8192\n" \
    "v_exp_f32 v33, v33\n" \
    "v_exp_f32 v34, v34\n" \
    "v_exp_f32 v35, v35\n" \
    "v_cvt_pk_bf16_f32 v64, v32, v33\n" \
    "v_mfma_f32_32x32x16_bf16 v[48:63], v[112:115], v[92:95], v[48:63]\n" \
    "v_exp_f32 v36, v36\n" \
    "ds_read_b128 v[112:115], v124 offset:9216\n" \
    "v_exp_f32 v37, v37\n" \
    "v_cvt_pk_bf16_f32 v65, v34, v35\n" \
    "v_exp_f32 v38, v38\n" \
    "v_exp_f32 v39, v39\n" \
    "v_mfma_f32_32x32x16_bf16 v[16:31], v[116:119], v[72:75], v[16:31]\n" \
    "v_cvt_pk_bf16_f32 v66, v36, v37\n" \
    "ds_read_b128 v[116:119], v124 offset:10240\n" \
    "v_exp_f32 v40, v40\n" \
    "v_exp_f32 v41, v41\n" \
    "v_cvt_pk_bf16_f32 v67, v38, v39\n" \
    "v_exp_f32 v42, v42\n" \
    "v_mfma_f32_32x32x16_bf16 v[16:31], v[120:123], v[76:79], v[16:31]\n" \
    "v_exp_f32 v43, v43\n" \
    "ds_read_b128 v[120:123], v124 offset:11264\n" \
    "v_cvt_pk_bf16_f32 v68, v40, v41\n" \
    "v_exp_f32 v44, v44\n" \
    "v_exp_f32 v45, v45\n" \
    "v_cvt_pk_bf16_f32 v69, v42, v43\n" \
    "v_exp_f32 v46, v46\n" \
    "v_smfmac_f32_16x16x64_bf16 v[104:107], v[96:99], v[72:79], v126\n" \
    "v_exp_f32 v47, v47\n" \
    "v_cvt_pk_bf16_f32 v70, v44, v45\n" \
    "v_cvt_pk_bf16_f32 v71, v46, v47\n" \
    "s_waitcnt lgkmcnt(2)\n" \
    "v_mfma_f32_32x32x16_bf16 v[32:47], v[108:111], v[80:83], 0\n" \
    "v_exp_f32 v48, v48\n" \
    "v_exp_f32 v49, v49\n" \
    "v_exp_f32 v50, v50\n" \
    "v_exp_f32 v51, v51\n" \
    "v_cvt_pk_bf16_f32 v72, v48, v49\n" \
    "v_mfma_f32_32x32x16_bf16 v[32:47], v[112:115], v[84:87], v[32:47]\n" \
    "v_exp_f32 v52, v52\n" \
    "v_exp_f32 v53, v53\n" \
    "v_cvt_pk_bf16_f32 v73, v50, v51\n" \
    "v_exp_f32 v54, v54\n" \
    "v_exp_f32 v55, v55\n" \
    "s_waitcnt lgkmcnt(1)\n" \
    "v_mfma_f32_32x32x16_bf16 v[0:15], v[116:119], v[64:67], v[0:15]\n" \
    "v_cvt_pk_bf16_f32 v74, v52, v53\n" \
    "v_exp_f32 v56, v56\n" \
    "v_exp_f32 v57, v57\n" \
    "v_cvt_pk_bf16_f32 v75, v54, v55\n" \
    "v_exp_f32 v58, v58\n" \
    "s_waitcnt lgkmcnt(0)\n" \
    "v_mfma_f32_32x32x16_bf16 v[0:15], v[120:123], v[68:71], v[0:15]\n" \
    "v_exp_f32 v59, v59\n" \
    "v_cvt_pk_bf16_f32 v76, v56, v57\n" \
    "v_exp_f32 v60, v60\n" \
    "v_exp_f32 v61, v61\n" \
    "v_cvt_pk_bf16_f32 v77, v58, v59\n" \
    "v_exp_f32 v62, v62\n" \
    "v_smfmac_f32_16x16x64_bf16 v[100:103], v[96:99], v[64:71], v126\n" \
    "v_exp_f32 v63, v63\n" \
    "v_cvt_pk_bf16_f32 v78, v60, v61\n" \
    "v_cvt_pk_bf16_f32 v79, v62, v63\n" \
    "v_mfma_f32_32x32x16_bf16 v[48:63], v[108:111], v[88:91], 0\n" \
    "v_exp_f32 v32, v32\n" \
    "ds_read_b128 v[108:111], v124 offset:12288\n" \
    "v_exp_f32 v33, v33\n" \
    "v_exp_f32 v34, v34\n" \
    "v_exp_f32 v35, v35\n" \
    "v_cvt_pk_bf16_f32 v64, v32, v33\n" \
    "v_mfma_f32_32x32x16_bf16 v[48:63], v[112:115], v[92:95], v[48:63]\n" \
    "v_exp_f32 v36, v36\n" \
    "ds_read_b128 v[112:115], v124 offset:13312\n" \
    "v_exp_f32 v37, v37\n" \
    "v_cvt_pk_bf16_f32 v65, v34, v35\n" \
    "v_exp_f32 v38, v38\n" \
    "v_exp_f32 v39, v39\n" \
    "v_mfma_f32_32x32x16_bf16 v[16:31], v[116:119], v[72:75], v[16:31]\n" \
    "v_cvt_pk_bf16_f32 v66, v36, v37\n" \
    "ds_read_b128 v[116:119], v124 offset:14336\n" \
    "v_exp_f32 v40, v40\n" \
    "v_exp_f32 v41, v41\n" \
    "v_cvt_pk_bf16_f32 v67, v38, v39\n" \
    "v_exp_f32 v42, v42\n" \
    "v_mfma_f32_32x32x16_bf16 v[16:31], v[120:123], v[76:79], v[16:31]\n" \
    "v_exp_f32 v43, v43\n" \
    "ds_read_b128 v[120:123], v124 offset:15360\n" \
    "v_cvt_pk_bf16_f32 v68, v40, v41\n" \
    "v_exp_f32 v44, v44\n" \
    "v_exp_f32 v45, v45\n" \
    "v_cvt_pk_bf16_f32 v69, v42, v43\n" \
    "v_exp_f32 v46, v46\n" \
    "v_smfmac_f32_16x16x64_bf16 v[104:107], v[96:99], v[72:79], v126\n" \
    "v_exp_f32 v47, v47\n" \
    "v_cvt_pk_bf16_f32 v70, v44, v45\n" \
    "v_cvt_pk_bf16_f32 v71, v46, v47\n" \
    "s_waitcnt lgkmcnt(2)\n" \
    "v_mfma_f32_32x32x16_bf16 v[32:47], v[108:111], v[80:83], 0\n" \
    "v_exp_f32 v48, v48\n" \
    "v_exp_f32 v49, v49\n" \
    "v_exp_f32 v50, v50\n" \
    "v_exp_f32 v51, v51\n" \
    "v_cvt_pk_bf16_f32 v72, v48, v49\n" \
    "v_mfma_f32_32x32x16_bf16 v[32:47], v[112:115], v[84:87], v[32:47]\n" \
    "v_exp_f32 v52, v52\n" \
    "v_exp_f32 v53, v53\n" \
    "v_cvt_pk_bf16_f32 v73, v50, v51\n" \
    "v_exp_f32 v54, v54\n" \
    "v_exp_f32 v55, v55\n" \
    "s_waitcnt lgkmcnt(1)\n" \
    "v_mfma_f32_32x32x16_bf16 v[0:15], v[116:119], v[64:67], v[0:15]\n" \
    "v_cvt_pk_bf16_f32 v74, v52, v53\n" \
    "v_exp_f32 v56, v56\n" \
    "v_exp_f32 v57, v57\n" \
    "v_cvt_pk_bf16_f32 v75, v54, v55\n" \
    "v_exp_f32 v58, v58\n" \
    "s_waitcnt lgkmcnt(0)\n" \
    "v_mfma_f32_32x32x16_bf16 v[0:15], v[120:123], v[68:71], v[0:15]\n" \
    "v_exp_f32 v59, v59\n" \
    "v_cvt_pk_bf16_f32 v76, v56, v57\n" \
    "v_exp_f32 v60, v60\n" \
    "v_exp_f32 v61, v61\n" \
    "v_cvt_pk_bf16_f32 v77, v58, v59\n" \
    "v_exp_f32 v62, v62\n" \
    "v_smfmac_f32_16x16x64_bf16 v[100:103], v[96:99], v[64:71], v126\n" \
    "v_exp_f32 v63, v63\n" \
    "v_cvt_pk_bf16_f32 v78, v60, v61\n" \
    "v_cvt_pk_bf16_f32 v79, v62, v63\n" \
    "v_mfma_f32_32x32x16_bf16 v[48:63], v[108:111], v[88:91], 0\n" \
    "v_exp_f32 v32, v32\n" \
    "ds_read_b128 v[108:111], v124 offset:16384\n" \
    "v_exp_f32 v33, v33\n" \
    "v_exp_f32 v34, v34\n" \
    "v_exp_f32 v35, v35\n" \
    "v_cvt_pk_bf16_f32 v64, v32, v33\n" \
    "v_mfma_f32_32x32x16_bf16 v[48:63], v[112:115], v[92:95], v[48:63]\n" \
    "v_exp_f32 v36, v36\n" \
    "ds_read_b128 v[112:115], v124 offset:17408\n" \
    "v_exp_f32 v37, v37\n" \
    "v_cvt_pk_bf16_f32 v65, v34, v35\n" \
    "v_exp_f32 v38, v38\n" \
    "v_exp_f32 v39, v39\n" \
    "v_mfma_f32_32x32x16_bf16 v[16:31], v[116:119], v[72:75], v[16:31]\n" \
    "v_cvt_pk_bf16_f32 v66, v36, v37\n" \
    "ds_read_b128 v[116:119], v124 offset:18432\n" \
    "v_exp_f32 v40, v40\n" \
    "v_exp_f32 v41, v41\n" \
    "v_cvt_pk_bf16_f32 v67, v38, v39\n" \
    "v_exp_f32 v42, v42\n" \
    "v_mfma_f32_32x32x16_bf16 v[16:31], v[120:123], v[76:79], v[16:31]\n" \
    "v_exp_f32 v43, v43\n" \
    "ds_read_b128 v[120:123], v124 offset:19456\n" \
    "v_cvt_pk_bf16_f32 v68, v40, v41\n" \
    "v_exp_f32 v44, v44\n" \
    "v_exp_f32 v45, v45\n" \
    "v_cvt_pk_bf16_f32 v69, v42, v43\n" \
    "v_exp_f32 v46, v46\n" \
    "v_smfmac_f32_16x16x64_bf16 v[104:107], v[96:99], v[72:79], v126\n" \
    "v_exp_f32 v47, v47\n" \
    "v_cvt_pk_bf16_f32 v70, v44, v45\n" \
    "v_cvt_pk_bf16_f32 v71, v46, v47\n" \
    "s_waitcnt lgkmcnt(2)\n" \
    "v_mfma_f32_32x32x16_bf16 v[32:47], v[108:111], v[80:83], 0\n" \
    "v_exp_f32 v48, v48\n" \
    "v_exp_f32 v49, v49\n" \
    "v_exp_f32 v50, v50\n" \
    "v_exp_f32 v51, v51\n" \
    "v_cvt_pk_bf16_f32 v72, v48, v49\n" \
    "v_mfma_f32_32x32x16_bf16 v[32:47], v[112:115], v[84:87], v[32:47]\n" \
    "v_exp_f32 v52, v52\n" \
    "v_exp_f32 v53, v53\n" \
    "v_cvt_pk_bf16_f32 v73, v50, v51\n" \
    "v_exp_f32 v54, v54\n" \
    "v_exp_f32 v55, v55\n" \
    "s_waitcnt lgkmcnt(1)\n" \
    "v_mfma_f32_32x32x16_bf16 v[0:15], v[116:119], v[64:67], v[0:15]\n" \
    "v_cvt_pk_bf16_f32 v74, v52, v53\n" \
    "v_exp_f32 v56, v56\n" \
    "v_exp_f32 v57, v57\n" \
    "v_cvt_pk_bf16_f32 v75, v54, v55\n" \
    "v_exp_f32 v58, v58\n" \
    "s_waitcnt lgkmcnt(0)\n" \
    "v_mfma_f32_32x32x16_bf16 v[0:15], v[120:123], v[68:71], v[0:15]\n" \
    "v_exp_f32 v59, v59\n" \
    "v_cvt_pk_bf16_f32 v76, v56, v57\n" \
    "v_exp_f32 v60, v60\n" \
    "v_exp_f32 v61, v61\n" \
    "v_cvt_pk_bf16_f32 v77, v58, v59\n" \
    "v_exp_f32 v62, v62\n" \
    "v_smfmac_f32_16x16x64_bf16 v[100:103], v[96:99], v[64:71], v126\n" \
    "v_exp_f32 v63, v63\n" \
    "v_cvt_pk_bf16_f32 v78, v60, v61\n" \
    "v_cvt_pk_bf16_f32 v79, v62, v63\n" \
    "v_mfma_f32_32x32x16_bf16 v[48:63], v[108:111], v[88:91], 0\n" \
    "v_exp_f32 v32, v32\n" \
    "ds_read_b128 v[108:111], v124 offset:20480\n" \
    "v_exp_f32 v33, v33\n" \
    "v_exp_f32 v34, v34\n" \
    "v_exp_f32 v35, v35\n" \
    "v_cvt_pk_bf16_f32 v64, v32, v33\n" \
    "v_mfma_f32_32x32x16_bf16 v[48:63], v[112:115], v[92:95], v[48:63]\n" \
    "v_exp_f32 v36, v36\n" \
    "ds_read_b128 v[112:115], v124 offset:21504\n" \
    "v_exp_f32 v37, v37\n" \
    "v_cvt_pk_bf16_f32 v65, v34, v35\n" \
    "v_exp_f32 v38, v38\n" \
    "v_exp_f32 v39, v39\n" \
    "v_mfma_f32_32x32x16_bf16 v[16:31], v[116:119], v[72:75], v[16:31]\n" \
    "v_cvt_pk_bf16_f32 v66, v36, v37\n" \
    "ds_read_b128 v[116:119], v124 offset:22528\n" \
    "v_exp_f32 v40, v40\n" \
    "v_exp_f32 v41, v41\n" \
    "v_cvt_pk_bf16_f32 v67, v38, v39\n" \
    "v_exp_f32 v42, v42\n" \
    "v_mfma_f32_32x32x16_bf16 v[16:31], v[120:123], v[76:79], v[16:31]\n" \
    "v_exp_f32 v43, v43\n" \
    "ds_read_b128 v[120:123], v124 offset:23552\n" \
    "v_cvt_pk_bf16_f32 v68, v40, v41\n" \
    "v_exp_f32 v44, v44\n" \
    "v_exp_f32 v45, v45\n" \
    "v_cvt_pk_bf16_f32 v69, v42, v43\n" \
    "v_exp_f32 v46, v46\n" \
    "v_smfmac_f32_16x16x64_bf16 v[104:107], v[96:99], v[72:79], v126\n" \
    "v_exp_f32 v47, v47\n" \
    "v_cvt_pk_bf16_f32 v70, v44, v45\n" \
    "v_cvt_pk_bf16_f32 v71, v46, v47\n" \
    "s_waitcnt lgkmcnt(2)\n" \
    "v_mfma_f32_32x32x16_bf16 v[32:47], v[108:111], v[80:83], 0\n" \
    "v_exp_f32 v48, v48\n" \
    "v_exp_f32 v49, v49\n" \
    "v_exp_f32 v50, v50\n" \
    "v_exp_f32 v51, v51\n" \
    "v_cvt_pk_bf16_f32 v72, v48, v49\n" \
    "v_mfma_f32_32x32x16_bf16 v[32:47], v[112:115], v[84:87], v[32:47]\n" \
    "v_exp_f32 v52, v52\n" \
    "v_exp_f32 v53, v53\n" \
    "v_cvt_pk_bf16_f32 v73, v50, v51\n" \
    "v_exp_f32 v54, v54\n" \
    "v_exp_f32 v55, v55\n" \
    "s_waitcnt lgkmcnt(1)\n" \
    "v_mfma_f32_32x32x16_bf16 v[0:15], v[116:119], v[64:67], v[0:15]\n" \
    "v_cvt_pk_bf16_f32 v74, v52, v53\n" \
    "v_exp_f32 v56, v56\n" \
    "v_exp_f32 v57, v57\n" \
    "v_cvt_pk_bf16_f32 v75, v54, v55\n" \
    "v_exp_f32 v58, v58\n" \
    "s_waitcnt lgkmcnt(0)\n" \
    "v_mfma_f32_32x32x16_bf16 v[0:15], v[120:123], v[68:71], v[0:15]\n" \
    "v_exp_f32 v59, v59\n" \
    "v_cvt_pk_bf16_f32 v76, v56, v57\n" \
    "v_exp_f32 v60, v60\n" \
    "v_exp_f32 v61, v61\n" \
    "v_cvt_pk_bf16_f32 v77, v58, v59\n" \
    "v_exp_f32 v62, v62\n" \
    "v_smfmac_f32_16x16x64_bf16 v[100:103], v[96:99], v[64:71], v126\n" \
    "v_exp_f32 v63, v63\n" \
    "v_cvt_pk_bf16_f32 v78, v60, v61\n" \
    "v_cvt_pk_bf16_f32 v79, v62, v63\n" \
    "v_mfma_f32_32x32x16_bf16 v[48:63], v[108:111], v[88:91], 0\n" \
    "v_exp_f32 v32, v32\n" \
    "ds_read_b128 v[108:111], v125 offset:16384\n" \
    "v_exp_f32 v33, v33\n" \
    "v_exp_f32 v34, v34\n" \
    "v_exp_f32 v35, v35\n" \
    "v_cvt_pk_bf16_f32 v64, v32, v33\n" \
    "v_mfma_f32_32x32x16_bf16 v[48:63], v[112:115], v[92:95], v[48:63]\n" \
    "v_exp_f32 v36, v36\n" \
    "ds_read_b128 v[112:115], v125 offset:17408\n" \
    "v_exp_f32 v37, v37\n" \
    "v_cvt_pk_bf16_f32 v65, v34, v35\n" \
    "v_exp_f32 v38, v38\n" \
    "v_exp_f32 v39, v39\n" \
    "v_mfma_f32_32x32x16_bf16 v[16:31], v[116:119], v[72:75], v[16:31]\n" \
    "v_cvt_pk_bf16_f32 v66, v36, v37\n" \
    "ds_read_b128 v[116:119], v125 offset:18432\n" \
    "v_exp_f32 v40, v40\n" \
    "v_exp_f32 v41, v41\n" \
    "v_cvt_pk_bf16_f32 v67, v38, v39\n" \
    "v_exp_f32 v42, v42\n" \
    "v_mfma_f32_32x32x16_bf16 v[16:31], v[120:123], v[76:79], v[16:31]\n" \
    "v_exp_f32 v43, v43\n" \
    "ds_read_b128 v[120:123], v125 offset:19456\n" \
    "v_cvt_pk_bf16_f32 v68, v40, v41\n" \
    "v_exp_f32 v44, v44\n" \
    "v_exp_f32 v45, v45\n" \
    "v_cvt_pk_bf16_f32 v69, v42, v43\n" \
    "v_exp_f32 v46, v46\n" \
    "v_smfmac_f32_16x16x64_bf16 v[104:107], v[96:99], v[72:79], v126\n" \
    "v_exp_f32 v47, v47\n" \
    "v_cvt_pk_bf16_f32 v70, v44, v45\n" \
    "v_cvt_pk_bf16_f32 v71, v46, v47\n" \
    "s_waitcnt lgkmcnt(2)\n" \
    "v_mfma_f32_32x32x16_bf16 v[32:47], v[108:111], v[80:83], 0\n" \
    "v_exp_f32 v48, v48\n" \
    "v_exp_f32 v49, v49\n" \
    "v_exp_f32 v50, v50\n" \
    "v_exp_f32 v51, v51\n" \
    "v_cvt_pk_bf16_f32 v72, v48, v49\n" \
    "v_mfma_f32_32x32x16_bf16 v[32:47], v[112:115], v[84:87], v[32:47]\n" \
    "v_exp_f32 v52, v52\n" \
    "v_exp_f32 v53, v53\n" \
    "v_cvt_pk_bf16_f32 v73, v50, v51\n" \
    "v_exp_f32 v54, v54\n" \
    "v_exp_f32 v55, v55\n" \
    "s_waitcnt lgkmcnt(1)\n" \
    "v_mfma_f32_32x32x16_bf16 v[0:15], v[116:119], v[64:67], v[0:15]\n" \
    "v_cvt_pk_bf16_f32 v74, v52, v53\n" \
    "v_exp_f32 v56, v56\n" \
    "v_exp_f32 v57, v57\n" \
    "v_cvt_pk_bf16_f32 v75, v54, v55\n" \
    "v_exp_f32 v58, v58\n" \
    "s_waitcnt lgkmcnt(0)\n" \
    "v_mfma_f32_32x32x16_bf16 v[0:15], v[120:123], v[68:71], v[0:15]\n" \
    "v_exp_f32 v59, v59\n" \
    "v_cvt_pk_bf16_f32 v76, v56, v57\n" \
    "v_exp_f32 v60, v60\n" \
    "v_exp_f32 v61, v61\n" \
    "v_cvt_pk_bf16_f32 v77, v58, v59\n" \
    "v_exp_f32 v62, v62\n" \
    "v_smfmac_f32_16x16x64_bf16 v[100:103], v[96:99], v[64:71], v126\n" \
    "v_exp_f32 v63, v63\n" \
    "v_cvt_pk_bf16_f32 v78, v60, v61\n" \
    "v_cvt_pk_bf16_f32 v79, v62, v63\n" \
    "v_mfma_f32_32x32x16_bf16 v[48:63], v[108:111], v[88:91], 0\n" \
    "v_exp_f32 v32, v32\n" \
    "ds_read_b128 v[108:111], v125 offset:20480\n" \
    "v_exp_f32 v33, v33\n" \
    "v_exp_f32 v34, v34\n" \
    "v_exp_f32 v35, v35\n" \
    "v_cvt_pk_bf16_f32 v64, v32, v33\n" \
    "v_mfma_f32_32x32x16_bf16 v[48:63], v[112:115], v[92:95], v[48:63]\n" \
    "v_exp_f32 v36, v36\n" \
    "ds_read_b128 v[112:115], v125 offset:21504\n" \
    "v_exp_f32 v37, v37\n" \
    "v_cvt_pk_bf16_f32 v65, v34, v35\n" \
    "v_exp_f32 v38, v38\n" \
    "v_exp_f32 v39, v39\n" \
    "v_mfma_f32_32x32x16_bf16 v[16:31], v[116:119], v[72:75], v[16:31]\n" \
    "v_cvt_pk_bf16_f32 v66, v36, v37\n" \
    "ds_read_b128 v[116:119], v125 offset:22528\n" \
    "v_exp_f32 v40, v40\n" \
    "v_exp_f32 v41, v41\n" \
    "v_cvt_pk_bf16_f32 v67, v38, v39\n" \
    "v_exp_f32 v42, v42\n" \
    "v_mfma_f32_32x32x16_bf16 v[16:31], v[120:123], v[76:79], v[16:31]\n" \
    "v_exp_f32 v43, v43\n" \
    "ds_read_b128 v[120:123], v125 offset:23552\n" \
    "v_cvt_pk_bf16_f32 v68, v40, v41\n" \
    "v_exp_f32 v44, v44\n" \
    "v_exp_f32 v45, v45\n" \
    "v_cvt_pk_bf16_f32 v69, v42, v43\n" \
    "v_exp_f32 v46, v46\n" \
    "v_smfmac_f32_16x16x64_bf16 v[104:107], v[96:99], v[72:79], v126\n" \
    "v_exp_f32 v47, v47\n" \
    "v_cvt_pk_bf16_f32 v70, v44, v45\n" \
    "v_cvt_pk_bf16_f32 v71, v46, v47\n" \
    "s_waitcnt lgkmcnt(2)\n" \
    "v_mfma_f32_32x32x16_bf16 v[32:47], v[108:111], v[80:83], 0\n" \
    "v_exp_f32 v48, v48\n" \
    "v_exp_f32 v49, v49\n" \
    "v_exp_f32 v50, v50\n" \
    "v_exp_f32 v51, v51\n" \
    "v_cvt_pk_bf16_f32 v72, v48, v49\n" \
    "v_mfma_f32_32x32x16_bf16 v[32:47], v[112:115], v[84:87], v[32:47]\n" \
    "v_exp_f32 v52, v52\n" \
    "v_exp_f32 v53, v53\n" \
    "v_cvt_pk_bf16_f32 v73, v50, v51\n" \
    "v_exp_f32 v54, v54\n" \
    "v_exp_f32 v55, v55\n" \
    "s_waitcnt lgkmcnt(1)\n" \
    "v_mfma_f32_32x32x16_bf16 v[0:15], v[116:119], v[64:67], v[0:15]\n" \
    "v_cvt_pk_bf16_f32 v74, v52, v53\n" \
    "v_exp_f32 v56, v56\n" \
    "v_exp_f32 v57, v57\n" \
    "v_cvt_pk_bf16_f32 v75, v54, v55\n" \
    "v_exp_f32 v58, v58\n" \
    "s_waitcnt lgkmcnt(0)\n" \
    "v_mfma_f32_32x32x16_bf16 v[0:15], v[120:123], v[68:71], v[0:15]\n" \
    "v_exp_f32 v59, v59\n" \
    "v_cvt_pk_bf16_f32 v76, v56, v57\n" \
    "v_exp_f32 v60, v60\n" \
    "v_exp_f32 v61, v61\n" \
    "v_cvt_pk_bf16_f32 v77, v58, v59\n" \
    "v_exp_f32 v62, v62\n" \
    "v_smfmac_f32_16x16x64_bf16 v[100:103], v[96:99], v[64:71], v126\n" \
    "v_exp_f32 v63, v63\n" \
    "v_cvt_pk_bf16_f32 v78, v60, v61\n" \
    "v_cvt_pk_bf16_f32 v79, v62, v63\n" \
    "s_barrier\n" \
    "v_mfma_f32_32x32x16_bf16 v[48:63], v[108:111], v[88:91], 0\n" \
    "v_exp_f32 v32, v32\n" \
    "ds_read_b128 v[108:111], v125 offset:40960\n" \
    "v_exp_f32 v33, v33\n" \
    "v_exp_f32 v34, v34\n" \
    "v_exp_f32 v35, v35\n" \
    "v_cvt_pk_bf16_f32 v64, v32, v33\n" \
    "v_mfma_f32_32x32x16_bf16 v[48:63], v[112:115], v[92:95], v[48:63]\n" \
    "v_exp_f32 v36, v36\n" \
    "ds_read_b128 v[112:115], v125 offset:41984\n" \
    "v_exp_f32 v37, v37\n" \
    "v_cvt_pk_bf16_f32 v65, v34, v35\n" \
    "v_exp_f32 v38, v38\n" \
    "v_exp_f32 v39, v39\n" \
    "v_mfma_f32_32x32x16_bf16 v[16:31], v[116:119], v[72:75], v[16:31]\n" \
    "v_cvt_pk_bf16_f32 v66, v36, v37\n" \
    "ds_read_b128 v[116:119], v125 offset:43008\n" \
    "v_exp_f32 v40, v40\n" \
    "v_exp_f32 v41, v41\n" \
    "v_cvt_pk_bf16_f32 v67, v38, v39\n" \
    "v_exp_f32 v42, v42\n" \
    "v_mfma_f32_32x32x16_bf16 v[16:31], v[120:123], v[76:79], v[16:31]\n" \
    "v_exp_f32 v43, v43\n" \
    "ds_read_b128 v[120:123], v125 offset:44032\n" \
    "v_cvt_pk_bf16_f32 v68, v40, v41\n" \
    "v_exp_f32 v44, v44\n" \
    "v_exp_f32 v45, v45\n" \
    "v_cvt_pk_bf16_f32 v69, v42, v43\n" \
    "v_exp_f32 v46, v46\n" \
    "v_smfmac_f32_16x16x64_bf16 v[104:107], v[96:99], v[72:79], v126\n" \
    "v_exp_f32 v47, v47\n" \
    "v_cvt_pk_bf16_f32 v70, v44, v45\n" \
    "v_cvt_pk_bf16_f32 v71, v46, v47\n" \
    "s_sub_u32 %[cnt], %[cnt], 1\n" \
    "s_cmp_lg_u32 %[cnt], 0\n" \
    "s_cbranch_scc1 1b\n" \
    "s_waitcnt lgkmcnt(0)\n" \
    "s_nop 15\n"
#define ASM_MIX1 \
    "s_mov_b32 %[cnt], 16\n" \
    "1:\n" \
    "s_waitcnt lgkmcnt(0)\n" \
    "s_barrier\n" \
    "v_mfma_f32_32x32x16_bf16 v[0:15], v[80:83], v[88:91], v[0:15]\n" \
    "ds_read_b64_tr_b16 v[64:65], v96 offset:0\n" \
    "ds_read_b64_tr_b16 v[66:67], v96 offset:1024\n" \
    "v_mfma_f32_32x32x16_bf16 v[16:31], v[80:83], v[92:95], v[16:31]\n" \
    "ds_read_b128 v[72:75], v98 offset:0\n" \
    "ds_read_b128 v[76:79], v98 offset:512\n" \
    "v_mfma_f32_32x32x16_bf16 v[32:47], v[84:87], v[88:91], v[32:47]\n" \
    "ds_read_b64_tr_b16 v[68:69], v97 offset:0\n" \
    "ds_read_b64_tr_b16 v[70:71], v97 offset:1024\n" \
    "v_mfma_f32_32x32x16_bf16 v[48:63], v[84:87], v[92:95], v[48:63]\n" \
    "s_waitcnt lgkmcnt(0)\n" \
    "v_mfma_f32_32x32x16_bf16 v[0:15], v[64:67], v[72:75], v[0:15]\n" \
    "ds_read_b64_tr_b16 v[80:81], v96 offset:4096\n" \
    "ds_read_b64_tr_b16 v[82:83], v96 offset:5120\n" \
    "v_mfma_f32_32x32x16_bf16 v[16:31], v[64:67], v[76:79], v[16:31]\n" \
    "ds_read_b128 v[88:91], v98 offset:4096\n" \
    "ds_read_b128 v[92:95], v98 offset:4608\n" \
    "v_mfma_f32_32x32x16_bf16 v[32:47], v[68:71], v[72:75], v[32:47]\n" \
    "ds_read_b64_tr_b16 v[84:85], v97 offset:4096\n" \
    "ds_read_b64_tr_b16 v[86:87], v97 offset:5120\n" \
    "v_mfma_f32_32x32x16_bf16 v[48:63], v[68:71], v[76:79], v[48:63]\n" \
    "s_waitcnt lgkmcnt(0)\n" \
    "s_barrier\n" \
    "v_mfma_f32_32x32x16_bf16 v[0:15], v[80:83], v[88:91], v[0:15]\n" \
    "ds_read_b64_tr_b16 v[64:65], v96 offset:16384\n" \
    "ds_read_b64_tr_b16 v[66:67], v96 offset:17408\n" \
    "v_mfma_f32_32x32x16_bf16 v[16:31], v[80:83], v[92:95], v[16:31]\n" \
    "ds_read_b128 v[72:75], v98 offset:16384\n" \
    "ds_read_b128 v[76:79], v98 offset:16896\n" \
    "v_mfma_f32_32x32x16_bf16 v[32:47], v[84:87], v[88:91], v[32:47]\n" \
    "ds_read_b64_tr_b16 v[68:69], v97 offset:16384\n" \
    "ds_read_b64_tr_b16 v[70:71], v97 offset:17408\n" \
    "v_mfma_f32_32x32x16_bf16 v[48:63], v[84:87], v[92:95], v[48:63]\n" \
    "s_waitcnt lgkmcnt(0)\n" \
    "v_mfma_f32_32x32x16_bf16 v[0:15], v[64:67], v[72:75], v[0:15]\n" \
    "ds_read_b64_tr_b16 v[80:81], v96 offset:20480\n" \
    "ds_read_b64_tr_b16 v[82:83], v96 offset:21504\n" \
    "v_mfma_f32_32x32x16_bf16 v[16:31], v[64:67], v[76:79], v[16:31]\n" \
    "ds_read_b128 v[88:91], v98 offset:20480\n" \
    "ds_read_b128 v[92:95], v98 offset:20992\n" \
    "v_mfma_f32_32x32x16_bf16 v[32:47], v[68:71], v[72:75], v[32:47]\n" \
    "ds_read_b64_tr_b16 v[84:85], v97 offset:20480\n" \
    "ds_read_b64_tr_b16 v[86:87], v97 offset:21504\n" \
    "v_mfma_f32_32x32x16_bf16 v[48:63], v[68:71], v[76:79], v[48:63]\n" \
    "s_waitcnt lgkmcnt(0)\n" \
    "s_barrier\n" \
    "v_mfma_f32_32x32x16_bf16 v[0:15], v[80:83], v[88:91], v[0:15]\n" \
    "ds_read_b64_tr_b16 v[64:65], v96 offset:32768\n" \
    "ds_read_b64_tr_b16 v[66:67], v96 offset:33792\n" \
    "v_mfma_f32_32x32x16_bf16 v[16:31], v[80:83], v[92:95], v[16:31]\n" \
    "ds_read_b128 v[72:75], v98 offset:32768\n" \
    "ds_read_b128 v[76:79], v98 offset:33280\n" \
    "v_mfma_f32_32x32x16_bf16 v[32:47], v[84:87], v[88:91], v[32:47]\n" \
    "ds_read_b64_tr_b16 v[68:69], v97 offset:32768\n" \
    "ds_read_b64_tr_b16 v[70:71], v97 offset:33792\n" \
    "v_mfma_f32_32x32x16_bf16 v[48:63], v[84:87], v[92:95], v[48:63]\n" \
    "s_waitcnt lgkmcnt(0)\n" \
    "v_mfma_f32_32x32x16_bf16 v[0:15], v[64:67], v[72:75], v[0:15]\n" \
    "ds_read_b64_tr_b16 v[80:81], v96 offset:36864\n" \
    "ds_read_b64_tr_b16 v[82:83], v96 offset:37888\n" \
    "v_mfma_f32_32x32x16_bf16 v[16:31], v[64:67], v[76:79], v[16:31]\n" \
    "ds_read_b128 v[88:91], v98 offset:36864\n" \
    "ds_read_b128 v[92:95], v98 offset:37376\n" \
    "v_mfma_f32_32x32x16_bf16 v[32:47], v[68:71], v[72:75], v[32:47]\n" \
    "ds_read_b64_tr_b16 v[84:85], v97 offset:36864\n" \
    "ds_read_b64_tr_b16 v[86:87], v97 offset:37888\n" \
    "v_mfma_f32_32x32x16_bf16 v[48:63], v[68:71], v[76:79], v[48:63]\n" \
    "s_waitcnt lgkmcnt(0)\n" \
    "s_barrier\n" \
    "v_mfma_f32_32x32x16_bf16 v[0:15], v[80:83], v[88:91], v[0:15]\n" \
    "ds_read_b64_tr_b16 v[64:65], v96 offset:49152\n" \
    "ds_read_b64_tr_b16 v[66:67], v96 offset:50176\n" \
    "v_mfma_f32_32x32x16_bf16 v[16:31], v[80:83], v[92:95], v[16:31]\n" \
    "ds_read_b128 v[72:75], v98 offset:49152\n" \
    "ds_read_b128 v[76:79], v98 offset:49664\n" \
    "v_mfma_f32_32x32x16_bf16 v[32:47], v[84:87], v[88:91], v[32:47]\n" \
    "ds_read_b64_tr_b16 v[68:69], v97 offset:49152\n" \
    "ds_read_b64_tr_b16 v[70:71], v97 offset:50176\n" \
    "v_mfma_f32_32x32x16_bf16 v[48:63], v[84:87], v[92:95], v[48:63]\n" \
    "s_waitcnt lgkmcnt(0)\n" \
    "v_mfma_f32_32x32x16_bf16 v[0:15], v[64:67], v[72:75], v[0:15]\n" \
    "ds_read_b64_tr_b16 v[80:81], v96 offset:53248\n" \
    "ds_read_b64_tr_b16 v[82:83], v96 offset:54272\n" \
    "v_mfma_f32_32x32x16_bf16 v[16:31], v[64:67], v[76:79], v[16:31]\n" \
    "ds_read_b128 v[88:91], v98 offset:53248\n" \
    "ds_read_b128 v[92:95], v98 offset:53760\n" \
    "v_mfma_f32_32x32x16_bf16 v[32:47], v[68:71], v[72:75], v[32:47]\n" \
    "ds_read_b64_tr_b16 v[84:85], v97 offset:53248\n" \
    "ds_read_b64_tr_b16 v[86:87], v97 offset:54272\n" \
    "v_mfma_f32_32x32x16_bf16 v[48:63], v[68:71], v[76:79], v[48:63]\n" \
    "s_sub_u32 %[cnt], %[cnt], 1\n" \
    "s_cmp_lg_u32 %[cnt], 0\n" \
    "s_cbranch_scc1 1b\n" \
    "s_waitcnt lgkmcnt(0)\n" \
    "v_mfma_f32_32x32x16_bf16 v[0:15], v[80:83], v[88:91], v[0:15]\n" \
    "v_mfma_f32_32x32x16_bf16 v[16:31], v[80:83], v[92:95], v[16:31]\n" \
    "v_mfma_f32_32x32x16_bf16 v[32:47], v[84:87], v[88:91], v[32:47]\n" \
    "v_mfma_f32_32x32x16_bf16 v[48:63], v[84:87], v[92:95], v[48:63]\n" \
    "s_nop 15\n"
#define ASM_MIX2 \
    "s_mov_b32 %[cnt], 14\n" \
    "1:\n" \
    "s_waitcnt lgkmcnt(0)\n" \
    "s_barrier\n" \
    "v_mfma_f32_32x32x16_bf16 v[0:15], v[88:91], v[80:83], v[0:15]\n" \
    "ds_read_b64_tr_b16 v[64:65], v96 offset:0\n" \
    "ds_read_b64_tr_b16 v[66:67], v96 offset:2048\n" \
    "v_mfma_f32_32x32x16_bf16 v[32:47], v[92:95], v[80:83], v[32:47]\n" \
    "ds_read_b128 v[72:75], v98 offset:0\n" \
    "ds_read_b128 v[76:79], v98 offset:512\n" \
    "v_mfma_f32_32x32x16_bf16 v[16:31], v[88:91], v[84:87], v[16:31]\n" \
    "ds_read_b64_tr_b16 v[68:69], v97 offset:0\n" \
    "ds_read_b64_tr_b16 v[70:71], v97 offset:2048\n" \
    "v_mfma_f32_32x32x16_bf16 v[48:63], v[92:95], v[84:87], v[48:63]\n" \
    "s_waitcnt lgkmcnt(0)\n" \
    "v_mfma_f32_32x32x16_bf16 v[0:15], v[72:75], v[64:67], v[0:15]\n" \
    "ds_read_b64_tr_b16 v[80:81], v96 offset:4096\n" \
    "ds_read_b64_tr_b16 v[82:83], v96 offset:6144\n" \
    "v_mfma_f32_32x32x16_bf16 v[32:47], v[76:79], v[64:67], v[32:47]\n" \
    "ds_read_b128 v[88:91], v98 offset:4096\n" \
    "ds_read_b128 v[92:95], v98 offset:4608\n" \
    "v_mfma_f32_32x32x16_bf16 v[16:31], v[72:75], v[68:71], v[16:31]\n" \
    "ds_read_b64_tr_b16 v[84:85], v97 offset:4096\n" \
    "ds_read_b64_tr_b16 v[86:87], v97 offset:6144\n" \
    "v_mfma_f32_32x32x16_bf16 v[48:63], v[76:79], v[68:71], v[48:63]\n" \
    "s_waitcnt lgkmcnt(0)\n" \
    "s_barrier\n" \
    "v_mfma_f32_32x32x16_bf16 v[0:15], v[88:91], v[80:83], v[0:15]\n" \
    "ds_read_b64_tr_b16 v[64:65], v96 offset:16384\n" \
    "ds_read_b64_tr_b16 v[66:67], v96 offset:18432\n" \
    "v_mfma_f32_32x32x16_bf16 v[32:47], v[92:95], v[80:83], v[32:47]\n" \
    "ds_read_b128 v[72:75], v98 offset:16384\n" \
    "ds_read_b128 v[76:79], v98 offset:16896\n" \
    "v_mfma_f32_32x32x16_bf16 v[16:31], v[88:91], v[84:87], v[16:31]\n" \
    "ds_read_b64_tr_b16 v[68:69], v97 offset:16384\n" \
    "ds_read_b64_tr_b16 v[70:71], v97 offset:18432\n" \
    "v_mfma_f32_32x32x16_bf16 v[48:63], v[92:95], v[84:87], v[48:63]\n" \
    "s_waitcnt lgkmcnt(0)\n" \
    "v_mfma_f32_32x32x16_bf16 v[0:15], v[72:75], v[64:67], v[0:15]\n" \
    "ds_read_b64_tr_b16 v[80:81], v96 offset:20480\n" \
    "ds_read_b64_tr_b16 v[82:83], v96 offset:22528\n" \
    "v_mfma_f32_32x32x16_bf16 v[32:47], v[76:79], v[64:67], v[32:47]\n" \
    "ds_read_b128 v[88:91], v98 offset:20480\n" \
    "ds_read_b128 v[92:95], v98 offset:20992\n" \
    "v_mfma_f32_32x32x16_bf16 v[16:31], v[72:75], v[68:71], v[16:31]\n" \
    "ds_read_b64_tr_b16 v[84:85], v97 offset:20480\n" \
    "ds_read_b64_tr_b16 v[86:87], v97 offset:22528\n" \
    "v_mfma_f32_32x32x16_bf16 v[48:63], v[76:79], v[68:71], v[48:63]\n" \
    "s_waitcnt lgkmcnt(0)\n" \
    "s_barrier\n" \
    "v_mfma_f32_32x32x16_bf16 v[0:15], v[88:91], v[80:83], v[0:15]\n" \
    "ds_read_b64_tr_b16 v[64:65], v96 offset:32768\n" \
    "ds_read_b64_tr_b16 v[66:67], v96 offset:34816\n" \
    "v_mfma_f32_32x32x16_bf16 v[32:47], v[92:95], v[80:83], v[32:47]\n" \
    "ds_read_b128 v[72:75], v98 offset:32768\n" \
    "ds_read_b128 v[76:79], v98 offset:33280\n" \
    "v_mfma_f32_32x32x16_bf16 v[16:31], v[88:91], v[84:87], v[16:31]\n" \
    "ds_read_b64_tr_b16 v[68:69], v97 offset:32768\n" \
    "ds_read_b64_tr_b16 v[70:71], v97 offset:34816\n" \
    "v_mfma_f32_32x32x16_bf16 v[48:63], v[92:95], v[84:87], v[48:63]\n" \
    "s_waitcnt lgkmcnt(0)\n" \
    "v_mfma_f32_32x32x16_bf16 v[0:15], v[72:75], v[64:67], v[0:15]\n" \
    "ds_read_b64_tr_b16 v[80:81], v96 offset:36864\n" \
    "ds_read_b64_tr_b16 v[82:83], v96 offset:38912\n" \
    "v_mfma_f32_32x32x16_bf16 v[32:47], v[76:79], v[64:67], v[32:47]\n" \
    "ds_read_b128 v[88:91], v98 offset:36864\n" \
    "ds_read_b128 v[92:95], v98 offset:37376\n" \
    "v_mfma_f32_32x32x16_bf16 v[16:31], v[72:75], v[68:71], v[16:31]\n" \
    "ds_read_b64_tr_b16 v[84:85], v97 offset:36864\n" \
    "ds_read_b64_tr_b16 v[86:87], v97 offset:38912\n" \
    "v_mfma_f32_32x32x16_bf16 v[48:63], v[76:79], v[68:71], v[48:63]\n" \
    "s_waitcnt lgkmcnt(0)\n" \
    "s_barrier\n" \
    "v_mfma_f32_32x32x16_bf16 v[0:15], v[88:91], v[80:83], v[0:15]\n" \
    "ds_read_b64_tr_b16 v[64:65], v96 offset:49152\n" \
    "ds_read_b64_tr_b16 v[66:67], v96 offset:51200\n" \
    "v_mfma_f32_32x32x16_bf16 v[32:47], v[92:95], v[80:83], v[32:47]\n" \
    "ds_read_b128 v[72:75], v98 offset:49152\n" \
    "ds_read_b128 v[76:79], v98 offset:49664\n" \
    "v_mfma_f32_32x32x16_bf16 v[16:31], v[88:91], v[84:87], v[16:31]\n" \
    "ds_read_b64_tr_b16 v[68:69], v97 offset:49152\n" \
    "ds_read_b64_tr_b16 v[70:71], v97 offset:51200\n" \
    "v_mfma_f32_32x32x16_bf16 v[48:63], v[92:95], v[84:87], v[48:63]\n" \
    "s_waitcnt lgkmcnt(0)\n" \
    "v_mfma_f32_32x32x16_bf16 v[0:15], v[72:75], v[64:67], v[0:15]\n" \
    "ds_read_b64_tr_b16 v[80:81], v96 offset:53248\n" \
    "ds_read_b64_tr_b16 v[82:83], v96 offset:55296\n" \
    "v_mfma_f32_32x32x16_bf16 v[32:47], v[76:79], v[64:67], v[32:47]\n" \
    "ds_read_b128 v[88:91], v98 offset:53248\n" \
    "ds_read_b128 v[92:95], v98 offset:53760\n" \
    "v_mfma_f32_32x32x16_bf16 v[16:31], v[72:75], v[68:71], v[16:31]\n" \
    "ds_read_b64_tr_b16 v[84:85], v97 offset:53248\n" \
    "ds_read_b64_tr_b16 v[86:87], v97 offset:55296\n" \
    "v_mfma_f32_32x32x16_bf16 v[48:63], v[76:79], v[68:71], v[48:63]\n" \
    "s_sub_u32 %[cnt], %[cnt], 1\n" \
    "s_cmp_lg_u32 %[cnt], 0\n" \
    "s_cbranch_scc1 1b\n" \
    "s_waitcnt lgkmcnt(0)\n" \
    "v_mfma_f32_32x32x16_bf16 v[0:15], v[88:91], v[80:83], v[0:15]\n" \
    "v_mfma_f32_32x32x16_bf16 v[32:47], v[92:95], v[80:83], v[32:47]\n" \
    "v_mfma_f32_32x32x16_bf16 v[16:31], v[88:91], v[84:87], v[16:31]\n" \
    "v_mfma_f32_32x32x16_bf16 v[48:63], v[92:95], v[84:87], v[48:63]\n" \
    "s_nop 15\n"
#define ASM_QPROJ \
    "s_mov_b32 %[cnt], 5\n" \
    "1:\n" \
    "s_waitcnt lgkmcnt(0)\n" \
    "s_barrier\n" \
    "v_mfma_f32_32x32x16_bf16 v[0:15], v[56:59], v[60:63], v[0:15]\n" \
    "ds_read_b64_tr_b16 v[32:33], v80 offset:0\n" \
    "ds_read_b64_tr_b16 v[34:35], v80 offset:512\n" \
    "ds_read_b128 v[36:39], v81 offset:0\n" \
    "v_mfma_f32_32x32x16_bf16 v[16:31], v[56:59], v[64:67], v[16:31]\n" \
    "ds_read_b128 v[40:43], v81 offset:4096\n" \
    "ds_read_b64_tr_b16 v[44:45], v80 offset:2048\n" \
    "ds_read_b64_tr_b16 v[46:47], v80 offset:2560\n" \
    "v_mfma_f32_32x32x16_bf16 v[0:15], v[68:71], v[72:75], v[0:15]\n" \
    "ds_read_b128 v[48:51], v82 offset:0\n" \
    "ds_read_b128 v[52:55], v82 offset:4096\n" \
    "v_mfma_f32_32x32x16_bf16 v[16:31], v[68:71], v[76:79], v[16:31]\n" \
    "s_waitcnt lgkmcnt(0)\n" \
    "v_mfma_f32_32x32x16_bf16 v[0:15], v[32:35], v[36:39], v[0:15]\n" \
    "ds_read_b64_tr_b16 v[56:57], v80 offset:4096\n" \
    "ds_read_b64_tr_b16 v[58:59], v80 offset:4608\n" \
    "ds_read_b128 v[60:63], v83 offset:0\n" \
    "v_mfma_f32_32x32x16_bf16 v[16:31], v[32:35], v[40:43], v[16:31]\n" \
    "ds_read_b128 v[64:67], v83 offset:4096\n" \
    "ds_read_b64_tr_b16 v[68:69], v80 offset:6144\n" \
    "ds_read_b64_tr_b16 v[70:71], v80 offset:6656\n" \
    "v_mfma_f32_32x32x16_bf16 v[0:15], v[44:47], v[48:51], v[0:15]\n" \
    "ds_read_b128 v[72:75], v84 offset:0\n" \
    "ds_read_b128 v[76:79], v84 offset:4096\n" \
    "v_mfma_f32_32x32x16_bf16 v[16:31], v[44:47], v[52:55], v[16:31]\n" \
    "s_waitcnt lgkmcnt(0)\n" \
    "s_barrier\n" \
    "v_mfma_f32_32x32x16_bf16 v[0:15], v[56:59], v[60:63], v[0:15]\n" \
    "ds_read_b64_tr_b16 v[32:33], v80 offset:24576\n" \
    "ds_read_b64_tr_b16 v[34:35], v80 offset:25088\n" \
    "ds_read_b128 v[36:39], v81 offset:24576\n" \
    "v_mfma_f32_32x32x16_bf16 v[16:31], v[56:59], v[64:67], v[16:31]\n" \
    "ds_read_b128 v[40:43], v81 offset:28672\n" \
    "ds_read_b64_tr_b16 v[44:45], v80 offset:26624\n" \
    "ds_read_b64_tr_b16 v[46:47], v80 offset:27136\n" \
    "v_mfma_f32_32x32x16_bf16 v[0:15], v[68:71], v[72:75], v[0:15]\n" \
    "ds_read_b128 v[48:51], v82 offset:24576\n" \
    "ds_read_b128 v[52:55], v82 offset:28672\n" \
    "v_mfma_f32_32x32x16_bf16 v[16:31], v[68:71], v[76:79], v[16:31]\n" \
    "s_waitcnt lgkmcnt(0)\n" \
    "v_mfma_f32_32x32x16_bf16 v[0:15], v[32:35], v[36:39], v[0:15]\n" \
    "ds_read_b64_tr_b16 v[56:57], v80 offset:28672\n" \
    "ds_read_b64_tr_b16 v[58:59], v80 offset:29184\n" \
    "ds_read_b128 v[60:63], v83 offset:24576\n" \
    "v_mfma_f32_32x32x16_bf16 v[16:31], v[32:35], v[40:43], v[16:31]\n" \
    "ds_read_b128 v[64:67], v83 offset:28672\n" \
    "ds_read_b64_tr_b16 v[68:69], v80 offset:30720\n" \
    "ds_read_b64_tr_b16 v[70:71], v80 offset:31232\n" \
    "v_mfma_f32_32x32x16_bf16 v[0:15], v[44:47], v[48:51], v[0:15]\n" \
    "ds_read_b128 v[72:75], v84 offset:24576\n" \
    "ds_read_b128 v[76:79], v84 offset:28672\n" \
    "v_mfma_f32_32x32x16_bf16 v[16:31], v[44:47], v[52:55], v[16:31]\n" \
    "s_waitcnt lgkmcnt(0)\n" \
    "s_barrier\n" \
    "v_mfma_f32_32x32x16_bf16 v[0:15], v[56:59], v[60:63], v[0:15]\n" \
    "ds_read_b64_tr_b16 v[32:33], v80 offset:49152\n" \
    "ds_read_b64_tr_b16 v[34:35], v80 offset:49664\n" \
    "ds_read_b128 v[36:39], v81 offset:49152\n" \
    "v_mfma_f32_32x32x16_bf16 v[16:31], v[56:59], v[64:67], v[16:31]\n" \
    "ds_read_b128 v[40:43], v81 offset:53248\n" \
    "ds_read_b64_tr_b16 v[44:45], v80 offset:51200\n" \
    "ds_read_b64_tr_b16 v[46:47], v80 offset:51712\n" \
    "v_mfma_f32_32x32x16_bf16 v[0:15], v[68:71], v[72:75], v[0:15]\n" \
    "ds_read_b128 v[48:51], v82 offset:49152\n" \
    "ds_read_b128 v[52:55], v82 offset:53248\n" \
    "v_mfma_f32_32x32x16_bf16 v[16:31], v[68:71], v[76:79], v[16:31]\n" \
    "s_waitcnt lgkmcnt(0)\n" \
    "v_mfma_f32_32x32x16_bf16 v[0:15], v[32:35], v[36:39], v[0:15]\n" \
    "ds_read_b64_tr_b16 v[56:57], v80 offset:53248\n" \
    "ds_read_b64_tr_b16 v[58:59], v80 offset:53760\n" \
    "ds_read_b128 v[60:63], v83 offset:49152\n" \
    "v_mfma_f32_32x32x16_bf16 v[16:31], v[32:35], v[40:43], v[16:31]\n" \
    "ds_read_b128 v[64:67], v83 offset:53248\n" \
    "ds_read_b64_tr_b16 v[68:69], v80 offset:55296\n" \
    "ds_read_b64_tr_b16 v[70:71], v80 offset:55808\n" \
    "v_mfma_f32_32x32x16_bf16 v[0:15], v[44:47], v[48:51], v[0:15]\n" \
    "ds_read_b128 v[72:75], v84 offset:49152\n" \
    "ds_read_b128 v[76:79], v84 offset:53248\n" \
    "v_mfma_f32_32x32x16_bf16 v[16:31], v[44:47], v[52:55], v[16:31]\n" \
    "s_sub_u32 %[cnt], %[cnt], 1\n" \
    "s_cmp_lg_u32 %[cnt], 0\n" \
    "s_cbranch_scc1 1b\n" \
    "s_waitcnt lgkmcnt(0)\n" \
    "s_barrier\n" \
    "v_mfma_f32_32x32x16_bf16 v[0:15], v[56:59], v[60:63], v[0:15]\n" \
    "ds_read_b64_tr_b16 v[32:33], v80 offset:0\n" \
    "ds_read_b64_tr_b16 v[34:35], v80 offset:512\n" \
    "ds_read_b128 v[36:39], v81 offset:0\n" \
    "v_mfma_f32_32x32x16_bf16 v[16:31], v[56:59], v[64:67], v[16:31]\n" \
    "ds_read_b128 v[40:43], v81 offset:4096\n" \
    "ds_read_b64_tr_b16 v[44:45], v80 offset:2048\n" \
    "ds_read_b64_tr_b16 v[46:47], v80 offset:2560\n" \
    "v_mfma_f32_32x32x16_bf16 v[0:15], v[68:71], v[72:75], v[0:15]\n" \
    "ds_read_b128 v[48:51], v82 offset:0\n" \
    "ds_read_b128 v[52:55], v82 offset:4096\n" \
    "v_mfma_f32_32x32x16_bf16 v[16:31], v[68:71], v[76:79], v[16:31]\n" \
    "s_waitcnt lgkmcnt(0)\n" \
    "v_mfma_f32_32x32x16_bf16 v[0:15], v[32:35], v[36:39], v[0:15]\n" \
    "ds_read_b64_tr_b16 v[56:57], v80 offset:4096\n" \
    "ds_read_b64_tr_b16 v[58:59], v80 offset:4608\n" \
    "ds_read_b128 v[60:63], v83 offset:0\n" \
    "v_mfma_f32_32x32x16_bf16 v[16:31], v[32:35], v[40:43], v[16:31]\n" \
    "ds_read_b128 v[64:67], v83 offset:4096\n" \
    "ds_read_b64_tr_b16 v[68:69], v80 offset:6144\n" \
    "ds_read_b64_tr_b16 v[70:71], v80 offset:6656\n" \
    "v_mfma_f32_32x32x16_bf16 v[0:15], v[44:47], v[48:51], v[0:15]\n" \
    "ds_read_b128 v[72:75], v84 offset:0\n" \
    "ds_read_b128 v[76:79], v84 offset:4096\n" \
    "v_mfma_f32_32x32x16_bf16 v[16:31], v[44:47], v[52:55], v[16:31]\n" \
    "s_waitcnt lgkmcnt(0)\n" \
    "v_mfma_f32_32x32x16_bf16 v[0:15], v[56:59], v[60:63], v[0:15]\n" \
    "v_mfma_f32_32x32x16_bf16 v[16:31], v[56:59], v[64:67], v[16:31]\n" \
    "v_mfma_f32_32x32x16_bf16 v[0:15], v[68:71], v[72:75], v[0:15]\n" \
    "v_mfma_f32_32x32x16_bf16 v[16:31], v[68:71], v[76:79], v[16:31]\n" \
    "s_nop 15\n"
#define ASM_OPROJ_A \
    "s_mov_b32 %[cnt], 4\n" \
    "1:\n" \
    "s_waitcnt lgkmcnt(0)\n" \
    "s_barrier\n" \
    "v_mfma_f32_32x32x16_bf16 v[0:15], v[56:59], v[60:63], v[0:15]\n" \
    "ds_read_b64_tr_b16 v[32:33], v80 offset:0\n" \
    "ds_read_b64_tr_b16 v[34:35], v80 offset:2048\n" \
    "ds_read_b128 v[36:39], v81 offset:0\n" \
    "v_mfma_f32_32x32x16_bf16 v[16:31], v[56:59], v[64:67], v[16:31]\n" \
    "ds_read_b128 v[40:43], v81 offset:512\n" \
    "ds_read_b64_tr_b16 v[44:45], v80 offset:4096\n" \
    "ds_read_b64_tr_b16 v[46:47], v80 offset:6144\n" \
    "v_mfma_f32_32x32x16_bf16 v[0:15], v[68:71], v[72:75], v[0:15]\n" \
    "ds_read_b128 v[48:51], v81 offset:2048\n" \
    "ds_read_b128 v[52:55], v81 offset:2560\n" \
    "v_mfma_f32_32x32x16_bf16 v[16:31], v[68:71], v[76:79], v[16:31]\n" \
    "s_waitcnt lgkmcnt(0)\n" \
    "v_mfma_f32_32x32x16_bf16 v[0:15], v[32:35], v[36:39], v[0:15]\n" \
    "ds_read_b64_tr_b16 v[56:57], v80 offset:8192\n" \
    "ds_read_b64_tr_b16 v[58:59], v80 offset:10240\n" \
    "ds_read_b128 v[60:63], v81 offset:4096\n" \
    "v_mfma_f32_32x32x16_bf16 v[16:31], v[32:35], v[40:43], v[16:31]\n" \
    "ds_read_b128 v[64:67], v81 offset:4608\n" \
    "ds_read_b64_tr_b16 v[68:69], v80 offset:12288\n" \
    "ds_read_b64_tr_b16 v[70:71], v80 offset:14336\n" \
    "v_mfma_f32_32x32x16_bf16 v[0:15], v[44:47], v[48:51], v[0:15]\n" \
    "ds_read_b128 v[72:75], v81 offset:6144\n" \
    "ds_read_b128 v[76:79], v81 offset:6656\n" \
    "v_mfma_f32_32x32x16_bf16 v[16:31], v[44:47], v[52:55], v[16:31]\n" \
    "s_waitcnt lgkmcnt(0)\n" \
    "s_barrier\n" \
    "v_mfma_f32_32x32x16_bf16 v[0:15], v[56:59], v[60:63], v[0:15]\n" \
    "ds_read_b64_tr_b16 v[32:33], v80 offset:24576\n" \
    "ds_read_b64_tr_b16 v[34:35], v80 offset:26624\n" \
    "ds_read_b128 v[36:39], v81 offset:24576\n" \
    "v_mfma_f32_32x32x16_bf16 v[16:31], v[56:59], v[64:67], v[16:31]\n" \
    "ds_read_b128 v[40:43], v81 offset:25088\n" \
    "ds_read_b64_tr_b16 v[44:45], v80 offset:28672\n" \
    "ds_read_b64_tr_b16 v[46:47], v80 offset:30720\n" \
    "v_mfma_f32_32x32x16_bf16 v[0:15], v[68:71], v[72:75], v[0:15]\n" \
    "ds_read_b128 v[48:51], v81 offset:26624\n" \
    "ds_read_b128 v[52:55], v81 offset:27136\n" \
    "v_mfma_f32_32x32x16_bf16 v[16:31], v[68:71], v[76:79], v[16:31]\n" \
    "s_waitcnt lgkmcnt(0)\n" \
    "v_mfma_f32_32x32x16_bf16 v[0:15], v[32:35], v[36:39], v[0:15]\n" \
    "ds_read_b64_tr_b16 v[56:57], v80 offset:32768\n" \
    "ds_read_b64_tr_b16 v[58:59], v80 offset:34816\n" \
    "ds_read_b128 v[60:63], v81 offset:28672\n" \
    "v_mfma_f32_32x32x16_bf16 v[16:31], v[32:35], v[40:43], v[16:31]\n" \
    "ds_read_b128 v[64:67], v81 offset:29184\n" \
    "ds_read_b64_tr_b16 v[68:69], v80 offset:36864\n" \
    "ds_read_b64_tr_b16 v[70:71], v80 offset:38912\n" \
    "v_mfma_f32_32x32x16_bf16 v[0:15], v[44:47], v[48:51], v[0:15]\n" \
    "ds_read_b128 v[72:75], v81 offset:30720\n" \
    "ds_read_b128 v[76:79], v81 offset:31232\n" \
    "v_mfma_f32_32x32x16_bf16 v[16:31], v[44:47], v[52:55], v[16:31]\n" \
    "s_waitcnt lgkmcnt(0)\n" \
    "s_barrier\n" \
    "v_mfma_f32_32x32x16_bf16 v[0:15], v[56:59], v[60:63], v[0:15]\n" \
    "ds_read_b64_tr_b16 v[32:33], v80 offset:49152\n" \
    "ds_read_b64_tr_b16 v[34:35], v80 offset:51200\n" \
    "ds_read_b128 v[36:39], v81 offset:49152\n" \
    "v_mfma_f32_32x32x16_bf16 v[16:31], v[56:59], v[64:67], v[16:31]\n" \
    "ds_read_b128 v[40:43], v81 offset:49664\n" \
    "ds_read_b64_tr_b16 v[44:45], v80 offset:53248\n" \
    "ds_read_b64_tr_b16 v[46:47], v80 offset:55296\n" \
    "v_mfma_f32_32x32x16_bf16 v[0:15], v[68:71], v[72:75], v[0:15]\n" \
    "ds_read_b128 v[48:51], v81 offset:51200\n" \
    "ds_read_b128 v[52:55], v81 offset:51712\n" \
    "v_mfma_f32_32x32x16_bf16 v[16:31], v[68:71], v[76:79], v[16:31]\n" \
    "s_waitcnt lgkmcnt(0)\n" \
    "v_mfma_f32_32x32x16_bf16 v[0:15], v[32:35], v[36:39], v[0:15]\n" \
    "ds_read_b64_tr_b16 v[56:57], v80 offset:57344\n" \
    "ds_read_b64_tr_b16 v[58:59], v80 offset:59392\n" \
    "ds_read_b128 v[60:63], v81 offset:53248\n" \
    "v_mfma_f32_32x32x16_bf16 v[16:31], v[32:35], v[40:43], v[16:31]\n" \
    "ds_read_b128 v[64:67], v81 offset:53760\n" \
    "ds_read_b64_tr_b16 v[68:69], v80 offset:61440\n" \
    "ds_read_b64_tr_b16 v[70:71], v80 offset:63488\n" \
    "v_mfma_f32_32x32x16_bf16 v[0:15], v[44:47], v[48:51], v[0:15]\n" \
    "ds_read_b128 v[72:75], v81 offset:55296\n" \
    "ds_read_b128 v[76:79], v81 offset:55808\n" \
    "v_mfma_f32_32x32x16_bf16 v[16:31], v[44:47], v[52:55], v[16:31]\n" \
    "s_sub_u32 %[cnt], %[cnt], 1\n" \
    "s_cmp_lg_u32 %[cnt], 0\n" \
    "s_cbranch_scc1 1b\n" \
    "s_waitcnt lgkmcnt(0)\n" \
    "s_barrier\n" \
    "v_mfma_f32_32x32x16_bf16 v[0:15], v[56:59], v[60:63], v[0:15]\n" \
    "ds_read_b64_tr_b16 v[32:33], v80 offset:0\n" \
    "ds_read_b64_tr_b16 v[34:35], v80 offset:2048\n" \
    "ds_read_b128 v[36:39], v81 offset:0\n" \
    "v_mfma_f32_32x32x16_bf16 v[16:31], v[56:59], v[64:67], v[16:31]\n" \
    "ds_read_b128 v[40:43], v81 offset:512\n" \
    "ds_read_b64_tr_b16 v[44:45], v80 offset:4096\n" \
    "ds_read_b64_tr_b16 v[46:47], v80 offset:6144\n" \
    "v_mfma_f32_32x32x16_bf16 v[0:15], v[68:71], v[72:75], v[0:15]\n" \
    "ds_read_b128 v[48:51], v81 offset:2048\n" \
    "ds_read_b128 v[52:55], v81 offset:2560\n" \
    "v_mfma_f32_32x32x16_bf16 v[16:31], v[68:71], v[76:79], v[16:31]\n" \
    "s_waitcnt lgkmcnt(0)\n" \
    "v_mfma_f32_32x32x16_bf16 v[0:15], v[32:35], v[36:39], v[0:15]\n" \
    "ds_read_b64_tr_b16 v[56:57], v80 offset:8192\n" \
    "ds_read_b64_tr_b16 v[58:59], v80 offset:10240\n" \
    "ds_read_b128 v[60:63], v81 offset:4096\n" \
    "v_mfma_f32_32x32x16_bf16 v[16:31], v[32:35], v[40:43], v[16:31]\n" \
    "ds_read_b128 v[64:67], v81 offset:4608\n" \
    "ds_read_b64_tr_b16 v[68:69], v80 offset:12288\n" \
    "ds_read_b64_tr_b16 v[70:71], v80 offset:14336\n" \
    "v_mfma_f32_32x32x16_bf16 v[0:15], v[44:47], v[48:51], v[0:15]\n" \
    "ds_read_b128 v[72:75], v81 offset:6144\n" \
    "ds_read_b128 v[76:79], v81 offset:6656\n" \
    "v_mfma_f32_32x32x16_bf16 v[16:31], v[44:47], v[52:55], v[16:31]\n" \
    "s_waitcnt lgkmcnt(0)\n" \
    "s_barrier\n" \
    "v_mfma_f32_32x32x16_bf16 v[0:15], v[56:59], v[60:63], v[0:15]\n" \
    "ds_read_b64_tr_b16 v[32:33], v80 offset:24576\n" \
    "ds_read_b64_tr_b16 v[34:35], v80 offset:26624\n" \
    "ds_read_b128 v[36:39], v81 offset:24576\n" \
    "v_mfma_f32_32x32x16_bf16 v[16:31], v[56:59], v[64:67], v[16:31]\n" \
    "ds_read_b128 v[40:43], v81 offset:25088\n" \
    "ds_read_b64_tr_b16 v[44:45], v80 offset:28672\n" \
    "ds_read_b64_tr_b16 v[46:47], v80 offset:30720\n" \
    "v_mfma_f32_32x32x16_bf16 v[0:15], v[68:71], v[72:75], v[0:15]\n" \
    "ds_read_b128 v[48:51], v81 offset:26624\n" \
    "ds_read_b128 v[52:55], v81 offset:27136\n" \
    "v_mfma_f32_32x32x16_bf16 v[16:31], v[68:71], v[76:79], v[16:31]\n" \
    "s_waitcnt lgkmcnt(0)\n" \
    "v_mfma_f32_32x32x16_bf16 v[0:15], v[32:35], v[36:39], v[0:15]\n" \
    "ds_read_b64_tr_b16 v[56:57], v80 offset:32768\n" \
    "ds_read_b64_tr_b16 v[58:59], v80 offset:34816\n" \
    "ds_read_b128 v[60:63], v81 offset:28672\n" \
    "v_mfma_f32_32x32x16_bf16 v[16:31], v[32:35], v[40:43], v[16:31]\n" \
    "ds_read_b128 v[64:67], v81 offset:29184\n" \
    "ds_read_b64_tr_b16 v[68:69], v80 offset:36864\n" \
    "ds_read_b64_tr_b16 v[70:71], v80 offset:38912\n" \
    "v_mfma_f32_32x32x16_bf16 v[0:15], v[44:47], v[48:51], v[0:15]\n" \
    "ds_read_b128 v[72:75], v81 offset:30720\n" \
    "ds_read_b128 v[76:79], v81 offset:31232\n" \
    "v_mfma_f32_32x32x16_bf16 v[16:31], v[44:47], v[52:55], v[16:31]\n" \
    "s_waitcnt lgkmcnt(0)\n"
#define ASM_OPROJ_B \
    "v_mfma_f32_32x32x16_bf16 v[0:15], v[56:59], v[60:63], v[0:15]\n" \
    "ds_read_b64_tr_b16 v[32:33], v80 offset:49152\n" \
    "ds_read_b64_tr_b16 v[34:35], v80 offset:51200\n" \
    "ds_read_b128 v[36:39], v81 offset:49152\n" \
    "v_mfma_f32_32x32x16_bf16 v[16:31], v[56:59], v[64:67], v[16:31]\n" \
    "ds_read_b128 v[40:43], v81 offset:49664\n" \
    "ds_read_b64_tr_b16 v[44:45], v80 offset:53248\n" \
    "ds_read_b64_tr_b16 v[46:47], v80 offset:55296\n" \
    "v_mfma_f32_32x32x16_bf16 v[0:15], v[68:71], v[72:75], v[0:15]\n" \
    "ds_read_b128 v[48:51], v81 offset:51200\n" \
    "ds_read_b128 v[52:55], v81 offset:51712\n" \
    "v_mfma_f32_32x32x16_bf16 v[16:31], v[68:71], v[76:79], v[16:31]\n" \
    "s_waitcnt lgkmcnt(0)\n" \
    "v_mfma_f32_32x32x16_bf16 v[0:15], v[32:35], v[36:39], v[0:15]\n" \
    "ds_read_b64_tr_b16 v[56:57], v80 offset:57344\n" \
    "ds_read_b64_tr_b16 v[58:59], v80 offset:59392\n" \
    "ds_read_b128 v[60:63], v81 offset:53248\n" \
    "v_mfma_f32_32x32x16_bf16 v[16:31], v[32:35], v[40:43], v[16:31]\n" \
    "ds_read_b128 v[64:67], v81 offset:53760\n" \
    "ds_read_b64_tr_b16 v[68:69], v80 offset:61440\n" \
    "ds_read_b64_tr_b16 v[70:71], v80 offset:63488\n" \
    "v_mfma_f32_32x32x16_bf16 v[0:15], v[44:47], v[48:51], v[0:15]\n" \
    "ds_read_b128 v[72:75], v81 offset:55296\n" \
    "ds_read_b128 v[76:79], v81 offset:55808\n" \
    "v_mfma_f32_32x32x16_bf16 v[16:31], v[44:47], v[52:55], v[16:31]\n" \
    "s_waitcnt lgkmcnt(0)\n" \
    "s_barrier\n" \
    "v_mfma_f32_32x32x16_bf16 v[0:15], v[56:59], v[60:63], v[0:15]\n" \
    "ds_read_b64_tr_b16 v[32:33], v80 offset:0\n" \
    "ds_read_b64_tr_b16 v[34:35], v80 offset:2048\n" \
    "ds_read_b128 v[36:39], v81 offset:0\n" \
    "v_mfma_f32_32x32x16_bf16 v[16:31], v[56:59], v[64:67], v[16:31]\n" \
    "ds_read_b128 v[40:43], v81 offset:512\n" \
    "ds_read_b64_tr_b16 v[44:45], v80 offset:4096\n" \
    "ds_read_b64_tr_b16 v[46:47], v80 offset:6144\n" \
    "v_mfma_f32_32x32x16_bf16 v[0:15], v[68:71], v[72:75], v[0:15]\n" \
    "ds_read_b128 v[48:51], v81 offset:2048\n" \
    "ds_read_b128 v[52:55], v81 offset:2560\n" \
    "v_mfma_f32_32x32x16_bf16 v[16:31], v[68:71], v[76:79], v[16:31]\n" \
    "s_waitcnt lgkmcnt(0)\n" \
    "v_mfma_f32_32x32x16_bf16 v[0:15], v[32:35], v[36:39], v[0:15]\n" \
    "ds_read_b64_tr_b16 v[56:57], v80 offset:8192\n" \
    "ds_read_b64_tr_b16 v[58:59], v80 offset:10240\n" \
    "ds_read_b128 v[60:63], v81 offset:4096\n" \
    "v_mfma_f32_32x32x16_bf16 v[16:31], v[32:35], v[40:43], v[16:31]\n" \
    "ds_read_b128 v[64:67], v81 offset:4608\n" \
    "ds_read_b64_tr_b16 v[68:69], v80 offset:12288\n" \
    "ds_read_b64_tr_b16 v[70:71], v80 offset:14336\n" \
    "v_mfma_f32_32x32x16_bf16 v[0:15], v[44:47], v[48:51], v[0:15]\n" \
    "ds_read_b128 v[72:75], v81 offset:6144\n" \
    "ds_read_b128 v[76:79], v81 offset:6656\n" \
    "v_mfma_f32_32x32x16_bf16 v[16:31], v[44:47], v[52:55], v[16:31]\n" \
    "s_waitcnt lgkmcnt(0)\n" \
    "v_mfma_f32_32x32x16_bf16 v[0:15], v[56:59], v[60:63], v[0:15]\n" \
    "v_mfma_f32_32x32x16_bf16 v[16:31], v[56:59], v[64:67], v[16:31]\n" \
    "v_mfma_f32_32x32x16_bf16 v[0:15], v[68:71], v[72:75], v[0:15]\n" \
    "v_mfma_f32_32x32x16_bf16 v[16:31], v[68:71], v[76:79], v[16:31]\n" \
    "s_nop 15\n"

struct Args {
    const char *x, *wq, *wk, *wv, *wo, *w1, *b1, *w2, *b2;
    char *ws, *out;
};

__global__ __launch_bounds__(256) void k_pre(const char* __restrict__ x, const char* __restrict__ gw, char* __restrict__ ws) {
    __shared__ __attribute__((aligned(1024))) char lds[20480];
    typedef __attribute__((address_space(3))) char* lds_ptr;
    const int t4 = threadIdx.x, lane = t4 & 63, w4 = __builtin_amdgcn_readfirstlane(t4 >> 6), gi = blockIdx.x;
    if (w4 == 0) *(unsigned*)(ws + gi * 256 + lane * 4) = 0u;
    const char* xr = x + (size_t)(gi * 8 + w4 * 2) * 2048 + lane * 16;
    const u32x4 a00 = *(const u32x4*)xr, a01 = *(const u32x4*)(xr + 1024), a10 = *(const u32x4*)(xr + 2048), a11 = *(const u32x4*)(xr + 3072);
    const u32x4 g0 = *(const u32x4*)(gw + lane * 16), g1 = *(const u32x4*)(gw + 1024 + lane * 16);
    u32x2 xt[8];
#pragma unroll
    for (int r = 0; r < 8; ++r) xt[r] = *(const u32x2*)(x + (size_t)(gi * 8 + r) * 2048 + t4 * 8);
    float v0[16], v1[16], gv[16];
#pragma unroll
    for (int i = 0; i < 4; ++i) {
        v0[2 * i] = bflo(a00[i]); v0[2 * i + 1] = bfhi(a00[i]); v0[8 + 2 * i] = bflo(a01[i]); v0[9 + 2 * i] = bfhi(a01[i]);
        v1[2 * i] = bflo(a10[i]); v1[2 * i + 1] = bfhi(a10[i]); v1[8 + 2 * i] = bflo(a11[i]); v1[9 + 2 * i] = bfhi(a11[i]);
        gv[2 * i] = bflo(g0[i]); gv[2 * i + 1] = bfhi(g0[i]); gv[8 + 2 * i] = bflo(g1[i]); gv[9 + 2 * i] = bfhi(g1[i]);
    }
    float s0 = 0, s1 = 0, d0 = 0, d1 = 0;
#pragma unroll
    for (int i = 0; i < 16; ++i) { s0 += v0[i]; s1 += v1[i]; d0 = fmaf(v0[i], gv[i], d0); d1 = fmaf(v1[i], gv[i], d1); }
    s0 = wave_sum(s0) * (1.f / DM); s1 = wave_sum(s1) * (1.f / DM);
    float q0 = 0, q1 = 0;
#pragma unroll
    for (int i = 0; i < 16; ++i) { const float e0 = v0[i] - s0, e1 = v1[i] - s1; q0 = fmaf(e0, e0, q0); q1 = fmaf(e1, e1, q1); }
    q0 = wave_sum(q0) * (1.f / DM); q1 = wave_sum(q1) * (1.f / DM); d0 = wave_sum(d0); d1 = wave_sum(d1);
    float* st = (float*)lds;
    if (lane == 0) {
        st[w4 * 4] = s0; st[w4 * 4 + 1] = 1.f / sqrtf(q0 + 1e-5f); st[w4 * 4 + 2] = s1; st[w4 * 4 + 3] = 1.f / sqrtf(q1 + 1e-5f);
        float* gp = (float*)(ws + WS_GATE) + gi * 8 + w4 * 2;
        gp[0] = 1.f / (1.f + __expf(-d0)); gp[1] = 1.f / (1.f + __expf(-d1));
    }
    LGKM_BAR();
    float hv[8][4];
#pragma unroll
    for (int r = 0; r < 8; ++r) {
        const float m = st[2 * r], rs = st[2 * r + 1];
        hv[r][0] = (bflo(xt[r][0]) - m) * rs; hv[r][1] = (bfhi(xt[r][0]) - m) * rs;
        hv[r][2] = (bflo(xt[r][1]) - m) * rs; hv[r][3] = (bfhi(xt[r][1]) - m) * rs;
    }
    const rsrc_t rw = __builtin_amdgcn_make_buffer_rsrc(ws, 0, WS_END, 0x00020000);
    lds_ptr tp = (lds_ptr)lds + 4096 + w4 * 4096;
#pragma unroll
    for (int c = 0; c < 4; ++c)
        *(__attribute__((address_space(3))) u32x4*)(tp + lane * 64 + c * 16) =
            (u32x4){cvtpk(hv[0][c], hv[1][c]), cvtpk(hv[2][c], hv[3][c]), cvtpk(hv[4][c], hv[5][c]), cvtpk(hv[6][c], hv[7][c])};
    asm volatile("s_waitcnt lgkmcnt(0)" ::: "memory");
#pragma unroll
    for (int c = 0; c < 4; ++c)
        __builtin_amdgcn_raw_buffer_store_b128(*(__attribute__((address_space(3))) u32x4*)(tp + c * 1024 + lane * 16), rw,
                                               WS_HT + gi * 16384 + w4 * 4096 + c * 1024 + lane * 16, 0, SC1);
}

__device__ __forceinline__ void attn_store(rsrc_t rw, const f32x16& o, float inv, int head, int s, int h) {
    const unsigned off = WS_OP + ((head * 4 + h) * SEQ + s) * 16;
    st16(rw, off, pack8(o, 0, inv));
    st16(rw, off + 2 * SEQ * 16, pack8(o, 1, inv));
}

__device__ __forceinline__ void attn_online(rsrc_t rw, bf16x8 q0, bf16x8 q1, int lane, f32x16& o, float& inv) {
    float m = -__builtin_inff(), l = 0.f;
    o = f32x16{};
    for (int t = 0; t < 64; ++t) {
        const unsigned kp = WS_KV + ((t + 63) & 63) * 4096, vp = WS_KV + t * 4096 + 2048;
        const bf16x8 k0 = BC8(__builtin_amdgcn_raw_buffer_load_b128(rw, lane * 16, kp, SC1));
        const bf16x8 k1 = BC8(__builtin_amdgcn_raw_buffer_load_b128(rw, lane * 16 + 1024, kp, SC1));
        const bf16x8 v0 = BC8(__builtin_amdgcn_raw_buffer_load_b128(rw, lane * 16, vp, SC1));
        const bf16x8 v1 = BC8(__builtin_amdgcn_raw_buffer_load_b128(rw, lane * 16 + 1024, vp, SC1));
        f32x16 s = MFMA32(k0, q0, f32x16{});
        s = MFMA32(k1, q1, s);
        float tm = s[0];
#pragma unroll
        for (int r = 1; r < 16; ++r) tm = fmaxf(tm, s[r]);
        tm = fmaxf(tm, __shfl_xor(tm, 32));
        const float mn = fmaxf(m, tm), al = __builtin_amdgcn_exp2f(m - mn);
        m = mn;
#pragma unroll
        for (int r = 0; r < 16; ++r) { s[r] = __builtin_amdgcn_exp2f(s[r] - mn); o[r] *= al; }
        const u32x4 p0 = pack8(s, 0, 1.f), p1 = pack8(s, 1, 1.f);
        float ts = 0.f;
#pragma unroll
        for (int r = 0; r < 4; ++r) ts += bflo(p0[r]) + bfhi(p0[r]) + bflo(p1[r]) + bfhi(p1[r]);
        l = l * al + ts;
        o = MFMA32(v0, BC8(p0), o);
        o = MFMA32(v1, BC8(p1), o);
    }
    l += __shfl_xor(l, 32);
    inv = 1.f / l;
}

__device__ __forceinline__ void side_a_loader(const Args& A, unsigned lds0, int lane) {
    char* ws = A.ws;
    const int xcd = blockIdx.x & 7, r = blockIdx.x >> 3;
    const unsigned lin = lane * 16;
    {
        const int rb = (xcd >> 1) * 4 + (r & 3), hp = (xcd & 1) * 8 + (r >> 2), rot = ((r & 3) + (r >> 2)) & 3;
        const unsigned xe = (lane >> 3) * 2048 + (((lane & 7) ^ (lane >> 4)) * 16);
        const unsigned we = (lane >> 3) * 2048 + (((((lane & 7) >> 2) ^ ((lane >> 4) & 1)) << 2) | (lane & 3)) * 16;
        unsigned xv[16], wv[8];
#pragma unroll
        for (int i = 0; i < 16; ++i) xv[i] = off4((xe ^ ((i & 1) * 64)) + i * 8 * 2048, i);
#pragma unroll
        for (int i = 0; i < 8; ++i) wv[i] = off4(we + i * 8 * 2048, i);
        const char* xb = A.x + (size_t)rb * 128 * 2048;
        const char* wb = A.wq + hp * 128;
        auto stage = [&](int j, int slot) {
            const int st = (j & ~3) | ((j + rot) & 3);
            const unsigned dst = lds0 + slot * 24576;
#pragma unroll
            for (int i = 0; i < 2; ++i) dma4(wv + 4 * i, wb + (size_t)st * 64 * 2048, dst + 16384 + i * 4096);
#pragma unroll
            for (int i = 0; i < 4; ++i) dma4(xv + 4 * i, xb + st * 128, dst + i * 4096);
        };
        const char* kv = ws + WS_KV;
        const unsigned l4[4] = {lin + 3072, lin + 3072, lin + 3072, lin + 3072};
        auto part = [&](int c, int u0, int n, unsigned dst, bool pace) {
            for (int i = 0; i < n; ++i) {
                dma4c(l4, kv + c * 32768 + (u0 + i) * 4096, lds0 + dst + i * 4096);
                if (pace) __builtin_amdgcn_s_sleep(4);
            }
        };
        const unsigned foff = ((CT_KV + (r & 7)) * 32 + (lane & 31)) * 4;
        unsigned pv;
        auto poll = [&]() { asm volatile("global_load_dword %0, %1, %2 sc1" : "=v"(pv) : "v"(foff), "s"(ws) : "memory"); };
        if (r >= 28) for (int i = 0; i < KV_HEAD; ++i) __builtin_amdgcn_s_sleep(16);
        stage(0, 0); stage(1, 1);
        int slot = 2;
        for (int j = 0; j < 13; ++j) { WAITV_BAR(24); stage(j + 2, slot); slot = slot == 2 ? 0 : slot + 1; }
        WAITV_BAR(24);
        poll();
        stage(15, 0);
        asm volatile("s_waitcnt vmcnt(24)\n\ts_barrier" : "+v"(pv)::"memory");
        if (__all(pv != 0u)) {
            part(0, 0, 2, LDS_X, false); part(0, 2, 6, 24576, false);
            WAITV_BAR(32);
        } else {
            WAITV_BAR(0);
            for (unsigned spins = 0; spins < (1u << 20); ++spins) {
                poll();
                asm volatile("s_waitcnt vmcnt(0)" : "+v"(pv)::"memory");
                if (__all(pv != 0u)) break;
                __builtin_amdgcn_s_sleep(POLL_SLEEP);
            }
            part(0, 0, 2, LDS_X, false); part(0, 2, 6, 24576, false);
        }
        asm volatile("s_mov_b32 m0, %2\n\ts_nop 0\n\tglobal_load_lds_dwordx4 %0, %1 sc1\n\tglobal_load_lds_dwordx4 %0, %1 offset:1024 sc1"
                     ::"v"(lin), "s"(kv + 63 * 4096), "s"(lds0 + LDS_K0) : "memory");
        *(__attribute__((address_space(3))) unsigned*)(uintptr_t)(lds0 + LDS_KN) = wave_umax(pv);
        asm volatile("s_waitcnt vmcnt(0) lgkmcnt(0)\n\ts_barrier" ::: "memory");
        const char* w2p = A.w2 + (blockIdx.x >> 7) * (7u << 20) + (blockIdx.x & 127) * 8192;
        int t = 0;
        auto ahead = [&](int k) {
            for (int i = (TOUCH_PLAN >> (4 * (6 - k))) & 15; i > 0; --i, ++t) touch(t < 7 ? w2p + t * (1u << 20) : A.wo + blockIdx.x * 8192, lane, lds0 + LDS_PF);
        };
        part(1, 0, 6, 0, true); part(1, 6, 2, 49152, true); ahead(0);
        for (int c = 0; c < 6; c += 2) {
            WAITV_BAR(0); part(c + 2, 0, 2, LDS_X, true); part(c + 2, 2, 6, 24576, true); ahead(c + 1);
            WAITV_BAR(0); part(c + 3, 0, 6, 0, true); part(c + 3, 6, 2, 49152, true); ahead(c + 2);
        }
        WAITV_BAR(0); BARRIER();
    }
    {
        const int rb = (xcd >> 1) * 4 + (r & 3), hp = (xcd & 1) * 8 + (r >> 2);
        const int sb = rb * 2 + (hp & 1), dt = hp >> 1;
        const unsigned woff = (lane >> 4) * 2048 + dt * 256 + (((((lane & 15) >> 2) ^ (lane >> 4)) << 2) | (lane & 3)) * 16;
        unsigned wv[16], ov[8];
#pragma unroll
        for (int i = 0; i < 16; ++i) wv[i] = off4(woff + i * 4 * 2048, i);
#pragma unroll
        for (int i = 0; i < 8; ++i) ov[i] = off4(lin + i * (SEQ * 16), i);
        const char* ob = ws + WS_OP + sb * 1024;
        unsigned foff = ((CT_O + rb * 4 + (dt & 3)) * 32 + (lane & 15)) * 4;
        unsigned todo = 1u << ((hp + 1) & 15), pv;
        auto poll = [&]() { asm volatile("global_load_dword %0, %1, %2 sc1" : "=v"(pv) : "v"(foff), "s"(ws) : "memory"); };
        auto ready = [&]() { return (unsigned)__ballot(pv != 0u) & todo; };
        auto stage = [&](unsigned avail, int slot) {
            const int st = __builtin_ctz(avail);
            todo = 1u << ((st + 1) & 15);
#pragma unroll
            for (int i = 0; i < 2; ++i) dma4c(ov + 4 * i, ob + (size_t)st * 8 * (SEQ * 16), lds0 + slot * 24576 + 16384 + i * 4096);
#pragma unroll
            for (int i = 0; i < 4; ++i) dma4(wv + 4 * i, A.wo + (size_t)st * 64 * 2048, lds0 + slot * 24576 + i * 4096);
        };
        auto wait_ready = [&](unsigned avail) {
            for (unsigned spins = 0; !avail && spins < (1u << 20); ++spins) {
                poll();
                asm volatile("s_waitcnt vmcnt(0)" : "+v"(pv)::"memory");
                avail = ready();
                if (!avail) __builtin_amdgcn_s_sleep(POLL_SLEEP);
            }
            return avail ? avail : todo;
        };
        poll();
        asm volatile("s_waitcnt vmcnt(0)" : "+v"(pv)::"memory");
        int n0 = 0;
        if (ready()) { stage(ready(), 0); n0 = 1; }
        if (n0 && ready()) { stage(ready(), 1); n0 = 2; }
        BARRIER();
        if (lane < 4) __hip_atomic_store((gu32*)ws + (CT_O + rb * 4 + lane) * 32 + hp, 1u, __ATOMIC_RELAXED, __HIP_MEMORY_SCOPE_AGENT);
        if (n0 < 1) stage(wait_ready(0), 0);
        if (n0 < 2) stage(wait_ready(ready()), 1);
        int slot = 2;
        for (int j = 0; j < 14; ++j) {
            asm volatile("s_waitcnt vmcnt(24)\n\ts_barrier" : "+v"(pv)::"memory");

            const unsigned avail = wait_ready(ready());
            if (j == 13) foff = (CT_Y + rb * 8 + dt) * 128;
            poll();
            stage(avail, slot);
            slot = slot == 2 ? 0 : slot + 1;
        }
        asm volatile("s_waitcnt vmcnt(24)" : "+v"(pv)::"memory");
        if (__builtin_amdgcn_readfirstlane(pv) < 2u) wait_count(ws, CT_Y + rb * 8 + dt, 2);
        BARRIER();
        WAITV_BAR(0);
    }
}

__device__ __forceinline__ void side_a(const Args& A, char* lds, int wid) {
    char* ws = A.ws;
    const unsigned lds0 = (unsigned)(uintptr_t)lds;
    const int xcd = blockIdx.x & 7, r = blockIdx.x >> 3;
    const rsrc_t rw = __builtin_amdgcn_make_buffer_rsrc(ws, 0, WS_END, 0x00020000);
    unsigned cnt;
    bf16x8 qA0, qA1, qB0, qB1;
    float qn;
    {
        const int lane = fresh_lane(), h = lane >> 5, r32 = lane & 31, q4 = (lane & 15) >> 2;
        const unsigned tb = lds0 + 16384 + (8 * h + q4) * 128 + (((wid & 1) ^ (q4 >> 1)) * 64) + ((lane >> 4) & 1) * 32 + (lane & 3) * 8;
        const unsigned xb = lds0 + ((wid >> 1) * 64 + r32) * 128 + ((h ^ ((r32 >> 1) & 7)) * 16);
        f32x16 a0 = {}, a1 = {}, y0 = {};
        f32x8 y1 = {};
        asm volatile("s_nop 1\n" ASM_QPROJ
                     : [cnt] "=&s"(cnt), "+{v[0:15]}"(a0), "+{v[16:31]}"(a1), "+{v[56:71]}"(y0), "+{v[72:79]}"(y1)
                     : "{v80}"(tb), "{v81}"(xb), "{v82}"(xb ^ 32), "{v83}"(xb ^ 64), "{v84}"(xb ^ 96)
                     : "memory", "scc", CLOB16(32, 33, 34, 35, 36, 37, 38, 39, 40, 41, 42, 43, 44, 45, 46, 47), CLOB8(48, 49, 50, 51, 52, 53, 54, 55));
        const u32x4 u0 = pack8(a0, 0, QSCALE), u1 = pack8(a0, 1, QSCALE), w0 = pack8(a1, 0, QSCALE), w1 = pack8(a1, 1, QSCALE);
        float nA = sumsq8(u0) + sumsq8(u1), nB = sumsq8(w0) + sumsq8(w1);
        const auto sw = __builtin_amdgcn_permlane32_swap(__float_as_uint(nA), __float_as_uint(nB), false, false);
        qn = __uint_as_float(sw[0]) + __uint_as_float(sw[1]);
        qA0 = BC8(u0); qA1 = BC8(u1); qB0 = BC8(w0); qB1 = BC8(w1);
    }
    const int head = ((xcd & 1) * 8 + (r >> 2)) * 2 + (wid & 1), s0 = ((xcd >> 1) * 4 + (r & 3)) * 128 + (wid >> 1) * 64;
    BARRIER();
    {
        const int lane = fresh_lane();
        lds_cptr p = (lds_cptr)lds + lane * 16;
        const float kn = *(const __attribute__((address_space(3))) float*)((lds_cptr)lds + LDS_KN);
        if (!__all(qn * kn <= SCORE_BOUND * SCORE_BOUND)) {
            f32x16 o; float inv;
            attn_online(rw, qA0, qA1, lane, o, inv); attn_store(rw, o, inv, head, s0 + (lane & 31), lane >> 5);
            attn_online(rw, qB0, qB1, lane, o, inv); attn_store(rw, o, inv, head, s0 + 32 + (lane & 31), lane >> 5);
            for (int c = 0; c < 8; ++c) BARRIER();
        } else {
            const unsigned one = (lane & 2) ? 0u : 0x3F803F80u, hi = (lane >> 2) & 1;
            const u32x4 ones = {hi ? 0u : one, hi ? 0u : one, hi ? one : 0u, hi ? one : 0u};
            const unsigned pick = (lane & 1) ? 0xEEEEEEEEu : 0x44444444u;
            bf16x8 k0 = lds128(p + LDS_K0), k1 = lds128(p + LDS_K0 + 1024);
            f32x16 oA = {}, oB = {};
            f32x4 lA = {}, lB = {};
            f32x16 sA = MFMA32(k0, qA0, f32x16{}), sB = MFMA32(k0, qB0, f32x16{});
            sA = MFMA32(k1, qA1, sA); sB = MFMA32(k1, qB1, sB);
            k0 = lds128(p + LDS_X); k1 = lds128(p + LDS_X + 1024);
            bf16x8 v0 = lds128(p + LDS_X + 2048), v1 = lds128(p + LDS_X + 3072);
#pragma unroll
            for (int i = 0; i < 16; ++i) sA[i] = __builtin_amdgcn_exp2f(sA[i]);
            u32x4 pA0 = pack8(sA, 0, 1.f), pA1 = pack8(sA, 1, 1.f);
            asm volatile("s_nop 15\n" ASM_ATTN
                         : [cnt] "=&s"(cnt), "+{v[0:15]}"(oA), "+{v[16:31]}"(oB), "+{v[48:63]}"(sB), "+{v[64:67]}"(pA0), "+{v[68:71]}"(pA1),
                           "+{v[100:103]}"(lA), "+{v[104:107]}"(lB), "+{v[108:111]}"(k0), "+{v[112:115]}"(k1), "+{v[116:119]}"(v0), "+{v[120:123]}"(v1)
                         : "{v[80:83]}"(qA0), "{v[84:87]}"(qA1), "{v[88:91]}"(qB0), "{v[92:95]}"(qB1), "{v[96:99]}"(ones),
                           "{v124}"((unsigned)(uintptr_t)p), "{v125}"((unsigned)(uintptr_t)p + 32768), "{v126}"(pick)
                         : "memory", "scc", CLOB16(32, 33, 34, 35, 36, 37, 38, 39, 40, 41, 42, 43, 44, 45, 46, 47), CLOB8(72, 73, 74, 75, 76, 77, 78, 79));
            const int ln = fresh_lane();
            attn_store(rw, oA, __builtin_amdgcn_rcpf(lA[0] + lA[1]), head, s0 + (ln & 31), ln >> 5);
            attn_store(rw, oB, __builtin_amdgcn_rcpf(lB[0] + lB[1]), head, s0 + 32 + (ln & 31), ln >> 5);
        }
    }
    WAITV_BAR(0);
    {
        const int lane = fresh_lane(), h = lane >> 5, r32 = lane & 31, q4 = (lane & 15) >> 2;
        const int sb = (xcd >> 1) * 8 + (r & 3) * 2 + ((r >> 2) & 1), dt = (xcd & 1) * 4 + (r >> 3);
        const unsigned tb = lds0 + (4 * h + q4) * 256 + ((wid ^ q4) * 64) + ((lane >> 4) & 1) * 32 + (lane & 3) * 8;
        const unsigned ob = lds0 + 16384 + h * 1024 + r32 * 16;
        f32x16 a0 = {}, a1 = {}, y0 = {};
        f32x8 y1 = {};
#define OPROJ(TXT) asm volatile(TXT : [cnt] "=&s"(cnt), "+{v[0:15]}"(a0), "+{v[16:31]}"(a1), "+{v[56:71]}"(y0), "+{v[72:79]}"(y1) : "{v80}"(tb), "{v81}"(ob) \
                                : "memory", "scc", CLOB16(32, 33, 34, 35, 36, 37, 38, 39, 40, 41, 42, 43, 44, 45, 46, 47), CLOB8(48, 49, 50, 51, 52, 53, 54, 55))
        OPROJ("s_nop 1\n" ASM_OPROJ_A);
        BARRIER();
        const int s = sb * 64 + r32;
        u32x4 yp[2][2][2];
        float gate[2], bias[2];
#pragma unroll
        for (int si = 0; si < 2; ++si) {
#pragma unroll
            for (int kh = 0; kh < 2; ++kh)
#pragma unroll
                for (int c = 0; c < 2; ++c)
                    yp[si][kh][c] = __builtin_amdgcn_raw_buffer_load_b128(
                        rw, lane * 16 + c * 1024, WS_Y + ((((sb >> 1) * 8 + dt) * 2 + kh) * 2 + (sb & 1)) * 16384 + (si * 4 + wid) * 2048, SC1);
            gate[si] = __uint_as_float(__builtin_amdgcn_raw_buffer_load_b32(rw, (s + si * 32) * 4, WS_GATE, 0));
            bias[si] = bflo(*(const unsigned short*)(A.b2 + (s + si * 32) * 2));
        }
        OPROJ(ASM_OPROJ_B);
#undef OPROJ
        const rsrc_t ro = __builtin_amdgcn_make_buffer_rsrc(A.out, 0, SEQ * DM * 2, 0x00020000);
#pragma unroll
        for (int si = 0; si < 2; ++si) {
            const f32x16& a = si ? a1 : a0;
            const float g = gate[si], og = 1.f - g;
            unsigned pk[8];
#pragma unroll
            for (int k = 0; k < 8; ++k) {
                const unsigned p0 = yp[si][0][k >> 2][k & 3], p1 = yp[si][1][k >> 2][k & 3];
                pk[k] = cvtpk(g * a[2 * k] + og * (bflo(p0) + bflo(p1) + bias[si]), g * a[2 * k + 1] + og * (bfhi(p0) + bfhi(p1) + bias[si]));
            }
#pragma unroll
            for (int k = 0; k < 2; ++k) {
                const auto x0 = __builtin_amdgcn_permlane32_swap(pk[4 * k], pk[4 * k + 2], false, false);
                const auto x1 = __builtin_amdgcn_permlane32_swap(pk[4 * k + 1], pk[4 * k + 3], false, false);
                *(__attribute__((address_space(3))) u32x4*)(uintptr_t)(lds0 + 24576 + (si * 32 + r32) * 256 + (((wid * 4 + k * 2 + h) ^ (r32 & 15)) * 16)) =
                    (u32x4){x0[0], x1[0], x0[1], x1[1]};
            }
        }
        LGKM_BAR();
#pragma unroll
        for (int i = 0; i < 4; ++i) {
            const int row = wid * 16 + i * 4 + (lane >> 4), c = lane & 15;
            const u32x4 v = *(const __attribute__((address_space(3))) u32x4*)(uintptr_t)(lds0 + 24576 + row * 256 + ((c ^ (row & 15)) * 16));
            __builtin_amdgcn_raw_buffer_store_b128(v, ro, (sb * 64 + row) * 2048 + dt * 256 + c * 16, 0, SC1 | NT);
        }
    }
}

__device__ __forceinline__ void side_b_loader(const Args& A, unsigned lds0, int lane) {
    char* ws = A.ws;
    const int gi = blockIdx.x - 256, xcd = gi & 7, r = gi >> 3;
    const unsigned sw256 = (((((lane & 15) >> 2) ^ (lane >> 4)) << 2) | (lane & 3)) * 16;
    if (r >= 28) {
        const int id = (r - 28) * 8 + xcd;
        const unsigned xe = (lane >> 3) * 2048 + (((lane & 7) ^ (lane >> 4)) * 16), lin = lane * 16;
        const unsigned l4[4] = {lin + 3072, lin + 3072, lin + 3072, lin + 3072};
        unsigned xv[8];
#pragma unroll
        for (int i = 0; i < 8; ++i) xv[i] = off4((xe ^ ((i & 1) * 64)) + i * 8 * 2048, i);
        auto stage = [&](int j) {
            const int st = (j & ~3) | ((j + r) & 3);
            const unsigned dst = lds0 + (j & 3) * 16384;
            dma4(l4, A.wk + st * 4096, dst + 8192);
            dma4(l4, A.wv + st * 4096, dst + 12288);
#pragma unroll
            for (int i = 0; i < 2; ++i) dma4(xv + 4 * i, A.x + (size_t)id * 64 * 2048 + st * 128, dst + i * 4096);
        };
        stage(0); stage(1); stage(2);
        for (int j = 0; j < 13; ++j) { WAITV_BAR(32); stage(j + 3); }
        WAITV_BAR(32); WAITV_BAR(16); WAITV_BAR(0);
        BARRIER();
        const u32x2 kn = *(const __attribute__((address_space(3))) u32x2*)(uintptr_t)(lds0 + 65536);
        if (lane < 8) __hip_atomic_store((gu32*)ws + (CT_KV + lane) * 32 + id, max(kn[0], kn[1]) | 1u, __ATOMIC_RELAXED, __HIP_MEMORY_SCOPE_AGENT);
    } else {
        const int dt = (xcd & 1) * 4 + (r & 3), ft = (xcd >> 1) * 7 + (r >> 2), rot = 2 * (((r & 3) + (r >> 2)) & 3);
        unsigned av[8], bv[8];
#pragma unroll
        for (int i = 0; i < 8; ++i) {
            av[i] = off4((i * 4 + (lane >> 4)) * (DF * 2) + ft * 256 + sw256, i);
            bv[i] = off4((i >> 1) * 16384 + (dt * 128 + (i & 1) * 64 + lane) * 16, i);
        }
        auto sta = [&](int j) {
            const int st = (j & ~7) | ((j + rot) & 7);
#pragma unroll
            for (int i = 0; i < 2; ++i) dma4(av + 4 * i, A.w1 + (size_t)st * 32 * (DF * 2), lds0 + (j & 3) * 16384 + i * 4096);
        };
        auto stb = [&](int j) {
            const int st = (j & ~7) | ((j + rot) & 7);
#pragma unroll
            for (int i = 0; i < 2; ++i) dma4(bv + 4 * i, ws + WS_HT + (size_t)st * 4 * 16384, lds0 + (j & 3) * 16384 + 8192 + i * 4096);
        };
        stb(0); sta(0); stb(1); sta(1); stb(2); sta(2);
        for (int j = 0; j < 61; ++j) { WAITV_BAR(32); stb(j + 3); sta(j + 3); }
        WAITV_BAR(32); WAITV_BAR(16); WAITV_BAR(0);
    }
    {
        const int kh = xcd >> 2, st_ = (xcd & 1) * 8 + (r & 7), dt = ((xcd >> 1) & 1) * 4 + (r >> 3), rot = 2 * (((r & 7) + (r >> 3)) & 3);
        unsigned av[8], bv[8];
#pragma unroll
        for (int i = 0; i < 8; ++i) {
            av[i] = off4((i * 4 + (lane >> 4)) * (SEQ * 2) + st_ * 256 + sw256, i);
            bv[i] = off4((i >> 1) * 16384 + (dt * 128 + (i & 1) * 64 + lane) * 16, i);
        }
        const char* ab = A.w2 + (size_t)kh * 1792 * (SEQ * 2);
        const char* bb = ws + WS_G + (size_t)kh * 224 * 16384;
        auto sta = [&](int j) {
            const int st = (j & ~7) | ((j + rot) & 7);
#pragma unroll
            for (int i = 0; i < 2; ++i) dma4(av + 4 * i, ab + (size_t)st * 32 * (SEQ * 2), lds0 + (j & 3) * 16384 + i * 4096);
        };
        auto stb = [&](int j) {
            const int st = (j & ~7) | ((j + rot) & 7);
#pragma unroll
            for (int i = 0; i < 2; ++i) dma4c(bv + 4 * i, bb + (size_t)st * 4 * 16384, lds0 + (j & 3) * 16384 + 8192 + i * 4096);
        };
        sta(0); sta(1); sta(2);
        if (r < 28) {
            BARRIER();
            if (lane < 4) __hip_atomic_fetch_add((gu32*)ws + (CT_G + (((xcd & 1) * 4 + (r & 3)) * 2 + kh) * 4 + lane) * 32, 1u, __ATOMIC_RELAXED, __HIP_MEMORY_SCOPE_AGENT);
        }
        wait_count(ws, CT_G + (dt * 2 + kh) * 4 + (r & 3), 14);
        stb(0); stb(1); stb(2);
        WAITV_BAR(16); stb(3); sta(3);
        WAITV_BAR(24); stb(4); sta(4);
        for (int j = 2; j < 53; ++j) { WAITV_BAR(32); stb(j + 3); sta(j + 3); }
        WAITV_BAR(32); WAITV_BAR(16); WAITV_BAR(0);
        BARRIER();
        if (lane == 0) __hip_atomic_fetch_add((gu32*)ws + (CT_Y + st_ * 8 + dt) * 32, 1u, __ATOMIC_RELAXED, __HIP_MEMORY_SCOPE_AGENT);
    }
}

__device__ __forceinline__ unsigned gelu2(float a, float b) {
    typedef float f2 __attribute__((ext_vector_type(2)));
    const f2 u = {a, b};
    const f2 z = u * __builtin_elementwise_fma(u * u, (f2)(-0.044715f * 2.302208198f), (f2)(-2.302208198f));
    const f2 d = (f2){__builtin_amdgcn_exp2f(z[0]), __builtin_amdgcn_exp2f(z[1])} + (f2)(1.f);
    const f2 r = u * (f2){__builtin_amdgcn_rcpf(d[0]), __builtin_amdgcn_rcpf(d[1])};
    return cvtpk(r[0], r[1]);
}

__device__ __forceinline__ void side_b(const Args& A, char* lds, int wid) {
    char* ws = A.ws;
    const unsigned lds0 = (unsigned)(uintptr_t)lds;
    const int gi = blockIdx.x - 256, xcd = gi & 7, r = gi >> 3;
    const rsrc_t rw = __builtin_amdgcn_make_buffer_rsrc(ws, 0, WS_END, 0x00020000);
    unsigned cnt;
#define MIX(TXT) asm volatile("s_nop 1\n" TXT \
        : [cnt] "=&s"(cnt), "+{v[0:15]}"(acc[0][0]), "+{v[16:31]}"(acc[0][1]), "+{v[32:47]}"(acc[1][0]), "+{v[48:63]}"(acc[1][1]), "+{v[80:95]}"(fy) \
        : "{v96}"(tb), "{v97}"(tb ^ 64), "{v98}"(rb) : "memory", "scc", CLOB16(64, 65, 66, 67, 68, 69, 70, 71, 72, 73, 74, 75, 76, 77, 78, 79))
    if (r < 28) {
        const int lane = fresh_lane(), h = lane >> 5, q4 = (lane & 15) >> 2, wf = wid >> 1, wd = wid & 1;
        const int dt = (xcd & 1) * 4 + (r & 3), ft = (xcd >> 1) * 7 + (r >> 2);
        const unsigned tb = lds0 + (8 * h + q4) * 256 + (((2 * wf) ^ q4) * 64) + ((lane >> 4) & 1) * 32 + (lane & 3) * 8;
        const unsigned rb = lds0 + 8192 + h * 2048 + (wd * 64 + (lane & 31)) * 16;
        f32x16 acc[2][2], fy = {};
#pragma unroll
        for (int fi = 0; fi < 2; ++fi)
#pragma unroll
            for (int k = 0; k < 4; ++k) {
                const u32x2 bv = *(const u32x2*)(A.b1 + ((ft * 4 + wf * 2 + fi) * 32 + 8 * k + 4 * h) * 2);
#pragma unroll
                for (int di = 0; di < 2; ++di) {
                    acc[fi][di][4 * k] = bflo(bv[0]); acc[fi][di][4 * k + 1] = bfhi(bv[0]);
                    acc[fi][di][4 * k + 2] = bflo(bv[1]); acc[fi][di][4 * k + 3] = bfhi(bv[1]);
                }
            }
        MIX(ASM_MIX1);
        __builtin_amdgcn_s_setprio(PRIO_EPI);
#pragma unroll
        for (int fi = 0; fi < 2; ++fi)
#pragma unroll
            for (int di = 0; di < 2; ++di)
#pragma unroll
                for (int c = 0; c < 2; ++c) {
                    const f32x16& u = acc[fi][di];
                    st16(rw, WS_G + (((ft * 4 + wf * 2 + fi) * 4 + 2 * c + h) * DM + dt * 128 + wd * 64 + di * 32 + (lane & 31)) * 16,
                         (u32x4){gelu2(u[8 * c], u[8 * c + 1]), gelu2(u[8 * c + 2], u[8 * c + 3]), gelu2(u[8 * c + 4], u[8 * c + 5]), gelu2(u[8 * c + 6], u[8 * c + 7])});
                }
        WAITV_BAR(0);
    } else {
        const int lane = fresh_lane(), h = lane >> 5, r32 = lane & 31, T = ((r - 28) * 8 + xcd) * 2 + (wid >> 1);
        const bool isv = wid & 1;
        lds_cptr wp = (lds_cptr)lds + 8192 + (wid & 1) * 4096 + (8 * h + ((lane & 15) >> 2)) * 64 + ((lane >> 4) & 1) * 32 + (lane & 3) * 8;
        const unsigned xa = ((wid >> 1) * 32 + r32) * 128 + ((h ^ ((r32 >> 1) & 7)) * 16);
        f32x16 a = {};
#pragma unroll 4
        for (int j = 0; j < 16; ++j) {
            LGKM_BAR();
#pragma unroll
            for (int ks = 0; ks < 4; ++ks) {
                lds_cptr w = wp + (j & 3) * 16384 + ks * 1024;
                const bf16x8 wf = __builtin_shufflevector(ldstr(w), ldstr(w + 256), 0, 1, 2, 3, 4, 5, 6, 7);
                const bf16x8 xf = lds128((lds_cptr)lds + (j & 3) * 16384 + (xa ^ (ks * 32)));
                a = MFMA32(isv ? xf : wf, isv ? wf : xf, a);
            }
        }
        const u32x4 u0 = pack8(a, 0, 1.f), u1 = pack8(a, 1, 1.f);
        const unsigned dst = WS_KV + (isv ? T * 4096 + 2048 : ((T + 63) & 63) * 4096) + lane * 16;
        st16(rw, dst, u0);
        st16(rw, dst + 1024, u1);
        if (!isv) {
            const unsigned n = __float_as_uint(sumsq8(u0) + sumsq8(u1));
            const auto sw = __builtin_amdgcn_permlane32_swap(n, n, false, false);
            const unsigned kn = wave_umax(__float_as_uint(__uint_as_float(sw[0]) + __uint_as_float(sw[1])));
            *(__attribute__((address_space(3))) unsigned*)(uintptr_t)(lds0 + 65536 + (wid >> 1) * 4) = kn;
        }
        asm volatile("s_waitcnt vmcnt(0) lgkmcnt(0)\n\ts_barrier" ::: "memory");
    }
    __builtin_amdgcn_s_setprio(0);
    {
        const int lane = fresh_lane(), h = lane >> 5, q4 = (lane & 15) >> 2, wsq = wid >> 1, wd = wid & 1;
        const int kh = xcd >> 2, st_ = (xcd & 1) * 8 + (r & 7), dt = ((xcd >> 1) & 1) * 4 + (r >> 3);
        const unsigned tb = lds0 + (4 * h + q4) * 256 + (((2 * wsq) ^ q4) * 64) + ((lane >> 4) & 1) * 32 + (lane & 3) * 8;
        const unsigned rb = lds0 + 8192 + h * 2048 + (wd * 64 + (lane & 31)) * 16;
        f32x16 acc[2][2] = {}, fy = {};
        MIX(ASM_MIX2);
        __builtin_amdgcn_s_setprio(PRIO_EPI);
        const unsigned dst = WS_Y + (((st_ * 8 + dt) * 2 + kh) * 2 + wsq) * 16384;
#pragma unroll
        for (int di = 0; di < 2; ++di)
#pragma unroll
            for (int si = 0; si < 2; ++si)
#pragma unroll
                for (int c = 0; c < 2; ++c)
                    st16(rw, dst + (si * 4 + wd * 2 + di) * 2048 + c * 1024 + lane * 16, pack8(acc[di][si], c, 1.f));
        WAITV_BAR(0);
    }
#undef MIX
}

__global__ __launch_bounds__(512, 4) void k_main(const char* x, const char* wq, const char* wk, const char* wv, const char* wo, const char* w1,
                                                  const char* b1, const char* w2, const char* b2, char* ws, char* out) {
    __shared__ __attribute__((aligned(1024))) char lds[LDS_BYTES];
    const int wid = __builtin_amdgcn_readfirstlane(threadIdx.x >> 6);
    if (wid > 4) return;
    const Args A = {x, wq, wk, wv, wo, w1, b1, w2, b2, ws, out};
    if (wid == 4) {
        __builtin_amdgcn_s_setprio(3);
        if (blockIdx.x < 256) side_a_loader(A, (unsigned)(uintptr_t)lds, threadIdx.x & 63);
        else side_b_loader(A, (unsigned)(uintptr_t)lds, threadIdx.x & 63);
    } else {
        if (blockIdx.x < 256) side_a(A, lds, wid);
        else side_b(A, lds, wid);
    }
}

extern "C" void kernel_launch(void* const* d_in, const int* in_sizes, int n_in, void* d_out, int out_size, void* d_ws, size_t ws_size,
                              hipStream_t stream) {
    k_pre<<<256, 256, 0, stream>>>((const char*)d_in[0], (const char*)d_in[10], (char*)d_ws);
    k_main<<<512, 512, 0, stream>>>((const char*)d_in[0], (const char*)d_in[1], (const char*)d_in[2], (const char*)d_in[3], (const char*)d_in[4], (const char*)d_in[6], (const char*)d_in[7],
                                    (const char*)d_in[8], (const char*)d_in[9], (char*)d_ws, (char*)d_out);
}
```
